```python
import torch

def _softmax(x, axis=-1):
    return torch.softmax(x, dim=axis)

def _gelu(x):
    return torch.nn.functional.gelu(x, approximate='tanh')

def _silu(x):
    return torch.nn.functional.silu(x)

def _sigmoid(x):
    return torch.sigmoid(x)

def _layernorm(x, axis=-1, eps=1e-05):
    m = x.mean(dim=axis, keepdim=True)
    v = x.var(dim=axis, keepdim=True, unbiased=False)
    return (x - m) / torch.sqrt(v + eps)

def _scatter_add(tgt, idx, src, axis=0):
    return tgt.clone().index_add_(axis, idx, src)

def _segment_sum(src, seg, n):
    out = torch.zeros((n,) + src.shape[1:], dtype=src.dtype)
    return out.index_add_(0, seg, src)

def _pad(x, pw):
    flat = tuple((v for pair in reversed(pw) for v in pair))
    return torch.nn.functional.pad(x, flat)
kernel_name = 'standalone__tree_speculative_verify_tree_attention_over_candidate_token_tree__sampling'

def setup_inputs():

    _w = lambda a: (lambda t: t.to(torch.bfloat16) if t.is_floating_point() else t)(torch.as_tensor(a))
    B, Hq, Hkv, T, C, D = (8, 16, 8, 16, 8192, 256)
    tree_mask = torch.zeros((B, T, T), dtype=float)
    for b in range(B):
        parent = torch.zeros(T, dtype=torch.int64)
        for i in range(1, T):
            parent[i] = int(torch.randint(0, i, (1,)).item())
        for i in range(T):
            j = i
            tree_mask[b, i, j] = 1.0
            while j > 0:
                j = parent[j]
                tree_mask[b, i, j] = 1.0
    return {'q': _w((lambda *s: torch.randn(*s, dtype=torch.bfloat16) * 0.8)(B, Hq, T, D)), 'k_cache': _w((lambda *s: torch.randn(*s, dtype=torch.bfloat16) * 0.8)(B, Hkv, C, D)), 'v_cache': _w((lambda *s: torch.randn(*s, dtype=torch.bfloat16) * 0.05)(B, Hkv, C, D)), 'k_tree': _w((lambda *s: torch.randn(*s, dtype=torch.bfloat16) * 0.8)(B, Hkv, T, D)), 'v_tree': _w((lambda *s: torch.randn(*s, dtype=torch.bfloat16) * 0.05)(B, Hkv, T, D)), 'tree_mask': _w(tree_mask)}

def reference(q, k_cache, v_cache, k_tree, v_tree, tree_mask):
    B, Hq, T, D = q.shape
    Hkv, C = (k_cache.shape[1], k_cache.shape[2])
    rep = Hq // Hkv
    kc = (lambda x, n, axis: torch.repeat_interleave(x, n, dim=axis))(k_cache, rep, axis=1)
    vc = (lambda x, n, axis: torch.repeat_interleave(x, n, dim=axis))(v_cache, rep, axis=1)
    kt = (lambda x, n, axis: torch.repeat_interleave(x, n, dim=axis))(k_tree, rep, axis=1)
    vt = (lambda x, n, axis: torch.repeat_interleave(x, n, dim=axis))(v_tree, rep, axis=1)
    k = torch.concatenate([kc, kt], axis=2)
    v = torch.concatenate([vc, vt], axis=2)
    scale = D ** (-0.5)
    scores = torch.einsum('bhqd,bhkd->bhqk', q, k) * scale
    ctx_mask = torch.ones((B, 1, T, C), dtype=scores.dtype)
    tm = tree_mask[:, None, :, :].clone()
    full_mask = torch.concatenate([ctx_mask, tm], axis=-1)
    scores = scores + (1.0 - full_mask) * -1000000000.0
    p = _softmax(scores, axis=-1)
    return torch.einsum('bhqk,bhkd->bhqd', p, v)
```

```cpp
#include <hip/hip_runtime.h>
#include <hip/hip_bf16.h>

#define LAS __attribute__((address_space(3)))
typedef __bf16 bf16x8 __attribute__((ext_vector_type(8)));
typedef __bf16 bf16x4 __attribute__((ext_vector_type(4)));
typedef __bf16 bf16x2 __attribute__((ext_vector_type(2)));
typedef float f32x16 __attribute__((ext_vector_type(16)));
typedef float f32x4 __attribute__((ext_vector_type(4)));
typedef float f32x2 __attribute__((ext_vector_type(2)));
typedef unsigned u32x4 __attribute__((ext_vector_type(4)));
typedef unsigned u32x2 __attribute__((ext_vector_type(2)));

constexpr int NB = 8, HQ = 16, HKV = 8, NT = 16, CTX = 8192, HD = 256;
constexpr int NGRP = NB * HKV;
constexpr int NSPL = 4;
constexpr int ROWB = HD * 2;
constexpr int BLKB = 16 * ROWB;
constexpr int TILEB = 2 * BLKB;
constexpr int RINGB = 2 * TILEB;
constexpr int TREE_OFF = 4 * RINGB;
constexpr int ORS = HD * 4 + 16;
constexpr int OIMG = 32 * ORS;
constexpr int ML_OFF = 4 * OIMG;
constexpr int LDS_BYTES = TREE_OFF + 2 * BLKB;
static_assert(ML_OFF + 4 * 32 * 8 <= LDS_BYTES, "epilogue image must fit");
constexpr float SCL2 = 0.0625f * 1.4426950408889634f;
constexpr float MASKRAW = -1.0e9f * 16.0f;

constexpr size_t WS_PO = 0, WS_PML = (size_t)NGRP * NSPL * 32 * HD * 4;

__device__ __forceinline__ void dma4(const char* src, unsigned lds, unsigned v0, unsigned v1, unsigned v2, unsigned v3) {
  unsigned keep;
  asm volatile("s_mov_b32 %0, m0\n\ts_mov_b32 m0, %2\n\ts_nop 2\n\t"
               "global_load_lds_dwordx4 %3, %1 nt\n\t"
               "global_load_lds_dwordx4 %4, %1 offset:1024 nt\n\t"
               "global_load_lds_dwordx4 %5, %1 offset:2048 nt\n\t"
               "global_load_lds_dwordx4 %6, %1 offset:3072 nt\n\t"
               "s_mov_b32 m0, %0"
               : "=&s"(keep) : "s"(src), "s"(lds), "v"(v0), "v"(v1), "v"(v2), "v"(v3) : "memory");
}
#define WAIT_VM(n) asm volatile("s_waitcnt vmcnt(" #n ")" ::: "memory")
#define WAIT_LGKM0() asm volatile("s_waitcnt lgkmcnt(0)" ::: "memory")

__device__ __forceinline__ float xhalf_max(float x) {
  auto r = __builtin_amdgcn_permlane32_swap(__float_as_uint(x), __float_as_uint(x), false, false);
  return fmaxf(__uint_as_float(r[0]), __uint_as_float(r[1]));
}
__device__ __forceinline__ float xhalf_sum(float x) {
  auto r = __builtin_amdgcn_permlane32_swap(__float_as_uint(x), __float_as_uint(x), false, false);
  return __uint_as_float(r[0]) + __uint_as_float(r[1]);
}
__device__ __forceinline__ bf16x8 pack8(const f32x16& p, int b) {
  const bf16x2 a0 = __builtin_convertvector((f32x2){p[b + 0], p[b + 1]}, bf16x2), a1 = __builtin_convertvector((f32x2){p[b + 2], p[b + 3]}, bf16x2);
  const bf16x2 a2 = __builtin_convertvector((f32x2){p[b + 4], p[b + 5]}, bf16x2), a3 = __builtin_convertvector((f32x2){p[b + 6], p[b + 7]}, bf16x2);
  const u32x4 w = {__builtin_bit_cast(unsigned, a0), __builtin_bit_cast(unsigned, a1), __builtin_bit_cast(unsigned, a2), __builtin_bit_cast(unsigned, a3)};
  return __builtin_bit_cast(bf16x8, w);
}

struct State { f32x16 o[8]; float m, l; };

__device__ __forceinline__ f32x16 qk_tile(LAS char* lds, unsigned krd, const bf16x8 (&q)[16]) {
  f32x16 s = {};
#pragma unroll
  for (int st = 0; st < 16; ++st) {
    const bf16x8 a = *(const LAS bf16x8*)(lds + ((krd ^ ((st & 7) << 5)) + (st >> 3) * 256));
    s = __builtin_amdgcn_mfma_f32_32x32x16_bf16(a, q[st], s, 0, 0, 0);
  }
  return s;
}
__device__ __forceinline__ void softmax_tile(f32x16& s, State& z, bf16x8& p0, bf16x8& p1) {
  float mx = s[0];
#pragma unroll
  for (int r = 1; r < 16; ++r) mx = fmaxf(mx, s[r]);
  mx = xhalf_max(mx);
  const float mn = fmaxf(z.m, mx);
  const float alpha = __builtin_amdgcn_exp2f((z.m - mn) * SCL2);
  const float nb = -mn * SCL2;
  float ps = 0.f;
#pragma unroll
  for (int r = 0; r < 16; ++r) { s[r] = __builtin_amdgcn_exp2f(fmaf(s[r], SCL2, nb)); ps += s[r]; }
  z.l = fmaf(z.l, alpha, ps);
  z.m = mn;
  if (!__all(alpha == 1.0f)) {
#pragma unroll
    for (int c = 0; c < 8; ++c) z.o[c] *= alpha;
  }
  p0 = pack8(s, 0); p1 = pack8(s, 8);
}
__device__ __forceinline__ void pv_step(LAS char* lds, unsigned vrd, int koff, const bf16x8& p, State& z) {
#pragma unroll
  for (int c = 0; c < 8; ++c) {
    const unsigned a = (vrd ^ ((c & 3) << 6)) + (c >> 2) * 256 + koff;
    const bf16x4 lo = __builtin_amdgcn_ds_read_tr16_b64_v4bf16((LAS bf16x4*)(lds + a));
    const bf16x4 hi = __builtin_amdgcn_ds_read_tr16_b64_v4bf16((LAS bf16x4*)(lds + a + 8 * ROWB));
    const bf16x8 v = __builtin_shufflevector(lo, hi, 0, 1, 2, 3, 4, 5, 6, 7);
    z.o[c] = __builtin_amdgcn_mfma_f32_32x32x16_bf16(v, p, z.o[c], 0, 0, 0);
  }
}

__global__ void __launch_bounds__(256, 1)
tree_attn_split(const char* __restrict__ Q, const char* __restrict__ K, const char* __restrict__ V, const char* __restrict__ KT,
                const char* __restrict__ VT, const unsigned short* __restrict__ MK, float* __restrict__ po, float* __restrict__ pml) {
  __shared__ __attribute__((aligned(1024))) char smem[LDS_BYTES];
  LAS char* lds = (LAS char*)smem;
  const unsigned lbase = (unsigned)(size_t)lds;
  const int tid = threadIdx.x, lane = tid & 63, wave = __builtin_amdgcn_readfirstlane(tid >> 6);
  const int wg = blockIdx.x, x8 = wg & 7;
  const int grp = (wg >> 3) * 2 + ((x8 >> 1) & 1), mem = (x8 >> 2) * 2 + (x8 & 1);
  const int r32 = lane & 31, hh = lane >> 5;
  const bool tree = (mem == 0) && (wave == 0);

  unsigned ko[8], vo[2];
#pragma unroll
  for (int i = 0; i < 8; ++i) ko[i] = hh * ROWB + ((r32 ^ ((2 * i + hh) & 15)) << 4);
#pragma unroll
  for (int i = 0; i < 2; ++i) vo[i] = hh * ROWB + ((r32 ^ (((2 * i + hh) & 3) << 2)) << 4);
  const unsigned ring = wave * RINGB;
  auto issue_k = [&](const char* src, unsigned dst) {
    dma4(src, lbase + dst, ko[0], ko[1], ko[2], ko[3]);
    dma4(src + 4096, lbase + dst + 4096, ko[4], ko[5], ko[6], ko[7]);
  };
  auto issue_v = [&](const char* src, unsigned dst) {
    dma4(src, lbase + dst, vo[0], vo[1], vo[0], vo[1]);
    dma4(src + 4096, lbase + dst + 4096, vo[0], vo[1], vo[0], vo[1]);
  };
  const char* Kg = K + (size_t)grp * CTX * ROWB;
  const char* Vg = V + (size_t)grp * CTX * ROWB;
  const int cls = wave & 1, i8 = mem * 2 + (wave >> 1);
  const size_t woff = (size_t)(4 * i8 + cls) * BLKB;
  constexpr size_t TSTEP = (size_t)32 * BLKB;
  constexpr int NTILE = CTX / 32 / 16;

  if (tree) { issue_k(KT + (size_t)grp * BLKB, TREE_OFF); issue_v(VT + (size_t)grp * BLKB, TREE_OFF + BLKB); }
  bf16x8 q[16];
  u32x2 mk0 = {0u, 0u}, mk1 = {0u, 0u};
  {
    const char* qp = Q + ((size_t)grp * 32 + r32) * ROWB + hh * 16;
#pragma unroll
    for (int st = 0; st < 16; ++st) asm volatile("global_load_dwordx4 %0, %1, off offset:%2" : "=v"(q[st]) : "v"(qp), "i"(st * 32) : "memory");
    const unsigned short* mp = MK + ((grp >> 3) * NT + (r32 & 15)) * NT + 4 * hh;
    asm volatile("global_load_dwordx2 %0, %1, off" : "=v"(mk0) : "v"(mp) : "memory");
    asm volatile("global_load_dwordx2 %0, %1, off offset:16" : "=v"(mk1) : "v"(mp) : "memory");
  }
  issue_k(Kg + woff, ring); issue_k(Kg + woff + 2 * BLKB, ring + BLKB);
  issue_v(Vg + woff, ring + TILEB); issue_v(Vg + woff + 2 * BLKB, ring + TILEB + BLKB);

  State z;
#pragma unroll
  for (int c = 0; c < 8; ++c) z.o[c] = f32x16{};
  z.m = -__builtin_inff(); z.l = 0.f;
  const unsigned krd = r32 * ROWB + ((hh ^ (r32 & 15)) << 4);
  const int q4 = (lane & 15) >> 2;
  const unsigned vrd = (4 * hh + q4) * ROWB + (q4 << 6) + ((lane >> 4) & 1) * 32 + (lane & 3) * 8;

  asm volatile("s_waitcnt vmcnt(32)"
               : "+v"(q[0]), "+v"(q[1]), "+v"(q[2]), "+v"(q[3]), "+v"(q[4]), "+v"(q[5]), "+v"(q[6]), "+v"(q[7]), "+v"(q[8]), "+v"(q[9]),
                 "+v"(q[10]), "+v"(q[11]), "+v"(q[12]), "+v"(q[13]), "+v"(q[14]), "+v"(q[15]), "+v"(mk0), "+v"(mk1) :: "memory");
  if (tree) {
    f32x16 s = qk_tile(lds, TREE_OFF + (r32 & 15) * ROWB + ((hh ^ (r32 & 15)) << 4), q);
#pragma unroll
    for (int r = 0; r < 8; ++r) {
      const unsigned w = (r < 4 ? mk0 : mk1)[(r >> 1) & 1];
      const float mv = __uint_as_float((r & 1) ? (w & 0xffff0000u) : (w << 16));
      s[r] = fmaf(1.0f - mv, MASKRAW, s[r]);
      s[r + 8] = -__builtin_inff();
    }
    bf16x8 p0, p1;
    softmax_tile(s, z, p0, p1);
    pv_step(lds, TREE_OFF + BLKB + vrd, 0, p0, z);
  }
  for (int n = 0; n < NTILE; ++n) {
    const bool more = n + 1 < NTILE;
    const size_t nx = woff + (size_t)(n + 1) * TSTEP;
    WAIT_VM(16);
    f32x16 s = qk_tile(lds, ring + krd, q);
    WAIT_LGKM0();
    if (more) { issue_k(Kg + nx, ring); issue_k(Kg + nx + 2 * BLKB, ring + BLKB); }
    bf16x8 p0, p1;
    softmax_tile(s, z, p0, p1);
    if (more) WAIT_VM(16); else WAIT_VM(0);
    pv_step(lds, ring + TILEB + vrd, 0, p0, z);
    pv_step(lds, ring + TILEB + vrd, BLKB, p1, z);
    WAIT_LGKM0();
    if (more) { issue_v(Vg + nx, ring + TILEB); issue_v(Vg + nx + 2 * BLKB, ring + TILEB + BLKB); }
  }

  z.l = xhalf_sum(z.l);
  __syncthreads();
  {
    LAS char* img = lds + wave * OIMG + r32 * ORS + hh * 16;
#pragma unroll
    for (int c = 0; c < 8; ++c)
#pragma unroll
      for (int g = 0; g < 4; ++g)
        *(LAS f32x4*)(img + c * 128 + g * 32) = f32x4{z.o[c][4 * g], z.o[c][4 * g + 1], z.o[c][4 * g + 2], z.o[c][4 * g + 3]};
    if (hh == 0) *(LAS f32x2*)(lds + ML_OFF + (wave * 32 + r32) * 8) = f32x2{z.m, z.l};
  }
  __syncthreads();
  const size_t slot = (size_t)grp * NSPL + mem;
#pragma unroll
  for (int k = 0; k < 8; ++k) {
    const int row = wave + 4 * k;
    f32x2 ml[4];
#pragma unroll
    for (int w = 0; w < 4; ++w) ml[w] = *(const LAS f32x2*)(lds + ML_OFF + (w * 32 + row) * 8);
    const float M = fmaxf(fmaxf(ml[0][0], ml[1][0]), fmaxf(ml[2][0], ml[3][0]));
    f32x4 acc = {0.f, 0.f, 0.f, 0.f}; float L = 0.f;
#pragma unroll
    for (int w = 0; w < 4; ++w) {
      const float f = __builtin_amdgcn_exp2f((ml[w][0] - M) * SCL2);
      acc += f * *(const LAS f32x4*)(lds + w * OIMG + row * ORS + lane * 16);
      L = fmaf(f, ml[w][1], L);
    }
    *(f32x4*)(po + (slot * 32 + row) * HD + lane * 4) = acc;
    if (lane == 0) *(f32x2*)(pml + (slot * 32 + row) * 2) = f32x2{M, L};
  }
}

__global__ void __launch_bounds__(256) tree_attn_combine(const float* __restrict__ po, const float* __restrict__ pml, unsigned short* __restrict__ out) {
  const int lane = threadIdx.x & 63, row = blockIdx.x * 4 + (threadIdx.x >> 6);
  const int grp = row >> 5, qr = row & 31;
  f32x2 ml[NSPL]; f32x4 a[NSPL];
#pragma unroll
  for (int s = 0; s < NSPL; ++s) {
    const size_t i = ((size_t)grp * NSPL + s) * 32 + qr;
    ml[s] = *(const f32x2*)(pml + i * 2);
    a[s] = *(const f32x4*)(po + i * HD + lane * 4);
  }
  float M = ml[0][0];
#pragma unroll
  for (int s = 1; s < NSPL; ++s) M = fmaxf(M, ml[s][0]);
  f32x4 acc = {0.f, 0.f, 0.f, 0.f}; float L = 0.f;
#pragma unroll
  for (int s = 0; s < NSPL; ++s) { const float f = __builtin_amdgcn_exp2f((ml[s][0] - M) * SCL2); acc += f * a[s]; L = fmaf(f, ml[s][1], L); }
  const float inv = 1.0f / L;
  acc *= inv;
  const bf16x2 o0 = __builtin_convertvector((f32x2){acc[0], acc[1]}, bf16x2), o1 = __builtin_convertvector((f32x2){acc[2], acc[3]}, bf16x2);
  *(u32x2*)(out + (size_t)row * HD + lane * 4) = u32x2{__builtin_bit_cast(unsigned, o0), __builtin_bit_cast(unsigned, o1)};
}

extern "C" void kernel_launch(void* const* d_in, const int* in_sizes, int n_in, void* d_out, int out_size, void* d_ws, size_t ws_size,
                              hipStream_t stream) {
  float* po = (float*)((char*)d_ws + WS_PO);
  float* pml = (float*)((char*)d_ws + WS_PML);
  tree_attn_split<<<NGRP * NSPL, 256, 0, stream>>>((const char*)d_in[0], (const char*)d_in[1], (const char*)d_in[2], (const char*)d_in[3],
                                                   (const char*)d_in[4], (const unsigned short*)d_in[5], po, pml);
  tree_attn_combine<<<NGRP * 32 / 4, 256, 0, stream>>>(po, pml, (unsigned short*)d_out);
}
```

```cpp
#include <hip/hip_runtime.h>
#include <hip/hip_bf16.h>

#define LAS __attribute__((address_space(3)))
typedef __bf16 bf16x8 __attribute__((ext_vector_type(8)));
typedef __bf16 bf16x4 __attribute__((ext_vector_type(4)));
typedef __bf16 bf16x2 __attribute__((ext_vector_type(2)));
typedef float f32x16 __attribute__((ext_vector_type(16)));
typedef float f32x4 __attribute__((ext_vector_type(4)));
typedef float f32x2 __attribute__((ext_vector_type(2)));
typedef unsigned u32x4 __attribute__((ext_vector_type(4)));
typedef unsigned u32x2 __attribute__((ext_vector_type(2)));

constexpr int NB = 8, HQ = 16, HKV = 8, NT = 16, CTX = 8192, HD = 256;
constexpr int NGRP = NB * HKV;
constexpr int NSPL = 4;
constexpr int ROWB = HD * 2;
constexpr int BLKB = 16 * ROWB;
constexpr int TILEB = 2 * BLKB;
constexpr int RINGB = 2 * TILEB;
constexpr int TREE_OFF = 4 * RINGB;
constexpr int ORS = HD * 4 + 16;
constexpr int OIMG = 32 * ORS;
constexpr int ML_OFF = 4 * OIMG;
constexpr int LDS_BYTES = TREE_OFF + 2 * BLKB;
static_assert(ML_OFF + 4 * 32 * 8 <= LDS_BYTES, "epilogue image must fit");
constexpr float SCL2 = 0.0625f * 1.4426950408889634f;
constexpr float MASKRAW = -1.0e9f * 16.0f;

constexpr size_t WS_PO = 0, WS_PML = (size_t)NGRP * NSPL * 32 * HD * 4;

__device__ __forceinline__ void dma4(const char* src, unsigned lds, unsigned v0, unsigned v1, unsigned v2, unsigned v3) {
  unsigned keep;
  asm volatile("s_mov_b32 %0, m0\n\ts_mov_b32 m0, %2\n\ts_nop 2\n\t"
               "global_load_lds_dwordx4 %3, %1 nt\n\t"
               "global_load_lds_dwordx4 %4, %1 offset:1024 nt\n\t"
               "global_load_lds_dwordx4 %5, %1 offset:2048 nt\n\t"
               "global_load_lds_dwordx4 %6, %1 offset:3072 nt\n\t"
               "s_mov_b32 m0, %0"
               : "=&s"(keep) : "s"(src), "s"(lds), "v"(v0), "v"(v1), "v"(v2), "v"(v3) : "memory");
}
#define WAIT_VM(n) asm volatile("s_waitcnt vmcnt(" #n ")" ::: "memory")
#define WAIT_LGKM0() asm volatile("s_waitcnt lgkmcnt(0)" ::: "memory")

__device__ __forceinline__ float xhalf_max(float x) {
  auto r = __builtin_amdgcn_permlane32_swap(__float_as_uint(x), __float_as_uint(x), false, false);
  return fmaxf(__uint_as_float(r[0]), __uint_as_float(r[1]));
}
__device__ __forceinline__ float xhalf_sum(float x) {
  auto r = __builtin_amdgcn_permlane32_swap(__float_as_uint(x), __float_as_uint(x), false, false);
  return __uint_as_float(r[0]) + __uint_as_float(r[1]);
}
__device__ __forceinline__ bf16x8 pack8(const f32x16& p, int b) {
  const bf16x2 a0 = __builtin_convertvector((f32x2){p[b + 0], p[b + 1]}, bf16x2), a1 = __builtin_convertvector((f32x2){p[b + 2], p[b + 3]}, bf16x2);
  const bf16x2 a2 = __builtin_convertvector((f32x2){p[b + 4], p[b + 5]}, bf16x2), a3 = __builtin_convertvector((f32x2){p[b + 6], p[b + 7]}, bf16x2);
  const u32x4 w = {__builtin_bit_cast(unsigned, a0), __builtin_bit_cast(unsigned, a1), __builtin_bit_cast(unsigned, a2), __builtin_bit_cast(unsigned, a3)};
  return __builtin_bit_cast(bf16x8, w);
}

struct State { f32x16 o[8]; float m, l; };

__device__ __forceinline__ f32x16 qk_tile(LAS char* lds, unsigned krd, const bf16x8 (&q)[16]) {
  f32x16 s = {};
#pragma unroll
  for (int st = 0; st < 16; ++st) {
    const bf16x8 a = *(const LAS bf16x8*)(lds + ((krd ^ ((st & 7) << 5)) + (st >> 3) * 256));
    s = __builtin_amdgcn_mfma_f32_32x32x16_bf16(a, q[st], s, 0, 0, 0);
  }
  return s;
}
__device__ __forceinline__ void softmax_tile(f32x16& s, State& z, bf16x8& p0, bf16x8& p1) {
  float mx = s[0];
#pragma unroll
  for (int r = 1; r < 16; ++r) mx = fmaxf(mx, s[r]);
  mx = xhalf_max(mx);
  const float mn = fmaxf(z.m, mx);
  const float alpha = __builtin_amdgcn_exp2f((z.m - mn) * SCL2);
  const float nb = -mn * SCL2;
  float ps = 0.f;
#pragma unroll
  for (int r = 0; r < 16; ++r) { s[r] = __builtin_amdgcn_exp2f(fmaf(s[r], SCL2, nb)); ps += s[r]; }
  z.l = fmaf(z.l, alpha, ps);
  z.m = mn;
  if (!__all(alpha == 1.0f)) {
#pragma unroll
    for (int c = 0; c < 8; ++c) z.o[c] *= alpha;
  }
  p0 = pack8(s, 0); p1 = pack8(s, 8);
}
__device__ __forceinline__ void pv_step(LAS char* lds, unsigned vrd, int koff, const bf16x8& p, State& z) {
#pragma unroll
  for (int c = 0; c < 8; ++c) {
    const unsigned a = (vrd ^ ((c & 3) << 6)) + (c >> 2) * 256 + koff;
    const bf16x4 lo = __builtin_amdgcn_ds_read_tr16_b64_v4bf16((LAS bf16x4*)(lds + a));
    const bf16x4 hi = __builtin_amdgcn_ds_read_tr16_b64_v4bf16((LAS bf16x4*)(lds + a + 8 * ROWB));
    const bf16x8 v = __builtin_shufflevector(lo, hi, 0, 1, 2, 3, 4, 5, 6, 7);
    z.o[c] = __builtin_amdgcn_mfma_f32_32x32x16_bf16(v, p, z.o[c], 0, 0, 0);
  }
}

__global__ void __launch_bounds__(256, 1)
tree_attn_split(const char* __restrict__ Q, const char* __restrict__ K, const char* __restrict__ V, const char* __restrict__ KT,
                const char* __restrict__ VT, const unsigned short* __restrict__ MK, float* __restrict__ po, float* __restrict__ pml) {
  __shared__ __attribute__((aligned(1024))) char smem[LDS_BYTES];
  LAS char* lds = (LAS char*)smem;
  const unsigned lbase = (unsigned)(size_t)lds;
  const int tid = threadIdx.x, lane = tid & 63, wave = __builtin_amdgcn_readfirstlane(tid >> 6);
  const int wg = blockIdx.x, x8 = wg & 7;
  const int grp = (wg >> 3) * 2 + ((x8 >> 1) & 1), mem = (x8 >> 2) * 2 + (x8 & 1);
  const int r32 = lane & 31, hh = lane >> 5;
  const bool tree = (mem == 0) && (wave == 0);

  unsigned ko[8], vo[2];
#pragma unroll
  for (int i = 0; i < 8; ++i) ko[i] = hh * ROWB + ((r32 ^ ((2 * i + hh) & 15)) << 4);
#pragma unroll
  for (int i = 0; i < 2; ++i) vo[i] = hh * ROWB + ((r32 ^ (((2 * i + hh) & 3) << 2)) << 4);
  const unsigned ring = wave * RINGB;
  auto issue_k = [&](const char* src, unsigned dst) {
    dma4(src, lbase + dst, ko[0], ko[1], ko[2], ko[3]);
    dma4(src + 4096, lbase + dst + 4096, ko[4], ko[5], ko[6], ko[7]);
  };
  auto issue_v = [&](const char* src, unsigned dst) {
    dma4(src, lbase + dst, vo[0], vo[1], vo[0], vo[1]);
    dma4(src + 4096, lbase + dst + 4096, vo[0], vo[1], vo[0], vo[1]);
  };
  const char* Kg = K + (size_t)grp * CTX * ROWB;
  const char* Vg = V + (size_t)grp * CTX * ROWB;
  const int cls = wave & 1, i8 = mem * 2 + (wave >> 1);
  const size_t woff = (size_t)(4 * i8 + cls) * BLKB;
  constexpr size_t TSTEP = (size_t)32 * BLKB;
  constexpr int NTILE = CTX / 32 / 16;

  if (tree) { issue_k(KT + (size_t)grp * BLKB, TREE_OFF); issue_v(VT + (size_t)grp * BLKB, TREE_OFF + BLKB); }
  bf16x8 q[16];
  u32x2 mk0 = {0u, 0u}, mk1 = {0u, 0u};
  {
    const char* qp = Q + ((size_t)grp * 32 + r32) * ROWB + hh * 16;
#pragma unroll
    for (int st = 0; st < 16; ++st) asm volatile("global_load_dwordx4 %0, %1, off offset:%2" : "=v"(q[st]) : "v"(qp), "i"(st * 32) : "memory");
    const unsigned short* mp = MK + ((grp >> 3) * NT + (r32 & 15)) * NT + 4 * hh;
    asm volatile("global_load_dwordx2 %0, %1, off" : "=v"(mk0) : "v"(mp) : "memory");
    asm volatile("global_load_dwordx2 %0, %1, off offset:16" : "=v"(mk1) : "v"(mp) : "memory");
  }
  issue_k(Kg + woff, ring); issue_k(Kg + woff + 2 * BLKB, ring + BLKB);
  issue_v(Vg + woff, ring + TILEB); issue_v(Vg + woff + 2 * BLKB, ring + TILEB + BLKB);

  State z;
#pragma unroll
  for (int c = 0; c < 8; ++c) z.o[c] = f32x16{};
  z.m = -__builtin_inff(); z.l = 0.f;
  const unsigned krd = r32 * ROWB + ((hh ^ (r32 & 15)) << 4);
  const int q4 = (lane & 15) >> 2;
  const unsigned vrd = (4 * hh + q4) * ROWB + (q4 << 6) + ((lane >> 4) & 1) * 32 + (lane & 3) * 8;

  asm volatile("s_waitcnt vmcnt(32)"
               : "+v"(q[0]), "+v"(q[1]), "+v"(q[2]), "+v"(q[3]), "+v"(q[4]), "+v"(q[5]), "+v"(q[6]), "+v"(q[7]), "+v"(q[8]), "+v"(q[9]),
                 "+v"(q[10]), "+v"(q[11]), "+v"(q[12]), "+v"(q[13]), "+v"(q[14]), "+v"(q[15]), "+v"(mk0), "+v"(mk1) :: "memory");
  if (tree) {
    f32x16 s = qk_tile(lds, TREE_OFF + (r32 & 15) * ROWB + ((hh ^ (r32 & 15)) << 4), q);
#pragma unroll
    for (int r = 0; r < 8; ++r) {
      const unsigned w = (r < 4 ? mk0 : mk1)[(r >> 1) & 1];
      const float mv = __uint_as_float((r & 1) ? (w & 0xffff0000u) : (w << 16));
      s[r] = fmaf(1.0f - mv, MASKRAW, s[r]);
      s[r + 8] = -__builtin_inff();
    }
    bf16x8 p0, p1;
    softmax_tile(s, z, p0, p1);
    pv_step(lds, TREE_OFF + BLKB + vrd, 0, p0, z);
  }
  for (int n = 0; n < NTILE; ++n) {
    const bool more = n + 1 < NTILE;
    const size_t nx = woff + (size_t)(n + 1) * TSTEP;
    WAIT_VM(16);
    f32x16 s = qk_tile(lds, ring + krd, q);
    WAIT_LGKM0();
    if (more) { issue_k(Kg + nx, ring); issue_k(Kg + nx + 2 * BLKB, ring + BLKB); }
    bf16x8 p0, p1;
    softmax_tile(s, z, p0, p1);
    if (more) WAIT_VM(16); else WAIT_VM(0);
    pv_step(lds, ring + TILEB + vrd, 0, p0, z);
    pv_step(lds, ring + TILEB + vrd, BLKB, p1, z);
    WAIT_LGKM0();
    if (more) { issue_v(Vg + nx, ring + TILEB); issue_v(Vg + nx + 2 * BLKB, ring + TILEB + BLKB); }
  }

  z.l = xhalf_sum(z.l);
  __syncthreads();
  {
    LAS char* img = lds + wave * OIMG + r32 * ORS + hh * 16;
#pragma unroll
    for (int c = 0; c < 8; ++c)
#pragma unroll
      for (int g = 0; g < 4; ++g)
        *(LAS f32x4*)(img + c * 128 + g * 32) = f32x4{z.o[c][4 * g], z.o[c][4 * g + 1], z.o[c][4 * g + 2], z.o[c][4 * g + 3]};
    if (hh == 0) *(LAS f32x2*)(lds + ML_OFF + (wave * 32 + r32) * 8) = f32x2{z.m, z.l};
  }
  __syncthreads();
  const size_t slot = (size_t)grp * NSPL + mem;
#pragma unroll
  for (int k = 0; k < 8; ++k) {
    const int row = wave + 4 * k;
    f32x2 ml[4];
#pragma unroll
    for (int w = 0; w < 4; ++w) ml[w] = *(const LAS f32x2*)(lds + ML_OFF + (w * 32 + row) * 8);
    const float M = fmaxf(fmaxf(ml[0][0], ml[1][0]), fmaxf(ml[2][0], ml[3][0]));
    f32x4 acc = {0.f, 0.f, 0.f, 0.f}; float L = 0.f;
#pragma unroll
    for (int w = 0; w < 4; ++w) {
      const float f = __builtin_amdgcn_exp2f((ml[w][0] - M) * SCL2);
      acc += f * *(const LAS f32x4*)(lds + w * OIMG + row * ORS + lane * 16);
      L = fmaf(f, ml[w][1], L);
    }
    *(f32x4*)(po + (slot * 32 + row) * HD + lane * 4) = acc;
    if (lane == 0) *(f32x2*)(pml + (slot * 32 + row) * 2) = f32x2{M, L};
  }
}

__global__ void __launch_bounds__(256) tree_attn_combine(const float* __restrict__ po, const float* __restrict__ pml, unsigned short* __restrict__ out) {
  const int lane = threadIdx.x & 63, row = blockIdx.x * 4 + (threadIdx.x >> 6);
  const int grp = row >> 5, qr = row & 31;
  f32x2 ml[NSPL]; f32x4 a[NSPL];
#pragma unroll
  for (int s = 0; s < NSPL; ++s) {
    const size_t i = ((size_t)grp * NSPL + s) * 32 + qr;
    ml[s] = *(const f32x2*)(pml + i * 2);
    a[s] = *(const f32x4*)(po + i * HD + lane * 4);
  }
  float M = ml[0][0];
#pragma unroll
  for (int s = 1; s < NSPL; ++s) M = fmaxf(M, ml[s][0]);
  f32x4 acc = {0.f, 0.f, 0.f, 0.f}; float L = 0.f;
#pragma unroll
  for (int s = 0; s < NSPL; ++s) { const float f = __builtin_amdgcn_exp2f((ml[s][0] - M) * SCL2); acc += f * a[s]; L = fmaf(f, ml[s][1], L); }
  const float inv = 1.0f / L;
  acc *= inv;
  const bf16x2 o0 = __builtin_convertvector((f32x2){acc[0], acc[1]}, bf16x2), o1 = __builtin_convertvector((f32x2){acc[2], acc[3]}, bf16x2);
  *(u32x2*)(out + (size_t)row * HD + lane * 4) = u32x2{__builtin_bit_cast(unsigned, o0), __builtin_bit_cast(unsigned, o1)};
}

#include <cstdio>
__device__ __forceinline__ unsigned lat1(const char* a, unsigned& acc) {
  unsigned v; unsigned long long t0, t1;
  asm volatile("s_memtime %0\n\ts_waitcnt lgkmcnt(0)" : "=s"(t0) :: "memory");
  asm volatile("global_load_dword %0, %1, off nt\n\ts_waitcnt vmcnt(0)" : "=&v"(v) : "v"(a) : "memory");
  asm volatile("s_memtime %0\n\ts_waitcnt lgkmcnt(0)" : "=s"(t1) :: "memory");
  acc ^= v; return (unsigned)(t1 - t0);
}
__global__ void probe_k(const char* __restrict__ p0, const char* __restrict__ p1, unsigned* sink) {
  const int xcc = __builtin_amdgcn_s_getreg((31 << 11) | 20) & 7;
  if (threadIdx.x != 0) return;
  if (blockIdx.x == 0) printf("block0 xcc %d\n", xcc);
  if (xcc != 0) return;
  unsigned acc = 0;
  for (int w = 0; w < 2; ++w) {
    const char* p = w ? p1 : p0;
    unsigned long long lo = 0, hi = 0; int weak = 0;
    for (int i = 0; i < 128; ++i) {
      const char* a = p + ((size_t)i << 21);
      lat1(a + 0x10000, acc);
      int vote = 0;
      for (int k = 0; k < 5; ++k) { unsigned l0 = lat1(a + 0x4000 * (k + 8) + 0x100, acc), l1 = lat1(a + 0x4000 * (k + 8) + 0x2100, acc); vote += (l0 > l1) ? 1 : -1; }
      if (vote > 0) { if (i < 64) lo |= 1ull << i; else hi |= 1ull << (i - 64); }
      if (vote < 3 && vote > -3) ++weak;
    }
    printf("%s %p parity hi %016llx lo %016llx weak %d\n", w ? "V" : "K", p, hi, lo, weak);
  }
  if (acc == 0x1234567u) sink[0] = acc;
}
extern "C" void kernel_launch(void* const* d_in, const int* in_sizes, int n_in, void* d_out, int out_size, void* d_ws, size_t ws_size,
                              hipStream_t stream) {
  fprintf(stderr, "ptrs q %p k %p v %p kt %p vt %p m %p out %p ws %p\n", d_in[0], d_in[1], d_in[2], d_in[3], d_in[4], d_in[5], d_out, d_ws);
  probe_k<<<8, 64, 0, stream>>>((const char*)d_in[1], (const char*)d_in[2], (unsigned*)((char*)d_ws + (600ull << 20)));
  float* po = (float*)((char*)d_ws + WS_PO);
  float* pml = (float*)((char*)d_ws + WS_PML);
  tree_attn_split<<<NGRP * NSPL, 256, 0, stream>>>((const char*)d_in[0], (const char*)d_in[1], (const char*)d_in[2], (const char*)d_in[3],
                                                   (const char*)d_in[4], (const unsigned short*)d_in[5], po, pml);
  tree_attn_combine<<<NGRP * 32 / 4, 256, 0, stream>>>(po, pml, (unsigned short*)d_out);
}
```

```cpp
#include <hip/hip_runtime.h>
#include <hip/hip_bf16.h>

#define LAS __attribute__((address_space(3)))
typedef __bf16 bf16x8 __attribute__((ext_vector_type(8)));
typedef __bf16 bf16x4 __attribute__((ext_vector_type(4)));
typedef __bf16 bf16x2 __attribute__((ext_vector_type(2)));
typedef float f32x16 __attribute__((ext_vector_type(16)));
typedef float f32x4 __attribute__((ext_vector_type(4)));
typedef float f32x2 __attribute__((ext_vector_type(2)));
typedef unsigned u32x4 __attribute__((ext_vector_type(4)));
typedef unsigned u32x2 __attribute__((ext_vector_type(2)));

constexpr int NB = 8, HQ = 16, HKV = 8, NT = 16, CTX = 8192, HD = 256;
constexpr int NGRP = NB * HKV;
constexpr int NSPL = 4;
constexpr int ROWB = HD * 2;
constexpr int BLKB = 16 * ROWB;
constexpr int TILEB = 2 * BLKB;
constexpr int RINGB = 2 * TILEB;
constexpr int TREE_OFF = 4 * RINGB;
constexpr int ORS = HD * 4 + 16;
constexpr int OIMG = 32 * ORS;
constexpr int ML_OFF = 4 * OIMG;
constexpr int LDS_BYTES = TREE_OFF + 2 * BLKB;
static_assert(ML_OFF + 4 * 32 * 8 <= LDS_BYTES, "epilogue image must fit");
constexpr float SCL2 = 0.0625f * 1.4426950408889634f;
constexpr float MASKRAW = -1.0e9f * 16.0f;

constexpr size_t WS_PO = 0, WS_PML = (size_t)NGRP * NSPL * 32 * HD * 4;

__device__ __forceinline__ void dma4(const char* src, unsigned lds, unsigned v0, unsigned v1, unsigned v2, unsigned v3) {
  unsigned keep;
  asm volatile("s_mov_b32 %0, m0\n\ts_mov_b32 m0, %2\n\ts_nop 2\n\t"
               "global_load_lds_dwordx4 %3, %1 nt\n\t"
               "global_load_lds_dwordx4 %4, %1 offset:1024 nt\n\t"
               "global_load_lds_dwordx4 %5, %1 offset:2048 nt\n\t"
               "global_load_lds_dwordx4 %6, %1 offset:3072 nt\n\t"
               "s_mov_b32 m0, %0"
               : "=&s"(keep) : "s"(src), "s"(lds), "v"(v0), "v"(v1), "v"(v2), "v"(v3) : "memory");
}
#define WAIT_VM(n) asm volatile("s_waitcnt vmcnt(" #n ")" ::: "memory")
#define WAIT_LGKM0() asm volatile("s_waitcnt lgkmcnt(0)" ::: "memory")

__device__ __forceinline__ float xhalf_max(float x) {
  auto r = __builtin_amdgcn_permlane32_swap(__float_as_uint(x), __float_as_uint(x), false, false);
  return fmaxf(__uint_as_float(r[0]), __uint_as_float(r[1]));
}
__device__ __forceinline__ float xhalf_sum(float x) {
  auto r = __builtin_amdgcn_permlane32_swap(__float_as_uint(x), __float_as_uint(x), false, false);
  return __uint_as_float(r[0]) + __uint_as_float(r[1]);
}
__device__ __forceinline__ bf16x8 pack8(const f32x16& p, int b) {
  const bf16x2 a0 = __builtin_convertvector((f32x2){p[b + 0], p[b + 1]}, bf16x2), a1 = __builtin_convertvector((f32x2){p[b + 2], p[b + 3]}, bf16x2);
  const bf16x2 a2 = __builtin_convertvector((f32x2){p[b + 4], p[b + 5]}, bf16x2), a3 = __builtin_convertvector((f32x2){p[b + 6], p[b + 7]}, bf16x2);
  const u32x4 w = {__builtin_bit_cast(unsigned, a0), __builtin_bit_cast(unsigned, a1), __builtin_bit_cast(unsigned, a2), __builtin_bit_cast(unsigned, a3)};
  return __builtin_bit_cast(bf16x8, w);
}

struct State { f32x16 o[8]; float m, l; };

__device__ __forceinline__ f32x16 qk_tile(LAS char* lds, unsigned krd, const bf16x8 (&q)[16]) {
  f32x16 s = {};
#pragma unroll
  for (int st = 0; st < 16; ++st) {
    const bf16x8 a = *(const LAS bf16x8*)(lds + ((krd ^ ((st & 7) << 5)) + (st >> 3) * 256));
    s = __builtin_amdgcn_mfma_f32_32x32x16_bf16(a, q[st], s, 0, 0, 0);
  }
  return s;
}
__device__ __forceinline__ void softmax_tile(f32x16& s, State& z, bf16x8& p0, bf16x8& p1) {
  float mx = s[0];
#pragma unroll
  for (int r = 1; r < 16; ++r) mx = fmaxf(mx, s[r]);
  mx = xhalf_max(mx);
  const float mn = fmaxf(z.m, mx);
  const float alpha = __builtin_amdgcn_exp2f((z.m - mn) * SCL2);
  const float nb = -mn * SCL2;
  float ps = 0.f;
#pragma unroll
  for (int r = 0; r < 16; ++r) { s[r] = __builtin_amdgcn_exp2f(fmaf(s[r], SCL2, nb)); ps += s[r]; }
  z.l = fmaf(z.l, alpha, ps);
  z.m = mn;
  if (!__all(alpha == 1.0f)) {
#pragma unroll
    for (int c = 0; c < 8; ++c) z.o[c] *= alpha;
  }
  p0 = pack8(s, 0); p1 = pack8(s, 8);
}
__device__ __forceinline__ void pv_step(LAS char* lds, unsigned vrd, int koff, const bf16x8& p, State& z) {
#pragma unroll
  for (int c = 0; c < 8; ++c) {
    const unsigned a = (vrd ^ ((c & 3) << 6)) + (c >> 2) * 256 + koff;
    const bf16x4 lo = __builtin_amdgcn_ds_read_tr16_b64_v4bf16((LAS bf16x4*)(lds + a));
    const bf16x4 hi = __builtin_amdgcn_ds_read_tr16_b64_v4bf16((LAS bf16x4*)(lds + a + 8 * ROWB));
    const bf16x8 v = __builtin_shufflevector(lo, hi, 0, 1, 2, 3, 4, 5, 6, 7);
    z.o[c] = __builtin_amdgcn_mfma_f32_32x32x16_bf16(v, p, z.o[c], 0, 0, 0);
  }
}

__global__ void __launch_bounds__(256, 1)
tree_attn_split(const char* __restrict__ Q, const char* __restrict__ K, const char* __restrict__ V, const char* __restrict__ KT,
                const char* __restrict__ VT, const unsigned short* __restrict__ MK, float* __restrict__ po, float* __restrict__ pml) {
  __shared__ __attribute__((aligned(1024))) char smem[LDS_BYTES];
  LAS char* lds = (LAS char*)smem;
  const unsigned lbase = (unsigned)(size_t)lds;
  const int tid = threadIdx.x, lane = tid & 63, wave = __builtin_amdgcn_readfirstlane(tid >> 6);
  const int wg = blockIdx.x, x8 = wg & 7;
  const int grp = (wg >> 3) * 2 + ((x8 >> 1) & 1), mem = (x8 >> 2) * 2 + (x8 & 1);
  const int r32 = lane & 31, hh = lane >> 5;
  const bool tree = (mem == 0) && (wave == 0);

  unsigned ko[8], vo[2];
#pragma unroll
  for (int i = 0; i < 8; ++i) ko[i] = hh * ROWB + ((r32 ^ ((2 * i + hh) & 15)) << 4);
#pragma unroll
  for (int i = 0; i < 2; ++i) vo[i] = hh * ROWB + ((r32 ^ (((2 * i + hh) & 3) << 2)) << 4);
  const unsigned ring = wave * RINGB;
  auto issue_k = [&](const char* src, unsigned dst) {
    dma4(src, lbase + dst, ko[0], ko[1], ko[2], ko[3]);
    dma4(src + 4096, lbase + dst + 4096, ko[4], ko[5], ko[6], ko[7]);
  };
  auto issue_v = [&](const char* src, unsigned dst) {
    dma4(src, lbase + dst, vo[0], vo[1], vo[0], vo[1]);
    dma4(src + 4096, lbase + dst + 4096, vo[0], vo[1], vo[0], vo[1]);
  };
  const char* Kg = K + (size_t)grp * CTX * ROWB;
  const char* Vg = V + (size_t)grp * CTX * ROWB;
  const int cls = wave & 1, i8 = mem * 2 + (wave >> 1);
  const size_t woff = (size_t)(4 * i8 + cls) * BLKB;
  constexpr size_t TSTEP = (size_t)32 * BLKB;
  constexpr int NTILE = CTX / 32 / 16;

  if (tree) { issue_k(KT + (size_t)grp * BLKB, TREE_OFF); issue_v(VT + (size_t)grp * BLKB, TREE_OFF + BLKB); }
  bf16x8 q[16];
  u32x2 mk0 = {0u, 0u}, mk1 = {0u, 0u};
  {
    const char* qp = Q + ((size_t)grp * 32 + r32) * ROWB + hh * 16;
#pragma unroll
    for (int st = 0; st < 16; ++st) asm volatile("global_load_dwordx4 %0, %1, off offset:%2" : "=v"(q[st]) : "v"(qp), "i"(st * 32) : "memory");
    const unsigned short* mp = MK + ((grp >> 3) * NT + (r32 & 15)) * NT + 4 * hh;
    asm volatile("global_load_dwordx2 %0, %1, off" : "=v"(mk0) : "v"(mp) : "memory");
    asm volatile("global_load_dwordx2 %0, %1, off offset:16" : "=v"(mk1) : "v"(mp) : "memory");
  }
  issue_k(Kg + woff, ring); issue_k(Kg + woff + 2 * BLKB, ring + BLKB);
  issue_v(Vg + woff, ring + TILEB); issue_v(Vg + woff + 2 * BLKB, ring + TILEB + BLKB);

  State z;
#pragma unroll
  for (int c = 0; c < 8; ++c) z.o[c] = f32x16{};
  z.m = -__builtin_inff(); z.l = 0.f;
  const unsigned krd = r32 * ROWB + ((hh ^ (r32 & 15)) << 4);
  const int q4 = (lane & 15) >> 2;
  const unsigned vrd = (4 * hh + q4) * ROWB + (q4 << 6) + ((lane >> 4) & 1) * 32 + (lane & 3) * 8;

  asm volatile("s_waitcnt vmcnt(32)"
               : "+v"(q[0]), "+v"(q[1]), "+v"(q[2]), "+v"(q[3]), "+v"(q[4]), "+v"(q[5]), "+v"(q[6]), "+v"(q[7]), "+v"(q[8]), "+v"(q[9]),
                 "+v"(q[10]), "+v"(q[11]), "+v"(q[12]), "+v"(q[13]), "+v"(q[14]), "+v"(q[15]), "+v"(mk0), "+v"(mk1) :: "memory");
  if (tree) {
    f32x16 s = qk_tile(lds, TREE_OFF + (r32 & 15) * ROWB + ((hh ^ (r32 & 15)) << 4), q);
#pragma unroll
    for (int r = 0; r < 8; ++r) {
      const unsigned w = (r < 4 ? mk0 : mk1)[(r >> 1) & 1];
      const float mv = __uint_as_float((r & 1) ? (w & 0xffff0000u) : (w << 16));
      s[r] = fmaf(1.0f - mv, MASKRAW, s[r]);
      s[r + 8] = -__builtin_inff();
    }
    bf16x8 p0, p1;
    softmax_tile(s, z, p0, p1);
    pv_step(lds, TREE_OFF + BLKB + vrd, 0, p0, z);
  }
  for (int n = 0; n < NTILE; ++n) {
    const bool more = n + 1 < NTILE;
    const size_t nx = woff + (size_t)(n + 1) * TSTEP;
    WAIT_VM(16);
    f32x16 s = qk_tile(lds, ring + krd, q);
    WAIT_LGKM0();
    if (more) { issue_k(Kg + nx, ring); issue_k(Kg + nx + 2 * BLKB, ring + BLKB); }
    bf16x8 p0, p1;
    softmax_tile(s, z, p0, p1);
    if (more) WAIT_VM(16); else WAIT_VM(0);
    pv_step(lds, ring + TILEB + vrd, 0, p0, z);
    pv_step(lds, ring + TILEB + vrd, BLKB, p1, z);
    WAIT_LGKM0();
    if (more) { issue_v(Vg + nx, ring + TILEB); issue_v(Vg + nx + 2 * BLKB, ring + TILEB + BLKB); }
  }

  z.l = xhalf_sum(z.l);
  __syncthreads();
  {
    LAS char* img = lds + wave * OIMG + r32 * ORS + hh * 16;
#pragma unroll
    for (int c = 0; c < 8; ++c)
#pragma unroll
      for (int g = 0; g < 4; ++g)
        *(LAS f32x4*)(img + c * 128 + g * 32) = f32x4{z.o[c][4 * g], z.o[c][4 * g + 1], z.o[c][4 * g + 2], z.o[c][4 * g + 3]};
    if (hh == 0) *(LAS f32x2*)(lds + ML_OFF + (wave * 32 + r32) * 8) = f32x2{z.m, z.l};
  }
  __syncthreads();
  const size_t slot = (size_t)grp * NSPL + mem;
#pragma unroll
  for (int k = 0; k < 8; ++k) {
    const int row = wave + 4 * k;
    f32x2 ml[4];
#pragma unroll
    for (int w = 0; w < 4; ++w) ml[w] = *(const LAS f32x2*)(lds + ML_OFF + (w * 32 + row) * 8);
    const float M = fmaxf(fmaxf(ml[0][0], ml[1][0]), fmaxf(ml[2][0], ml[3][0]));
    f32x4 acc = {0.f, 0.f, 0.f, 0.f}; float L = 0.f;
#pragma unroll
    for (int w = 0; w < 4; ++w) {
      const float f = __builtin_amdgcn_exp2f((ml[w][0] - M) * SCL2);
      acc += f * *(const LAS f32x4*)(lds + w * OIMG + row * ORS + lane * 16);
      L = fmaf(f, ml[w][1], L);
    }
    *(f32x4*)(po + (slot * 32 + row) * HD + lane * 4) = acc;
    if (lane == 0) *(f32x2*)(pml + (slot * 32 + row) * 2) = f32x2{M, L};
  }
}

__global__ void __launch_bounds__(256) tree_attn_combine(const float* __restrict__ po, const float* __restrict__ pml, unsigned short* __restrict__ out) {
  const int lane = threadIdx.x & 63, row = blockIdx.x * 4 + (threadIdx.x >> 6);
  const int grp = row >> 5, qr = row & 31;
  f32x2 ml[NSPL]; f32x4 a[NSPL];
#pragma unroll
  for (int s = 0; s < NSPL; ++s) {
    const size_t i = ((size_t)grp * NSPL + s) * 32 + qr;
    ml[s] = *(const f32x2*)(pml + i * 2);
    a[s] = *(const f32x4*)(po + i * HD + lane * 4);
  }
  float M = ml[0][0];
#pragma unroll
  for (int s = 1; s < NSPL; ++s) M = fmaxf(M, ml[s][0]);
  f32x4 acc = {0.f, 0.f, 0.f, 0.f}; float L = 0.f;
#pragma unroll
  for (int s = 0; s < NSPL; ++s) { const float f = __builtin_amdgcn_exp2f((ml[s][0] - M) * SCL2); acc += f * a[s]; L = fmaf(f, ml[s][1], L); }
  const float inv = 1.0f / L;
  acc *= inv;
  const bf16x2 o0 = __builtin_convertvector((f32x2){acc[0], acc[1]}, bf16x2), o1 = __builtin_convertvector((f32x2){acc[2], acc[3]}, bf16x2);
  *(u32x2*)(out + (size_t)row * HD + lane * 4) = u32x2{__builtin_bit_cast(unsigned, o0), __builtin_bit_cast(unsigned, o1)};
}

extern "C" void kernel_launch(void* const* d_in, const int* in_sizes, int n_in, void* d_out, int out_size, void* d_ws, size_t ws_size,
                              hipStream_t stream) {
  float* po = (float*)((char*)d_ws + WS_PO);
  float* pml = (float*)((char*)d_ws + WS_PML);
  tree_attn_split<<<NGRP * NSPL, 256, 0, stream>>>((const char*)d_in[0], (const char*)d_in[1], (const char*)d_in[2], (const char*)d_in[3],
                                                   (const char*)d_in[4], (const unsigned short*)d_in[5], po, pml);
  tree_attn_combine<<<NGRP * 32 / 4, 256, 0, stream>>>(po, pml, (unsigned short*)d_out);
}
```

```cpp
#include <hip/hip_runtime.h>
#include <hip/hip_bf16.h>

#define LAS __attribute__((address_space(3)))
typedef __bf16 bf16x8 __attribute__((ext_vector_type(8)));
typedef __bf16 bf16x4 __attribute__((ext_vector_type(4)));
typedef __bf16 bf16x2 __attribute__((ext_vector_type(2)));
typedef float f32x16 __attribute__((ext_vector_type(16)));
typedef float f32x4 __attribute__((ext_vector_type(4)));
typedef float f32x2 __attribute__((ext_vector_type(2)));
typedef unsigned u32x4 __attribute__((ext_vector_type(4)));
typedef unsigned u32x2 __attribute__((ext_vector_type(2)));

constexpr int NB = 8, HKV = 8, NT = 16, CTX = 8192, HD = 256;
constexpr int NGRP = NB * HKV;
constexpr int NSPL = 4;
constexpr int ROWB = HD * 2;
constexpr int BLKB = 16 * ROWB;
constexpr int TILEB = 2 * BLKB;
constexpr int RINGB = 2 * TILEB;
constexpr int TREE_OFF = 4 * RINGB;
constexpr int Q_OFF = TREE_OFF + 2 * BLKB;
constexpr int LDS_BYTES = Q_OFF + TILEB;
constexpr int ML_OFF = Q_OFF;
constexpr int AR_OFF = ML_OFF + 4 * 32 * 8;
static_assert(LDS_BYTES <= 160 * 1024 && RINGB == 32 * HD * 4, "LDS budget");
constexpr float SCL2 = 0.0625f * 1.4426950408889634f;
constexpr float MASKRAW = -1.0e9f * 16.0f;
constexpr float DEFER = 8.0f / SCL2;

constexpr int NSUP = CTX / 64;
constexpr int USTAT = 16;
constexpr int NDYN = NSUP - USTAT;
constexpr unsigned W_T0 = 1u, W_T1 = 1u << 9, W_CHK = 1u << 18, W_ARR = 1u << 22, W_PUB = 1u << 27, W_EPOCH = 1u << 29;
constexpr unsigned W_EPOCH_MASK = 0xE0000000u;
constexpr int SPIN_MAX = 1 << 16;

constexpr size_t WS_CTL = 0, WS_PML = 65536, WS_PO = WS_PML + (size_t)NGRP * NSPL * 32 * 8;
constexpr size_t WS_END = WS_PO + (size_t)NGRP * NSPL * 32 * HD * 4;

__device__ __forceinline__ void dma4(const char* src, unsigned lds, unsigned v0, unsigned v1, unsigned v2, unsigned v3) {
  unsigned keep;
  asm volatile("s_mov_b32 %0, m0\n\ts_mov_b32 m0, %2\n\ts_nop 2\n\t"
               "global_load_lds_dwordx4 %3, %1 nt\n\t"
               "global_load_lds_dwordx4 %4, %1 offset:1024 nt\n\t"
               "global_load_lds_dwordx4 %5, %1 offset:2048 nt\n\t"
               "global_load_lds_dwordx4 %6, %1 offset:3072 nt\n\t"
               "s_mov_b32 m0, %0"
               : "=&s"(keep) : "s"(src), "s"(lds), "v"(v0), "v"(v1), "v"(v2), "v"(v3) : "memory");
}
#define WAIT_VM(n) asm volatile("s_waitcnt vmcnt(" #n ")" ::: "memory")
#define WAIT_LGKM0() asm volatile("s_waitcnt lgkmcnt(0)" ::: "memory")
#define BARRIER() do { WAIT_LGKM0(); __builtin_amdgcn_s_barrier(); asm volatile("" ::: "memory"); } while (0)

__device__ __forceinline__ void ctl_add_ret(unsigned* w, unsigned zero, unsigned inc) {
  unsigned long long ex;
  asm volatile("v_accvgpr_write_b32 a254, %3\n\ts_mov_b64 %0, exec\n\ts_mov_b64 exec, 1\n\ts_nop 2\n\t"
               "global_atomic_add a255, %2, a254, %1 sc0\n\ts_mov_b64 exec, %0\n\ts_nop 1"
               : "=&s"(ex) : "s"(w), "v"(zero), "v"(inc) : "memory", "a254", "a255");
}
__device__ __forceinline__ unsigned ctl_result() {
  unsigned t, r;
  asm volatile("v_accvgpr_read_b32 %0, a255\n\ts_nop 1\n\tv_readfirstlane_b32 %1, %0\n\ts_nop 4" : "=&v"(t), "=s"(r) :: "memory");
  return r;
}
__device__ __forceinline__ void ctl_add(unsigned* w, unsigned zero, unsigned inc) {
  unsigned long long ex;
  asm volatile("s_mov_b64 %0, exec\n\ts_mov_b64 exec, 1\n\ts_nop 2\n\tglobal_atomic_add %2, %3, %1\n\ts_mov_b64 exec, %0\n\ts_nop 1"
               : "=&s"(ex) : "s"(w), "v"(zero), "v"(inc) : "memory");
}
__device__ __forceinline__ void ctl_and(unsigned* w, unsigned zero, unsigned m) {
  unsigned long long ex;
  asm volatile("s_mov_b64 %0, exec\n\ts_mov_b64 exec, 1\n\ts_nop 2\n\tglobal_atomic_and %2, %3, %1\n\ts_mov_b64 exec, %0\n\ts_nop 1"
               : "=&s"(ex) : "s"(w), "v"(zero), "v"(m) : "memory");
}

__device__ __forceinline__ float xhalf_max(float x) {
  auto r = __builtin_amdgcn_permlane32_swap(__float_as_uint(x), __float_as_uint(x), false, false);
  return fmaxf(__uint_as_float(r[0]), __uint_as_float(r[1]));
}
__device__ __forceinline__ float xhalf_sum(float x) {
  auto r = __builtin_amdgcn_permlane32_swap(__float_as_uint(x), __float_as_uint(x), false, false);
  return __uint_as_float(r[0]) + __uint_as_float(r[1]);
}
__device__ __forceinline__ unsigned pack2(float a, float b) {
  return __builtin_bit_cast(unsigned, __builtin_convertvector((f32x2){a, b}, bf16x2));
}
__device__ __forceinline__ bf16x8 pack8(const f32x16& p, int b) {
  const u32x4 w = {pack2(p[b], p[b + 1]), pack2(p[b + 2], p[b + 3]), pack2(p[b + 4], p[b + 5]), pack2(p[b + 6], p[b + 7])};
  return __builtin_bit_cast(bf16x8, w);
}

struct State { f32x16 o[8]; float m, l; };

__device__ __forceinline__ bf16x8 row_frag(LAS char* lds, unsigned rd, int st) {
  return *(const LAS bf16x8*)(lds + ((rd ^ ((st & 7) << 5)) + (st >> 3) * 256));
}
__device__ __forceinline__ f32x16 qk_tile(LAS char* lds, unsigned krd, const bf16x8 (&q)[16]) {
  f32x16 s = {};
#pragma unroll
  for (int st = 0; st < 16; ++st) s = __builtin_amdgcn_mfma_f32_32x32x16_bf16(row_frag(lds, krd, st), q[st], s, 0, 0, 0);
  return s;
}
template <bool FIRST>
__device__ __forceinline__ void softmax_tile(f32x16& s, State& z, bf16x8& p0, bf16x8& p1) {
  float mx = s[0];
#pragma unroll
  for (int r = 1; r < 16; ++r) mx = fmaxf(mx, s[r]);
  mx = xhalf_max(mx);
  if constexpr (FIRST) z.m = mx;
  else if (!__all(mx <= z.m + DEFER)) {
    const float mn = fmaxf(z.m, mx);
    float alpha = __builtin_amdgcn_exp2f((z.m - mn) * SCL2);
    z.m = mn; z.l *= alpha;
    asm volatile("s_nop 1" : "+v"(alpha));
#pragma unroll
    for (int c = 0; c < 8; ++c)
#pragma unroll
      for (int r = 0; r < 16; r += 4) {
        float x0 = z.o[c][r], x1 = z.o[c][r + 1], x2 = z.o[c][r + 2], x3 = z.o[c][r + 3], t0, t1, t2, t3;
        asm volatile("v_accvgpr_read_b32 %4, %0\n\tv_accvgpr_read_b32 %5, %1\n\tv_accvgpr_read_b32 %6, %2\n\tv_accvgpr_read_b32 %7, %3\n\t"
                     "v_mul_f32 %4, %4, %8\n\tv_mul_f32 %5, %5, %8\n\tv_mul_f32 %6, %6, %8\n\tv_mul_f32 %7, %7, %8\n\t"
                     "v_accvgpr_write_b32 %0, %4\n\tv_accvgpr_write_b32 %1, %5\n\tv_accvgpr_write_b32 %2, %6\n\tv_accvgpr_write_b32 %3, %7\n\ts_nop 1"
                     : "+a"(x0), "+a"(x1), "+a"(x2), "+a"(x3), "=&v"(t0), "=&v"(t1), "=&v"(t2), "=&v"(t3) : "v"(alpha));
        z.o[c][r] = x0; z.o[c][r + 1] = x1; z.o[c][r + 2] = x2; z.o[c][r + 3] = x3;
      }
  }
  const float nb = -z.m * SCL2;
  float ps = 0.f;
#pragma unroll
  for (int r = 0; r < 16; ++r) { s[r] = __builtin_amdgcn_exp2f(fmaf(s[r], SCL2, nb)); ps += s[r]; }
  z.l += ps;
  p0 = pack8(s, 0); p1 = pack8(s, 8);
}
__device__ __forceinline__ void pv_step(LAS char* lds, unsigned vrd, int koff, const bf16x8& p, State& z) {
#pragma unroll
  for (int c = 0; c < 8; ++c) {
    const unsigned a = (vrd ^ ((c & 3) << 6)) + (c >> 2) * 256 + koff;
    const bf16x4 lo = __builtin_amdgcn_ds_read_tr16_b64_v4bf16((LAS bf16x4*)(lds + a));
    const bf16x4 hi = __builtin_amdgcn_ds_read_tr16_b64_v4bf16((LAS bf16x4*)(lds + a + 8 * ROWB));
    const bf16x8 v = __builtin_shufflevector(lo, hi, 0, 1, 2, 3, 4, 5, 6, 7);
    z.o[c] = __builtin_amdgcn_mfma_f32_32x32x16_bf16(v, p, z.o[c], 0, 0, 0);
  }
}
__device__ __forceinline__ int tile_off(int u, int c) { return (4 * u + (c ^ (u >> 6))) * BLKB; }
__device__ __forceinline__ int ticket_off(unsigned w, int c) {
  const int n0 = w & 0x1ff, n1 = (w >> 9) & 0x1ff;
  const int mine = c ? n1 : n0;
  const bool own = mine < NDYN;
  const int off = tile_off(USTAT + (own ? mine : 2 * NDYN - 1 - mine), own ? c : c ^ 1);
  return n0 + n1 < 2 * NDYN ? off : -1;
}

__global__ void __launch_bounds__(256, 1)
tree_attn(const char* __restrict__ Q, const char* __restrict__ K, const char* __restrict__ V, const char* __restrict__ KT,
          const char* __restrict__ VT, const char* __restrict__ MK, char* __restrict__ ws, unsigned short* __restrict__ out) {
  __shared__ __attribute__((aligned(1024))) char smem[LDS_BYTES];
  LAS char* lds = (LAS char*)smem;
  const unsigned lbase = (unsigned)(size_t)lds;
  const int tid = threadIdx.x, lane = tid & 63, wave = __builtin_amdgcn_readfirstlane(tid >> 6);
  const int wg = blockIdx.x, x8 = wg & 7;
  const int grp = (wg >> 3) * 2 + ((x8 >> 1) & 1), mem = (x8 >> 2) * 2 + (x8 & 1);
  const int r32 = lane & 31, hh = lane >> 5;
  const bool tree = (mem == 0) && (wave == 0);
  const bool leader = (mem == 1) && (wave == 0);
  unsigned* cw = (unsigned*)(ws + WS_CTL + grp * 256);
  const unsigned vz = 0;
  if (leader) { ctl_and(cw, vz, W_EPOCH_MASK); ctl_add(cw, vz, W_EPOCH); }
  else ctl_add_ret(cw, vz, 0u);

  unsigned ko[8], vo[2];
#pragma unroll
  for (int i = 0; i < 8; ++i) ko[i] = hh * ROWB + ((r32 ^ ((2 * i + hh) & 15)) << 4);
#pragma unroll
  for (int i = 0; i < 2; ++i) vo[i] = hh * ROWB + ((r32 ^ (((2 * i + hh) & 3) << 2)) << 4);
  const unsigned ring = wave * RINGB;
  auto issue_k = [&](const char* src, unsigned dst) {
    dma4(src, lbase + dst, ko[0], ko[1], ko[2], ko[3]);
    dma4(src + 4096, lbase + dst + 4096, ko[4], ko[5], ko[6], ko[7]);
  };
  auto issue_v = [&](const char* src, unsigned dst) {
    dma4(src, lbase + dst, vo[0], vo[1], vo[0], vo[1]);
    dma4(src + 4096, lbase + dst + 4096, vo[0], vo[1], vo[0], vo[1]);
  };
  const char* Kg = K + (size_t)grp * CTX * ROWB;
  const char* Vg = V + (size_t)grp * CTX * ROWB;
  auto tile_k = [&](int off) { issue_k(Kg + off, ring); issue_k(Kg + off + 2 * BLKB, ring + BLKB); };
  auto tile_v = [&](int off) { issue_v(Vg + off, ring + TILEB); issue_v(Vg + off + 2 * BLKB, ring + TILEB + BLKB); };
  const int cls = wave & 1, i8 = mem * 2 + (wave >> 1);
  const unsigned inc = cls ? W_T1 : W_T0;

  {
    const unsigned x = (wave & 1) << 7;
    dma4(Q + (size_t)grp * TILEB + wave * 4096, lbase + Q_OFF + wave * 4096, ko[0] ^ x, ko[1] ^ x, ko[2] ^ x, ko[3] ^ x);
  }
  if (tree) {
    issue_k(KT + (size_t)grp * BLKB, TREE_OFF); issue_v(VT + (size_t)grp * BLKB, TREE_OFF + BLKB);
    const unsigned mo = (r32 & 15) * (NT * 2) + 8 * hh;
    asm volatile("s_nop 4\n\tglobal_load_dwordx2 a[248:249], %0, %1\n\tglobal_load_dwordx2 a[250:251], %0, %1 offset:16"
                 :: "v"(mo), "s"(MK + (grp >> 3) * (NT * NT * 2)) : "memory", "a248", "a249", "a250", "a251");
  }
  tile_k(tile_off(i8, cls)); tile_v(tile_off(i8, cls));

  State z;
#pragma unroll
  for (int c = 0; c < 8; ++c) z.o[c] = f32x16{};
  z.m = -__builtin_inff(); z.l = 0.f;
  const unsigned krd = r32 * ROWB + ((hh ^ (r32 & 15)) << 4);
  const int q4 = (lane & 15) >> 2;
  const unsigned vrd = (4 * hh + q4) * ROWB + (q4 << 6) + ((lane >> 4) & 1) * 32 + (lane & 3) * 8;

  WAIT_VM(32);
  const unsigned w0 = ctl_result();
  if (leader) ctl_add(cw, vz, W_EPOCH);
  BARRIER();
  bf16x8 q[16];
#pragma unroll
  for (int st = 0; st < 16; ++st) q[st] = row_frag(lds, Q_OFF + krd, st);
  BARRIER();

  if (tree) {
    unsigned mk[4];
    asm volatile("v_accvgpr_read_b32 %0, a248\n\tv_accvgpr_read_b32 %1, a249\n\tv_accvgpr_read_b32 %2, a250\n\tv_accvgpr_read_b32 %3, a251"
                 : "=v"(mk[0]), "=v"(mk[1]), "=v"(mk[2]), "=v"(mk[3]) :: "memory");
    f32x16 s = qk_tile(lds, TREE_OFF + (r32 & 15) * ROWB + ((hh ^ (r32 & 15)) << 4), q);
#pragma unroll
    for (int r = 0; r < 8; ++r) {
      const float mv = __uint_as_float((r & 1) ? (mk[r >> 1] & 0xffff0000u) : (mk[r >> 1] << 16));
      s[r] = fmaf(1.0f - mv, MASKRAW, s[r]);
      s[r + 8] = -__builtin_inff();
    }
    bf16x8 p0, p1;
    softmax_tile<true>(s, z, p0, p1);
    pv_step(lds, TREE_OFF + BLKB + vrd, 0, p0, z);
  }

  bool ready = leader;
  bool have = false;
  unsigned chk = leader ? 0u : W_CHK;
  int nxt = tile_off(i8 + 8, cls);
  for (;;) {
    for (;;) {
      WAIT_VM(16);
      if (have) nxt = ticket_off(ctl_result(), cls);
      f32x16 s = qk_tile(lds, ring + krd, q);
      WAIT_LGKM0();
      if (!ready) ctl_add_ret(cw, vz, 0u);
      if (nxt >= 0) tile_k(nxt);
      bf16x8 p0, p1;
      softmax_tile<false>(s, z, p0, p1);
      if (nxt >= 0) WAIT_VM(16); else WAIT_VM(0);
      if (!ready) ready = ctl_result() != w0;
      pv_step(lds, ring + TILEB + vrd, 0, p0, z);
      pv_step(lds, ring + TILEB + vrd, BLKB, p1, z);
      WAIT_LGKM0();
      if (nxt < 0) break;
      have = ready;
      if (have) { ctl_add_ret(cw, vz, inc + chk); chk = 0u; }
      tile_v(nxt);
      nxt = -1;
    }
    if (have) break;
    for (int i = 0; i < SPIN_MAX && !ready; ++i) {
      __builtin_amdgcn_s_sleep(16);
      ctl_add_ret(cw, vz, 0u); WAIT_VM(0);
      ready = ctl_result() != w0;
    }
    ready = true;
    ctl_add_ret(cw, vz, inc + chk); chk = 0u; WAIT_VM(0);
    const int cur = ticket_off(ctl_result(), cls);
    if (cur < 0) break;
    tile_k(cur); ctl_add_ret(cw, vz, inc); tile_v(cur);
    have = true;
  }
  ctl_add_ret(cw, vz, W_ARR);

  z.l = xhalf_sum(z.l);
  {
    const unsigned img = ring + r32 * (HD * 4) + ((hh ^ (r32 & 7)) << 4);
#pragma unroll
    for (int c = 0; c < 8; ++c)
#pragma unroll
      for (int g = 0; g < 4; ++g)
        *(LAS f32x4*)(lds + ((img ^ (g << 5)) + c * 128)) = f32x4{z.o[c][4 * g], z.o[c][4 * g + 1], z.o[c][4 * g + 2], z.o[c][4 * g + 3]};
    if (hh == 0) *(LAS f32x2*)(lds + ML_OFF + (wave * 32 + r32) * 8) = f32x2{z.m, z.l};
  }
  BARRIER();
  f32x4 acc[8]; float Mr = 0.f, Lr = 0.f;
#pragma unroll
  for (int k = 0; k < 8; ++k) {
    const int row = wave + 4 * k;
    f32x2 ml[4];
#pragma unroll
    for (int w = 0; w < 4; ++w) ml[w] = *(const LAS f32x2*)(lds + ML_OFF + (w * 32 + row) * 8);
    const float M = fmaxf(fmaxf(ml[0][0], ml[1][0]), fmaxf(ml[2][0], ml[3][0]));
    f32x4 a = {0.f, 0.f, 0.f, 0.f}; float L = 0.f;
#pragma unroll
    for (int w = 0; w < 4; ++w) {
      const float f = __builtin_amdgcn_exp2f((ml[w][0] - M) * SCL2);
      a += f * *(const LAS f32x4*)(lds + w * RINGB + row * (HD * 4) + ((lane ^ (row & 7)) << 4));
      L = fmaf(f, ml[w][1], L);
    }
    acc[k] = a;
    if ((lane & 7) == k) { Mr = M; Lr = L; }
  }
  WAIT_VM(0);
  const unsigned war = ctl_result();
  if (lane == 0) *(LAS unsigned*)(lds + AR_OFF + wave * 4) = war;
  BARRIER();
  const u32x4 ar = *(const LAS u32x4*)(lds + AR_OFF);
  unsigned last = 0;
#pragma unroll
  for (int w = 0; w < 4; ++w) if (((ar[w] >> 22) & 31u) == 15u) last = ar[w] | 1u;
  last = __builtin_amdgcn_readfirstlane(last);

  const __amdgpu_buffer_rsrc_t rs = __builtin_amdgcn_make_buffer_rsrc(ws, 0, (int)WS_END, 0x00020000);
  const int ml_off = (int)WS_PML + (grp * NSPL * 32 + wave + 4 * (lane & 7)) * 8;
  const int po_off = (int)WS_PO + (grp * NSPL * 32 + wave) * (HD * 4) + lane * 16;
  if (!last) {
#pragma unroll
    for (int k = 0; k < 8; ++k) __builtin_amdgcn_raw_buffer_store_b128(__builtin_bit_cast(u32x4, acc[k]), rs, po_off + k * 4096, mem * RINGB, 16);
    if (lane < 8) __builtin_amdgcn_raw_buffer_store_b64(__builtin_bit_cast(u32x2, f32x2{Mr, Lr}), rs, ml_off, mem * 256, 16);
    WAIT_VM(0);
    BARRIER();
    if (wave == 0) ctl_add(cw, vz, W_PUB);
    if (leader) {
      unsigned w = war;
      for (int i = 0; i < SPIN_MAX && ((w >> 18) & 15u) != 15u; ++i) {
        __builtin_amdgcn_s_sleep(16);
        ctl_add_ret(cw, vz, W_EPOCH); WAIT_VM(0);
        w = ctl_result();
      }
    }
  } else {
    if (((last >> 27) & 3u) != 3u) {
      if (wave == 0) {
        unsigned w = last;
        for (int i = 0; i < SPIN_MAX && ((w >> 27) & 3u) != 3u; ++i) {
          ctl_add_ret(cw, vz, 0u); WAIT_VM(0);
          w = ctl_result();
        }
      }
      BARRIER();
    }
    f32x2 oml[3]; f32x4 oa[3][8];
#pragma unroll
    for (int j = 0; j < 3; ++j) {
      const int s = (mem + 1 + j) & 3;
      oml[j] = __builtin_bit_cast(f32x2, __builtin_amdgcn_raw_buffer_load_b64(rs, ml_off, s * 256, 16));
#pragma unroll
      for (int k = 0; k < 8; ++k) oa[j][k] = __builtin_bit_cast(f32x4, __builtin_amdgcn_raw_buffer_load_b128(rs, po_off + k * 4096, s * RINGB, 16));
    }
    const float M = fmaxf(fmaxf(Mr, oml[0][0]), fmaxf(oml[1][0], oml[2][0]));
    float f[4]; float L = 0.f;
    f[3] = __builtin_amdgcn_exp2f((Mr - M) * SCL2); L = f[3] * Lr;
#pragma unroll
    for (int j = 0; j < 3; ++j) { f[j] = __builtin_amdgcn_exp2f((oml[j][0] - M) * SCL2); L = fmaf(f[j], oml[j][1], L); }
    const float inv = 1.0f / L;
#pragma unroll
    for (int j = 0; j < 4; ++j) f[j] *= inv;
#pragma unroll
    for (int k = 0; k < 8; ++k) {
      f32x4 o = acc[k] * __uint_as_float(__builtin_amdgcn_readlane(__float_as_uint(f[3]), k));
#pragma unroll
      for (int j = 0; j < 3; ++j) o += oa[j][k] * __uint_as_float(__builtin_amdgcn_readlane(__float_as_uint(f[j]), k));
      *(u32x2*)(out + (size_t)(grp * 32 + wave + 4 * k) * HD + lane * 4) = u32x2{pack2(o[0], o[1]), pack2(o[2], o[3])};
    }
  }
}

extern "C" void kernel_launch(void* const* d_in, const int* in_sizes, int n_in, void* d_out, int out_size, void* d_ws, size_t ws_size,
                              hipStream_t stream) {
  tree_attn<<<NGRP * NSPL, 256, 0, stream>>>((const char*)d_in[0], (const char*)d_in[1], (const char*)d_in[2], (const char*)d_in[3],
                                             (const char*)d_in[4], (const char*)d_in[5], (char*)d_ws, (unsigned short*)d_out);
}
```

```cpp
#include <hip/hip_runtime.h>
#include <hip/hip_bf16.h>

#define LAS __attribute__((address_space(3)))
typedef __bf16 bf16x8 __attribute__((ext_vector_type(8)));
typedef __bf16 bf16x4 __attribute__((ext_vector_type(4)));
typedef __bf16 bf16x2 __attribute__((ext_vector_type(2)));
typedef float f32x16 __attribute__((ext_vector_type(16)));
typedef float f32x4 __attribute__((ext_vector_type(4)));
typedef float f32x2 __attribute__((ext_vector_type(2)));
typedef unsigned u32x4 __attribute__((ext_vector_type(4)));
typedef unsigned u32x2 __attribute__((ext_vector_type(2)));

constexpr int NB = 8, HKV = 8, NT = 16, CTX = 8192, HD = 256;
constexpr int NGRP = NB * HKV;
constexpr int NSPL = 4;
constexpr int ROWB = HD * 2;
constexpr int BLKB = 16 * ROWB;
constexpr int TILEB = 2 * BLKB;
constexpr int RINGB = 2 * TILEB;
constexpr int TREE_OFF = 4 * RINGB;
constexpr int Q_OFF = TREE_OFF + 2 * BLKB;
constexpr int LDS_BYTES = Q_OFF + TILEB;
constexpr int CAP = 20;
constexpr int MT_OFF = Q_OFF;
constexpr int ML_OFF = MT_OFF + 4 * CAP * 128;
constexpr int AR_OFF = ML_OFF + 4 * 32 * 8;
static_assert(LDS_BYTES <= 160 * 1024 && RINGB == 32 * HD * 4 && AR_OFF + 16 <= LDS_BYTES, "LDS budget");
constexpr float SCL2 = 0.0625f * 1.4426950408889634f;
constexpr float MASKRAW = -1.0e9f * 16.0f;
constexpr float DEFER = 8.0f / SCL2;

constexpr int NSUP = CTX / 64;
constexpr int NSTAT = 4;
constexpr int USTAT = 8 * NSTAT;
constexpr int NDYN = NSUP - USTAT;
#define RBOUND 132
constexpr unsigned W_T0 = 1u, W_T1 = 1u << 9, W_CHK = 1u << 18, W_ARR = 1u << 22, W_PUB = 1u << 27, W_EPOCH = 1u << 29;
constexpr unsigned W_EPOCH_MASK = 0xE0000000u;
constexpr int SPIN_MAX = 1 << 16;

constexpr size_t WS_CTL = 0, WS_PML = 65536, WS_PO = WS_PML + (size_t)NGRP * NSPL * 32 * 8;
constexpr size_t WS_END = WS_PO + (size_t)NGRP * NSPL * 32 * HD * 4;

__device__ __forceinline__ void dma4(const char* src, unsigned lds, unsigned v0, unsigned v1, unsigned v2, unsigned v3) {
  unsigned keep;
  asm volatile("s_mov_b32 %0, m0\n\ts_mov_b32 m0, %2\n\ts_nop 2\n\t"
               "global_load_lds_dwordx4 %3, %1 nt\n\t"
               "global_load_lds_dwordx4 %4, %1 offset:1024 nt\n\t"
               "global_load_lds_dwordx4 %5, %1 offset:2048 nt\n\t"
               "global_load_lds_dwordx4 %6, %1 offset:3072 nt\n\t"
               "s_mov_b32 m0, %0"
               : "=&s"(keep) : "s"(src), "s"(lds), "v"(v0), "v"(v1), "v"(v2), "v"(v3) : "memory");
}
#define WAIT_VM(n) asm volatile("s_waitcnt vmcnt(" #n ")" ::: "memory")
#define WAIT_LGKM0() asm volatile("s_waitcnt lgkmcnt(0)" ::: "memory")
#define BARRIER() do { WAIT_LGKM0(); __builtin_amdgcn_s_barrier(); asm volatile("" ::: "memory"); } while (0)

__device__ __forceinline__ void ctl_add_ret(unsigned* w, unsigned zero, unsigned inc) {
  unsigned long long ex;
  asm volatile("v_accvgpr_write_b32 a254, %3\n\ts_mov_b64 %0, exec\n\ts_mov_b64 exec, 1\n\ts_nop 2\n\t"
               "global_atomic_add a255, %2, a254, %1 sc0\n\ts_mov_b64 exec, %0\n\ts_nop 1"
               : "=&s"(ex) : "s"(w), "v"(zero), "v"(inc) : "memory", "a254", "a255");
}
__device__ __forceinline__ unsigned ctl_result() {
  unsigned t, r;
  asm volatile("v_accvgpr_read_b32 %0, a255\n\ts_nop 1\n\tv_readfirstlane_b32 %1, %0\n\ts_nop 4" : "=&v"(t), "=s"(r) :: "memory");
  return r;
}
__device__ __forceinline__ void ctl_add(unsigned* w, unsigned zero, unsigned inc) {
  unsigned long long ex;
  asm volatile("s_mov_b64 %0, exec\n\ts_mov_b64 exec, 1\n\ts_nop 2\n\tglobal_atomic_add %2, %3, %1\n\ts_mov_b64 exec, %0\n\ts_nop 1"
               : "=&s"(ex) : "s"(w), "v"(zero), "v"(inc) : "memory");
}
__device__ __forceinline__ void ctl_and(unsigned* w, unsigned zero, unsigned m) {
  unsigned long long ex;
  asm volatile("s_mov_b64 %0, exec\n\ts_mov_b64 exec, 1\n\ts_nop 2\n\tglobal_atomic_and %2, %3, %1\n\ts_mov_b64 exec, %0\n\ts_nop 1"
               : "=&s"(ex) : "s"(w), "v"(zero), "v"(m) : "memory");
}
#define PARK_HEAD "s_getpc_b64 vcc\n\t.Lp0_%=:\n\ts_add_u32 vcc_lo, vcc_lo, %8\n\ts_addc_u32 vcc_hi, vcc_hi, 0\n\t" \
                  "s_add_u32 vcc_lo, vcc_lo, .Lp1_%=-.Lp0_%=\n\ts_addc_u32 vcc_hi, vcc_hi, 0\n\ts_setpc_b64 vcc\n\t.Lp1_%=:\n\t.set .Lpi_%=, 128\n\t.rept 15\n\t"
#define PARK_MID "s_branch .Lp2_%=\n\t.set .Lpi_%=, .Lpi_%=+8\n\t.endr\n\t.set .Lpi_%=, 216\n\t.rept 5\n\t"
#define PARK_TAIL "s_branch .Lp2_%=\n\t.set .Lpi_%=, .Lpi_%=+8\n\t.endr\n\t.Lp2_%=:\n\ts_nop 1"
#define PARK_PUT(op, f) op " " f "[.Lpi_%=], %0\n\t" op " " f "[.Lpi_%=+1], %1\n\t" op " " f "[.Lpi_%=+2], %2\n\t" op " " f "[.Lpi_%=+3], %3\n\t" \
                        op " " f "[.Lpi_%=+4], %4\n\t" op " " f "[.Lpi_%=+5], %5\n\t" op " " f "[.Lpi_%=+6], %6\n\t" op " " f "[.Lpi_%=+7], %7\n\t"
#define PARK_GET(op, f) op " %0, " f "[.Lpi_%=]\n\t" op " %1, " f "[.Lpi_%=+1]\n\t" op " %2, " f "[.Lpi_%=+2]\n\t" op " %3, " f "[.Lpi_%=+3]\n\t" \
                        op " %4, " f "[.Lpi_%=+4]\n\t" op " %5, " f "[.Lpi_%=+5]\n\t" op " %6, " f "[.Lpi_%=+6]\n\t" op " %7, " f "[.Lpi_%=+7]\n\t"
__device__ __forceinline__ void park_put(int j, const u32x4& p0, const u32x4& p1) {
  asm volatile(PARK_HEAD PARK_PUT("v_accvgpr_write_b32", "a") PARK_MID PARK_PUT("v_mov_b32_e64", "v") PARK_TAIL
               :: "v"(p0[0]), "v"(p0[1]), "v"(p0[2]), "v"(p0[3]), "v"(p1[0]), "v"(p1[1]), "v"(p1[2]), "v"(p1[3]), "s"(j * 68)
               : "memory", "vcc", "scc", "v216", "v255");
}
__device__ __forceinline__ void park_get(int j, u32x4& p0, u32x4& p1) {
  asm volatile(PARK_HEAD PARK_GET("v_accvgpr_read_b32", "a") PARK_MID PARK_GET("v_mov_b32_e64", "v") PARK_TAIL
               : "=v"(p0[0]), "=v"(p0[1]), "=v"(p0[2]), "=v"(p0[3]), "=v"(p1[0]), "=v"(p1[1]), "=v"(p1[2]), "=v"(p1[3]) : "s"(j * 68)
               : "memory", "vcc", "scc");
}
static_assert(CAP == 20, "park_put / park_get hold 15 + 5 blocks");
__device__ __forceinline__ void acc_zero() {
  asm volatile(".set .Lz_%=, 0\n\t.rept 128\n\tv_accvgpr_write_b32 a[.Lz_%=], 0\n\t.set .Lz_%=, .Lz_%=+1\n\t.endr" ::: "memory", "a0", "a127", "a247");
}
__device__ __forceinline__ void acc_scale(float alpha) {
  float t0, t1, t2, t3;
  asm volatile("s_nop 15\n\t.set .Ls_%=, 0\n\t.rept 32\n\t"
               "v_accvgpr_read_b32 %0, a[.Ls_%=]\n\tv_accvgpr_read_b32 %1, a[.Ls_%=+1]\n\tv_accvgpr_read_b32 %2, a[.Ls_%=+2]\n\tv_accvgpr_read_b32 %3, a[.Ls_%=+3]\n\t"
               "v_mul_f32 %0, %0, %4\n\tv_mul_f32 %1, %1, %4\n\tv_mul_f32 %2, %2, %4\n\tv_mul_f32 %3, %3, %4\n\t"
               "v_accvgpr_write_b32 a[.Ls_%=], %0\n\tv_accvgpr_write_b32 a[.Ls_%=+1], %1\n\tv_accvgpr_write_b32 a[.Ls_%=+2], %2\n\tv_accvgpr_write_b32 a[.Ls_%=+3], %3\n\t"
               ".set .Ls_%=, .Ls_%=+4\n\t.endr\n\ts_nop 1"
               : "=&v"(t0), "=&v"(t1), "=&v"(t2), "=&v"(t3) : "v"(alpha) : "memory");
}
__device__ __forceinline__ f32x16 mfma_qk(const bf16x8 (&f)[16], const bf16x8 (&q)[16]) {
  f32x16 s;
#define M_(a, b) "v_mfma_f32_32x32x16_bf16 %0, %" #a ", %" #b ", %0\n\t"
  asm volatile("s_nop 1\n\tv_mfma_f32_32x32x16_bf16 %0, %1, %9, 0\n\t" M_(2, 10) M_(3, 11) M_(4, 12) M_(5, 13) M_(6, 14) M_(7, 15) M_(8, 16)
               : "=&v"(s) : "v"(f[0]), "v"(f[1]), "v"(f[2]), "v"(f[3]), "v"(f[4]), "v"(f[5]), "v"(f[6]), "v"(f[7]),
                 "v"(q[0]), "v"(q[1]), "v"(q[2]), "v"(q[3]), "v"(q[4]), "v"(q[5]), "v"(q[6]), "v"(q[7]));
  asm volatile(M_(1, 9) M_(2, 10) M_(3, 11) M_(4, 12) M_(5, 13) M_(6, 14) M_(7, 15) M_(8, 16) "s_nop 15"
               : "+v"(s) : "v"(f[8]), "v"(f[9]), "v"(f[10]), "v"(f[11]), "v"(f[12]), "v"(f[13]), "v"(f[14]), "v"(f[15]),
                 "v"(q[8]), "v"(q[9]), "v"(q[10]), "v"(q[11]), "v"(q[12]), "v"(q[13]), "v"(q[14]), "v"(q[15]));
#undef M_
  return s;
}
__device__ __forceinline__ void mfma_pv(const bf16x8* f, const u32x4& p) {
#define M_(a, lo, hi) "v_mfma_f32_32x32x16_bf16 a[" #lo ":" #hi "], %" #a ", %8, a[" #lo ":" #hi "]\n\t"
  asm volatile("s_nop 1\n\t" M_(0, 0, 15) M_(1, 16, 31) M_(2, 32, 47) M_(3, 48, 63) M_(4, 64, 79) M_(5, 80, 95) M_(6, 96, 111) M_(7, 112, 127)
               :: "v"(f[0]), "v"(f[1]), "v"(f[2]), "v"(f[3]), "v"(f[4]), "v"(f[5]), "v"(f[6]), "v"(f[7]), "v"(p) : "memory");
#undef M_
}
__device__ __forceinline__ void acc_store(unsigned a0, unsigned a1, unsigned a2, unsigned a3) {
  asm volatile("s_nop 15\n\t.set .Lw_%=, 0\n\t.rept 8\n\t"
               "ds_write_b128 %0, a[.Lw_%=*16:.Lw_%=*16+3] offset:.Lw_%=*128\n\tds_write_b128 %1, a[.Lw_%=*16+4:.Lw_%=*16+7] offset:.Lw_%=*128\n\t"
               "ds_write_b128 %2, a[.Lw_%=*16+8:.Lw_%=*16+11] offset:.Lw_%=*128\n\tds_write_b128 %3, a[.Lw_%=*16+12:.Lw_%=*16+15] offset:.Lw_%=*128\n\t"
               ".set .Lw_%=, .Lw_%=+1\n\t.endr"
               :: "v"(a0), "v"(a1), "v"(a2), "v"(a3) : "memory");
}

__device__ __forceinline__ float xhalf_max(float x) {
  auto r = __builtin_amdgcn_permlane32_swap(__float_as_uint(x), __float_as_uint(x), false, false);
  return fmaxf(__uint_as_float(r[0]), __uint_as_float(r[1]));
}
__device__ __forceinline__ float xhalf_sum(float x) {
  auto r = __builtin_amdgcn_permlane32_swap(__float_as_uint(x), __float_as_uint(x), false, false);
  return __uint_as_float(r[0]) + __uint_as_float(r[1]);
}
__device__ __forceinline__ unsigned pack2(float a, float b) {
  return __builtin_bit_cast(unsigned, __builtin_convertvector((f32x2){a, b}, bf16x2));
}
__device__ __forceinline__ float lane_bcast(float x, int l) { return __uint_as_float(__builtin_amdgcn_readlane(__float_as_uint(x), l)); }

__device__ __forceinline__ bf16x8 row_frag(LAS char* lds, unsigned rd, int st) {
  return *(const LAS bf16x8*)(lds + ((rd ^ ((st & 7) << 5)) + (st >> 3) * 256));
}
__device__ __forceinline__ void softmax_tile(f32x16& s, float& m, float& l, u32x4& p0, u32x4& p1) {
  float mx = s[0];
#pragma unroll
  for (int r = 1; r < 16; ++r) mx = fmaxf(mx, s[r]);
  mx = xhalf_max(mx);
  if (!__all(mx <= m + DEFER)) {
    const float mn = fmaxf(m, mx);
    l *= __builtin_amdgcn_exp2f((m - mn) * SCL2);
    m = mn;
  }
  const float nb = -m * SCL2;
  float ps = 0.f;
#pragma unroll
  for (int r = 0; r < 16; ++r) { s[r] = __builtin_amdgcn_exp2f(fmaf(s[r], SCL2, nb)); ps += s[r]; }
  l += ps;
#pragma unroll
  for (int i = 0; i < 4; ++i) { p0[i] = pack2(s[2 * i], s[2 * i + 1]); p1[i] = pack2(s[8 + 2 * i], s[9 + 2 * i]); }
}
__device__ __forceinline__ bf16x8 col_frag(LAS char* lds, unsigned vrd, int c, int koff) {
  const unsigned a = (vrd ^ ((c & 3) << 6)) + (c >> 2) * 256 + koff;
  const bf16x4 lo = __builtin_amdgcn_ds_read_tr16_b64_v4bf16((LAS bf16x4*)(lds + a));
  const bf16x4 hi = __builtin_amdgcn_ds_read_tr16_b64_v4bf16((LAS bf16x4*)(lds + a + 8 * ROWB));
  return __builtin_shufflevector(lo, hi, 0, 1, 2, 3, 4, 5, 6, 7);
}
__device__ __forceinline__ int tile_off(int u, int c) { return (4 * u + (c ^ (u >> 6))) * BLKB; }
__device__ __forceinline__ int ticket(unsigned w, int c) {
  const int n0 = w & 0x1ff, n1 = (w >> 9) & 0x1ff, tot = n0 + n1;
  const int mine = c ? n1 : n0;
  const bool own = mine < NDYN;
  const int off = tile_off(USTAT + (own ? mine : 2 * NDYN - 1 - mine), own ? c : c ^ 1);
  return tot < 2 * NDYN ? off + (tot >= RBOUND) : -1;
}

__global__ void __launch_bounds__(256, 1)
tree_attn(const char* __restrict__ Q, const char* __restrict__ K, const char* __restrict__ V, const char* __restrict__ KT,
          const char* __restrict__ VT, const char* __restrict__ MK, char* __restrict__ ws, unsigned short* __restrict__ out) {
  __shared__ __attribute__((aligned(1024))) char smem[LDS_BYTES];
  LAS char* lds = (LAS char*)smem;
  const unsigned lbase = (unsigned)(size_t)lds;
  const int tid = threadIdx.x, lane = tid & 63, wave = __builtin_amdgcn_readfirstlane(tid >> 6);
  const int wg = blockIdx.x, x8 = wg & 7;
  const int grp = (wg >> 3) * 2 + ((x8 >> 1) & 1), mem = (x8 >> 2) * 2 + (x8 & 1);
  const int r32 = lane & 31, hh = lane >> 5;
  const bool tree = (mem == 0) && (wave == 0);
  const bool leader = (mem == 1) && (wave == 0);
  unsigned* cw = (unsigned*)(ws + WS_CTL + grp * 256);
  const unsigned vz = 0;
  ctl_add_ret(cw, vz, 0u);

  unsigned ko[8], vo[2];
#pragma unroll
  for (int i = 0; i < 8; ++i) ko[i] = hh * ROWB + ((r32 ^ ((2 * i + hh) & 15)) << 4);
#pragma unroll
  for (int i = 0; i < 2; ++i) vo[i] = hh * ROWB + ((r32 ^ (((2 * i + hh) & 3) << 2)) << 4);
  const unsigned ring = wave * RINGB;
  auto blk_k = [&](const char* src, unsigned dst) {
    dma4(src, lbase + dst, ko[0], ko[1], ko[2], ko[3]);
    dma4(src + 4096, lbase + dst + 4096, ko[4], ko[5], ko[6], ko[7]);
  };
  auto blk_v = [&](const char* src, unsigned dst) {
    dma4(src, lbase + dst, vo[0], vo[1], vo[0], vo[1]);
    dma4(src + 4096, lbase + dst + 4096, vo[0], vo[1], vo[0], vo[1]);
  };
  const char* Kg = K + (size_t)grp * CTX * ROWB;
  const char* Vg = V + (size_t)grp * CTX * ROWB;
  auto tile_k = [&](int off, unsigned dst) { blk_k(Kg + off, dst); blk_k(Kg + off + 2 * BLKB, dst + BLKB); };
  auto tile_v = [&](int off, unsigned dst) { blk_v(Vg + off, dst); blk_v(Vg + off + 2 * BLKB, dst + BLKB); };
  const int cls = wave & 1, i8 = mem * 2 + (wave >> 1);
  const unsigned inc = cls ? W_T1 : W_T0;

  {
    const unsigned x = (wave & 1) << 7;
    dma4(Q + (size_t)grp * TILEB + wave * 4096, lbase + Q_OFF + wave * 4096, ko[0] ^ x, ko[1] ^ x, ko[2] ^ x, ko[3] ^ x);
  }
  tile_k(tile_off(i8, cls), ring);
  const unsigned krd = r32 * ROWB + ((hh ^ (r32 & 15)) << 4);
  const int q4 = (lane & 15) >> 2;
  const unsigned vrd = (4 * hh + q4) * ROWB + (q4 << 6) + ((lane >> 4) & 1) * 32 + (lane & 3) * 8;
  acc_zero();
  WAIT_VM(16);
  const unsigned w0 = ctl_result();
  if (leader) { ctl_and(cw, vz, W_EPOCH_MASK); ctl_add(cw, vz, W_EPOCH); }
  BARRIER();
  bf16x8 q[16];
#pragma unroll
  for (int st = 0; st < 16; ++st) q[st] = row_frag(lds, Q_OFF + krd, st);
  BARRIER();
  if (tree) {
    blk_k(KT + (size_t)grp * BLKB, TREE_OFF); blk_v(VT + (size_t)grp * BLKB, TREE_OFF + BLKB);
    const unsigned mo = (r32 & 15) * (NT * 2) + 8 * hh;
    asm volatile("s_nop 4\n\tglobal_load_dwordx2 a[248:249], %0, %1\n\tglobal_load_dwordx2 a[250:251], %0, %1 offset:16"
                 :: "v"(mo), "s"(MK + (grp >> 3) * (NT * NT * 2)) : "memory", "a248", "a249", "a250", "a251");
  }
  tile_k(tile_off(i8 + 8, cls), ring + TILEB);

  float zm = -__builtin_inff(), zl = 0.f, om = -__builtin_inff();

  if (tree) {
    WAIT_VM(16);
    unsigned mk[4];
    asm volatile("v_accvgpr_read_b32 %0, a248\n\tv_accvgpr_read_b32 %1, a249\n\tv_accvgpr_read_b32 %2, a250\n\tv_accvgpr_read_b32 %3, a251"
                 : "=v"(mk[0]), "=v"(mk[1]), "=v"(mk[2]), "=v"(mk[3]) :: "memory");
    bf16x8 f[16];
#pragma unroll
    for (int st = 0; st < 16; ++st) f[st] = row_frag(lds, TREE_OFF + (r32 & 15) * ROWB + ((hh ^ (r32 & 15)) << 4), st);
    f32x16 s = mfma_qk(f, q);
#pragma unroll
    for (int r = 0; r < 8; ++r) {
      const float mv = __uint_as_float((r & 1) ? (mk[r >> 1] & 0xffff0000u) : (mk[r >> 1] << 16));
      s[r] = fmaf(1.0f - mv, MASKRAW, s[r]);
      s[r + 8] = -__builtin_inff();
    }
    u32x4 p0, p1;
    softmax_tile(s, zm, zl, p0, p1);
    om = zm;
#pragma unroll
    for (int c = 0; c < 8; ++c) f[c] = col_frag(lds, TREE_OFF + BLKB + vrd, c, 0);
    mfma_pv(f, p0);
  }

  int cur = 0, nx1 = 1;
  unsigned slot = ring;
  int offs = lane ? tile_off(i8 + 8, cls) : tile_off(i8, cls);
  int nk = 2, iv = -1, nst = 2;
  int held = -2;
  int pend = 0;
  bool ready = leader;
  unsigned chk = leader ? 0u : W_CHK;
  const unsigned mt = MT_OFF + wave * (CAP * 128) + r32 * 4;

  auto advance = [&]() {
    if (nst < NSTAT) { held = tile_off(i8 + 8 * nst, cls); ++nst; }
    if (iv >= nk) {
      if (held == -2) {
#pragma unroll 1
        for (int i = 0; i < SPIN_MAX && !ready; ++i) {
          ctl_add_ret(cw, vz, 0u); WAIT_VM(0);
          ready = ctl_result() != w0;
          if (!ready) __builtin_amdgcn_s_sleep(16);
        }
        ready = true;
        ctl_add_ret(cw, vz, inc + chk); chk = 0u; WAIT_VM(0);
        held = ticket(ctl_result(), cls);
      }
      if (held >= 0) { nk = 0; iv = -1; }
    }
    int nx2 = -1, off = 0;
    if (iv < 0 && held >= 0) {
      off = held & ~1;
      offs = lane == nk ? off : offs;
      nx2 = nk; ++nk;
      if ((held & 1) || nk == CAP) iv = 0;
      held = -2;
    } else {
      if (iv < 0) iv = 0;
      if (iv < nk) { off = __builtin_amdgcn_readlane(offs, iv); nx2 = 32 + iv; ++iv; }
    }
    if (!ready) { ctl_add_ret(cw, vz, 0u); pend = 1; }
    else if (held == -2 && nst >= NSTAT && (iv < 0 || iv >= nk)) {
      ctl_add_ret(cw, vz, inc + chk); chk = 0u; pend = 2;
    }
    WAIT_LGKM0();
    if (nx2 >= 32) tile_v(off, slot); else if (nx2 >= 0) tile_k(off, slot);
    cur = nx1; nx1 = nx2; slot ^= TILEB;
  };

  while (cur >= 0) {
    if (nx1 >= 0) WAIT_VM(16); else WAIT_VM(0);
    const int j = cur & 31;
    const bool isk = cur < 32;
    bf16x8 f[16];
    if (isk) {
#pragma unroll
      for (int st = 0; st < 16; ++st) f[st] = row_frag(lds, slot + krd, st);
    } else {
#pragma unroll
      for (int c = 0; c < 8; ++c) { f[c] = col_frag(lds, slot + vrd, c, 0); f[8 + c] = col_frag(lds, slot + vrd, c, BLKB); }
    }
    if (pend == 2) held = ticket(ctl_result(), cls);
    else if (pend == 1) ready = ctl_result() != w0;
    pend = 0;
    if (leader && nst < NSTAT) ctl_add(cw, vz, W_EPOCH);
    advance();
    u32x4 p0, p1;
    if (isk) {
      f32x16 s = mfma_qk(f, q);
      softmax_tile(s, zm, zl, p0, p1);
      park_put(j, p0, p1);
      *(LAS float*)(lds + mt + j * 128) = zm;
    } else {
      const float tm = *(const LAS float*)(lds + mt + j * 128);
      park_get(j, p0, p1);
      if (!__all(tm == om)) {
        if (!__all(om == -__builtin_inff())) acc_scale(__builtin_amdgcn_exp2f((om - tm) * SCL2));
        om = tm;
      }
      mfma_pv(f, p0);
      mfma_pv(f + 8, p1);
    }
  }
  ctl_add_ret(cw, vz, W_ARR);

  zl = xhalf_sum(zl);
  {
    const unsigned img = lbase + ring + r32 * (HD * 4) + ((hh ^ (r32 & 7)) << 4);
    acc_store(img, img ^ 32, img ^ 64, img ^ 96);
    if (hh == 0) *(LAS f32x2*)(lds + ML_OFF + (wave * 32 + r32) * 8) = f32x2{zm, zl};
  }
  BARRIER();
  float Mr, Lr, fw[4];
  {
    f32x2 ml[4];
#pragma unroll
    for (int w = 0; w < 4; ++w) ml[w] = *(const LAS f32x2*)(lds + ML_OFF + (w * 32 + wave + 4 * (lane & 7)) * 8);
    Mr = fmaxf(fmaxf(ml[0][0], ml[1][0]), fmaxf(ml[2][0], ml[3][0]));
    Lr = 0.f;
#pragma unroll
    for (int w = 0; w < 4; ++w) { fw[w] = __builtin_amdgcn_exp2f((ml[w][0] - Mr) * SCL2); Lr = fmaf(fw[w], ml[w][1], Lr); }
  }
  f32x4 acc[8];
#pragma unroll
  for (int k = 0; k < 8; ++k) {
    const int row = wave + 4 * k;
    const unsigned a = row * (HD * 4) + ((lane ^ (row & 7)) << 4);
    acc[k] = lane_bcast(fw[0], k) * *(const LAS f32x4*)(lds + a);
#pragma unroll
    for (int w = 1; w < 4; ++w) acc[k] += lane_bcast(fw[w], k) * *(const LAS f32x4*)(lds + w * RINGB + a);
  }
  WAIT_VM(0);
  const unsigned war = ctl_result();
  if (lane == 0) *(LAS unsigned*)(lds + AR_OFF + wave * 4) = war;
  BARRIER();
  const u32x4 ar = *(const LAS u32x4*)(lds + AR_OFF);
  unsigned last = 0;
#pragma unroll
  for (int w = 0; w < 4; ++w) if (((ar[w] >> 22) & 31u) == 15u) last = ar[w];
  last = __builtin_amdgcn_readfirstlane(last);

  const __amdgpu_buffer_rsrc_t rs = __builtin_amdgcn_make_buffer_rsrc(ws, 0, (int)WS_END, 0x00020000);
  const int ml_off = (int)WS_PML + (grp * NSPL * 32 + wave + 4 * (lane & 7)) * 8;
  const int po_off = (int)WS_PO + (grp * NSPL * 32 + wave) * (HD * 4) + lane * 16;
  if (!last) {
#pragma unroll
    for (int k = 0; k < 8; ++k) __builtin_amdgcn_raw_buffer_store_b128(__builtin_bit_cast(u32x4, acc[k]), rs, po_off + k * 4096, mem * RINGB, 16);
    if (lane < 8) __builtin_amdgcn_raw_buffer_store_b64(__builtin_bit_cast(u32x2, f32x2{Mr, Lr}), rs, ml_off, mem * 256, 16);
    WAIT_VM(0);
    BARRIER();
    if (wave == 0) ctl_add(cw, vz, W_PUB);
    if (leader) {
      unsigned w = war;
      for (int i = 0; i < SPIN_MAX && ((w >> 18) & 15u) != 15u; ++i) {
        __builtin_amdgcn_s_sleep(16);
        ctl_add_ret(cw, vz, W_EPOCH); WAIT_VM(0);
        w = ctl_result();
      }
    }
  } else {
    if (((last >> 27) & 3u) != 3u) {
      if (wave == 0) {
        unsigned w = last;
        for (int i = 0; i < SPIN_MAX && ((w >> 27) & 3u) != 3u; ++i) {
          ctl_add_ret(cw, vz, 0u); WAIT_VM(0);
          w = ctl_result();
        }
      }
      BARRIER();
    }
    f32x2 oml[3]; f32x4 oa[3][8];
#pragma unroll
    for (int jj = 0; jj < 3; ++jj) {
      const int s = (mem + 1 + jj) & 3;
      oml[jj] = __builtin_bit_cast(f32x2, __builtin_amdgcn_raw_buffer_load_b64(rs, ml_off, s * 256, 16));
#pragma unroll
      for (int k = 0; k < 8; ++k) oa[jj][k] = __builtin_bit_cast(f32x4, __builtin_amdgcn_raw_buffer_load_b128(rs, po_off + k * 4096, s * RINGB, 16));
    }
    const float M = fmaxf(fmaxf(Mr, oml[0][0]), fmaxf(oml[1][0], oml[2][0]));
    float f[4]; float L = 0.f;
    f[3] = __builtin_amdgcn_exp2f((Mr - M) * SCL2); L = f[3] * Lr;
#pragma unroll
    for (int jj = 0; jj < 3; ++jj) { f[jj] = __builtin_amdgcn_exp2f((oml[jj][0] - M) * SCL2); L = fmaf(f[jj], oml[jj][1], L); }
    const float inv = 1.0f / L;
#pragma unroll
    for (int jj = 0; jj < 4; ++jj) f[jj] *= inv;
#pragma unroll
    for (int k = 0; k < 8; ++k) {
      f32x4 r = acc[k] * lane_bcast(f[3], k);
#pragma unroll
      for (int jj = 0; jj < 3; ++jj) r += oa[jj][k] * lane_bcast(f[jj], k);
      *(u32x2*)(out + (size_t)(grp * 32 + wave + 4 * k) * HD + lane * 4) = u32x2{pack2(r[0], r[1]), pack2(r[2], r[3])};
    }
  }
}

extern "C" void kernel_launch(void* const* d_in, const int* in_sizes, int n_in, void* d_out, int out_size, void* d_ws, size_t ws_size,
                              hipStream_t stream) {
  tree_attn<<<NGRP * NSPL, 256, 0, stream>>>((const char*)d_in[0], (const char*)d_in[1], (const char*)d_in[2], (const char*)d_in[3],
                                             (const char*)d_in[4], (const char*)d_in[5], (char*)d_ws, (unsigned short*)d_out);
}
```

```cpp
#define DBG_STAMPS
#include <hip/hip_runtime.h>
#include <hip/hip_bf16.h>

#define LAS __attribute__((address_space(3)))
typedef __bf16 bf16x8 __attribute__((ext_vector_type(8)));
typedef __bf16 bf16x4 __attribute__((ext_vector_type(4)));
typedef __bf16 bf16x2 __attribute__((ext_vector_type(2)));
typedef float f32x16 __attribute__((ext_vector_type(16)));
typedef float f32x4 __attribute__((ext_vector_type(4)));
typedef float f32x2 __attribute__((ext_vector_type(2)));
typedef unsigned u32x4 __attribute__((ext_vector_type(4)));
typedef unsigned u32x2 __attribute__((ext_vector_type(2)));

constexpr int NB = 8, HKV = 8, NT = 16, CTX = 8192, HD = 256;
constexpr int NGRP = NB * HKV;
constexpr int NSPL = 4;
constexpr int ROWB = HD * 2;
constexpr int BLKB = 16 * ROWB;
constexpr int TILEB = 2 * BLKB;
constexpr int RINGB = 2 * TILEB;
constexpr int TREE_OFF = 4 * RINGB;
constexpr int Q_OFF = TREE_OFF + 2 * BLKB;
constexpr int LDS_BYTES = Q_OFF + TILEB;
constexpr int CAP = 20;
constexpr int MT_OFF = Q_OFF;
constexpr int ML_OFF = MT_OFF + 4 * CAP * 128;
constexpr int AR_OFF = ML_OFF + 4 * 32 * 8;
static_assert(LDS_BYTES <= 160 * 1024 && RINGB == 32 * HD * 4 && AR_OFF + 16 <= LDS_BYTES, "LDS budget");
constexpr float SCL2 = 0.0625f * 1.4426950408889634f;
constexpr float MASKRAW = -1.0e9f * 16.0f;
constexpr float DEFER = 8.0f / SCL2;

constexpr int NSUP = CTX / 64;
constexpr int NSTAT = 4;
constexpr int USTAT = 8 * NSTAT;
constexpr int NDYN = NSUP - USTAT;
#define RBOUND 132
constexpr unsigned W_T0 = 1u, W_T1 = 1u << 9, W_CHK = 1u << 18, W_ARR = 1u << 22, W_PUB = 1u << 27, W_EPOCH = 1u << 29;
constexpr unsigned W_EPOCH_MASK = 0xE0000000u;
constexpr int SPIN_MAX = 1 << 16;

constexpr size_t WS_CTL = 0, WS_PML = 65536, WS_PO = WS_PML + (size_t)NGRP * NSPL * 32 * 8;
constexpr size_t WS_END = WS_PO + (size_t)NGRP * NSPL * 32 * HD * 4;

__device__ __forceinline__ void dma4(const char* src, unsigned lds, unsigned v0, unsigned v1, unsigned v2, unsigned v3) {
  unsigned keep;
  asm volatile("s_mov_b32 %0, m0\n\ts_mov_b32 m0, %2\n\ts_nop 2\n\t"
               "global_load_lds_dwordx4 %3, %1 nt\n\t"
               "global_load_lds_dwordx4 %4, %1 offset:1024 nt\n\t"
               "global_load_lds_dwordx4 %5, %1 offset:2048 nt\n\t"
               "global_load_lds_dwordx4 %6, %1 offset:3072 nt\n\t"
               "s_mov_b32 m0, %0"
               : "=&s"(keep) : "s"(src), "s"(lds), "v"(v0), "v"(v1), "v"(v2), "v"(v3) : "memory");
}
#define WAIT_VM(n) asm volatile("s_waitcnt vmcnt(" #n ")" ::: "memory")
#define WAIT_LGKM0() asm volatile("s_waitcnt lgkmcnt(0)" ::: "memory")
#define BARRIER() do { WAIT_LGKM0(); __builtin_amdgcn_s_barrier(); asm volatile("" ::: "memory"); } while (0)

__device__ __forceinline__ void ctl_add_ret(unsigned* w, unsigned zero, unsigned inc) {
  unsigned long long ex;
  asm volatile("v_accvgpr_write_b32 a254, %3\n\ts_mov_b64 %0, exec\n\ts_mov_b64 exec, 1\n\ts_nop 2\n\t"
               "global_atomic_add a255, %2, a254, %1 sc0\n\ts_mov_b64 exec, %0\n\ts_nop 1"
               : "=&s"(ex) : "s"(w), "v"(zero), "v"(inc) : "memory", "a254", "a255");
}
__device__ __forceinline__ unsigned ctl_result() {
  unsigned t, r;
  asm volatile("v_accvgpr_read_b32 %0, a255\n\ts_nop 1\n\tv_readfirstlane_b32 %1, %0\n\ts_nop 4" : "=&v"(t), "=s"(r) :: "memory");
  return r;
}
__device__ __forceinline__ void ctl_add(unsigned* w, unsigned zero, unsigned inc) {
  unsigned long long ex;
  asm volatile("s_mov_b64 %0, exec\n\ts_mov_b64 exec, 1\n\ts_nop 2\n\tglobal_atomic_add %2, %3, %1\n\ts_mov_b64 exec, %0\n\ts_nop 1"
               : "=&s"(ex) : "s"(w), "v"(zero), "v"(inc) : "memory");
}
__device__ __forceinline__ void ctl_and(unsigned* w, unsigned zero, unsigned m) {
  unsigned long long ex;
  asm volatile("s_mov_b64 %0, exec\n\ts_mov_b64 exec, 1\n\ts_nop 2\n\tglobal_atomic_and %2, %3, %1\n\ts_mov_b64 exec, %0\n\ts_nop 1"
               : "=&s"(ex) : "s"(w), "v"(zero), "v"(m) : "memory");
}
#define PARK_HEAD "s_getpc_b64 vcc\n\t.Lp0_%=:\n\ts_add_u32 vcc_lo, vcc_lo, %8\n\ts_addc_u32 vcc_hi, vcc_hi, 0\n\t" \
                  "s_add_u32 vcc_lo, vcc_lo, .Lp1_%=-.Lp0_%=\n\ts_addc_u32 vcc_hi, vcc_hi, 0\n\ts_setpc_b64 vcc\n\t.Lp1_%=:\n\t.set .Lpi_%=, 128\n\t.rept 15\n\t"
#define PARK_MID "s_branch .Lp2_%=\n\t.set .Lpi_%=, .Lpi_%=+8\n\t.endr\n\t.set .Lpi_%=, 216\n\t.rept 5\n\t"
#define PARK_TAIL "s_branch .Lp2_%=\n\t.set .Lpi_%=, .Lpi_%=+8\n\t.endr\n\t.Lp2_%=:\n\ts_nop 1"
#define PARK_PUT(op, f) op " " f "[.Lpi_%=], %0\n\t" op " " f "[.Lpi_%=+1], %1\n\t" op " " f "[.Lpi_%=+2], %2\n\t" op " " f "[.Lpi_%=+3], %3\n\t" \
                        op " " f "[.Lpi_%=+4], %4\n\t" op " " f "[.Lpi_%=+5], %5\n\t" op " " f "[.Lpi_%=+6], %6\n\t" op " " f "[.Lpi_%=+7], %7\n\t"
#define PARK_GET(op, f) op " %0, " f "[.Lpi_%=]\n\t" op " %1, " f "[.Lpi_%=+1]\n\t" op " %2, " f "[.Lpi_%=+2]\n\t" op " %3, " f "[.Lpi_%=+3]\n\t" \
                        op " %4, " f "[.Lpi_%=+4]\n\t" op " %5, " f "[.Lpi_%=+5]\n\t" op " %6, " f "[.Lpi_%=+6]\n\t" op " %7, " f "[.Lpi_%=+7]\n\t"
__device__ __forceinline__ void park_put(int j, const u32x4& p0, const u32x4& p1) {
  asm volatile(PARK_HEAD PARK_PUT("v_accvgpr_write_b32", "a") PARK_MID PARK_PUT("v_mov_b32_e64", "v") PARK_TAIL
               :: "v"(p0[0]), "v"(p0[1]), "v"(p0[2]), "v"(p0[3]), "v"(p1[0]), "v"(p1[1]), "v"(p1[2]), "v"(p1[3]), "s"(j * 68)
               : "memory", "vcc", "scc", "v216", "v255");
}
__device__ __forceinline__ void park_get(int j, u32x4& p0, u32x4& p1) {
  asm volatile(PARK_HEAD PARK_GET("v_accvgpr_read_b32", "a") PARK_MID PARK_GET("v_mov_b32_e64", "v") PARK_TAIL
               : "=v"(p0[0]), "=v"(p0[1]), "=v"(p0[2]), "=v"(p0[3]), "=v"(p1[0]), "=v"(p1[1]), "=v"(p1[2]), "=v"(p1[3]) : "s"(j * 68)
               : "memory", "vcc", "scc");
}
static_assert(CAP == 20, "park_put / park_get hold 15 + 5 blocks");
__device__ __forceinline__ void acc_zero() {
  asm volatile(".set .Lz_%=, 0\n\t.rept 128\n\tv_accvgpr_write_b32 a[.Lz_%=], 0\n\t.set .Lz_%=, .Lz_%=+1\n\t.endr" ::: "memory", "a0", "a127", "a247");
}
__device__ __forceinline__ void acc_scale(float alpha) {
  float t0, t1, t2, t3;
  asm volatile("s_nop 15\n\t.set .Ls_%=, 0\n\t.rept 32\n\t"
               "v_accvgpr_read_b32 %0, a[.Ls_%=]\n\tv_accvgpr_read_b32 %1, a[.Ls_%=+1]\n\tv_accvgpr_read_b32 %2, a[.Ls_%=+2]\n\tv_accvgpr_read_b32 %3, a[.Ls_%=+3]\n\t"
               "v_mul_f32 %0, %0, %4\n\tv_mul_f32 %1, %1, %4\n\tv_mul_f32 %2, %2, %4\n\tv_mul_f32 %3, %3, %4\n\t"
               "v_accvgpr_write_b32 a[.Ls_%=], %0\n\tv_accvgpr_write_b32 a[.Ls_%=+1], %1\n\tv_accvgpr_write_b32 a[.Ls_%=+2], %2\n\tv_accvgpr_write_b32 a[.Ls_%=+3], %3\n\t"
               ".set .Ls_%=, .Ls_%=+4\n\t.endr\n\ts_nop 1"
               : "=&v"(t0), "=&v"(t1), "=&v"(t2), "=&v"(t3) : "v"(alpha) : "memory");
}
__device__ __forceinline__ f32x16 mfma_qk(const bf16x8 (&f)[16], const bf16x8 (&q)[16]) {
  f32x16 s;
#define M_(a, b) "v_mfma_f32_32x32x16_bf16 %0, %" #a ", %" #b ", %0\n\t"
  asm volatile("s_nop 1\n\tv_mfma_f32_32x32x16_bf16 %0, %1, %9, 0\n\t" M_(2, 10) M_(3, 11) M_(4, 12) M_(5, 13) M_(6, 14) M_(7, 15) M_(8, 16)
               : "=&v"(s) : "v"(f[0]), "v"(f[1]), "v"(f[2]), "v"(f[3]), "v"(f[4]), "v"(f[5]), "v"(f[6]), "v"(f[7]),
                 "v"(q[0]), "v"(q[1]), "v"(q[2]), "v"(q[3]), "v"(q[4]), "v"(q[5]), "v"(q[6]), "v"(q[7]));
  asm volatile(M_(1, 9) M_(2, 10) M_(3, 11) M_(4, 12) M_(5, 13) M_(6, 14) M_(7, 15) M_(8, 16) "s_nop 15"
               : "+v"(s) : "v"(f[8]), "v"(f[9]), "v"(f[10]), "v"(f[11]), "v"(f[12]), "v"(f[13]), "v"(f[14]), "v"(f[15]),
                 "v"(q[8]), "v"(q[9]), "v"(q[10]), "v"(q[11]), "v"(q[12]), "v"(q[13]), "v"(q[14]), "v"(q[15]));
#undef M_
  return s;
}
__device__ __forceinline__ void mfma_pv(const bf16x8* f, const u32x4& p) {
#define M_(a, lo, hi) "v_mfma_f32_32x32x16_bf16 a[" #lo ":" #hi "], %" #a ", %8, a[" #lo ":" #hi "]\n\t"
  asm volatile("s_nop 1\n\t" M_(0, 0, 15) M_(1, 16, 31) M_(2, 32, 47) M_(3, 48, 63) M_(4, 64, 79) M_(5, 80, 95) M_(6, 96, 111) M_(7, 112, 127)
               :: "v"(f[0]), "v"(f[1]), "v"(f[2]), "v"(f[3]), "v"(f[4]), "v"(f[5]), "v"(f[6]), "v"(f[7]), "v"(p) : "memory");
#undef M_
}
__device__ __forceinline__ void acc_store(unsigned a0, unsigned a1, unsigned a2, unsigned a3) {
  asm volatile("s_nop 15\n\t.set .Lw_%=, 0\n\t.rept 8\n\t"
               "ds_write_b128 %0, a[.Lw_%=*16:.Lw_%=*16+3] offset:.Lw_%=*128\n\tds_write_b128 %1, a[.Lw_%=*16+4:.Lw_%=*16+7] offset:.Lw_%=*128\n\t"
               "ds_write_b128 %2, a[.Lw_%=*16+8:.Lw_%=*16+11] offset:.Lw_%=*128\n\tds_write_b128 %3, a[.Lw_%=*16+12:.Lw_%=*16+15] offset:.Lw_%=*128\n\t"
               ".set .Lw_%=, .Lw_%=+1\n\t.endr"
               :: "v"(a0), "v"(a1), "v"(a2), "v"(a3) : "memory");
}

__device__ __forceinline__ float xhalf_max(float x) {
  auto r = __builtin_amdgcn_permlane32_swap(__float_as_uint(x), __float_as_uint(x), false, false);
  return fmaxf(__uint_as_float(r[0]), __uint_as_float(r[1]));
}
__device__ __forceinline__ float xhalf_sum(float x) {
  auto r = __builtin_amdgcn_permlane32_swap(__float_as_uint(x), __float_as_uint(x), false, false);
  return __uint_as_float(r[0]) + __uint_as_float(r[1]);
}
__device__ __forceinline__ unsigned pack2(float a, float b) {
  return __builtin_bit_cast(unsigned, __builtin_convertvector((f32x2){a, b}, bf16x2));
}
__device__ __forceinline__ float lane_bcast(float x, int l) { return __uint_as_float(__builtin_amdgcn_readlane(__float_as_uint(x), l)); }

__device__ __forceinline__ bf16x8 row_frag(LAS char* lds, unsigned rd, int st) {
  return *(const LAS bf16x8*)(lds + ((rd ^ ((st & 7) << 5)) + (st >> 3) * 256));
}
__device__ __forceinline__ void softmax_tile(f32x16& s, float& m, float& l, u32x4& p0, u32x4& p1) {
  float mx = s[0];
#pragma unroll
  for (int r = 1; r < 16; ++r) mx = fmaxf(mx, s[r]);
  mx = xhalf_max(mx);
  if (!__all(mx <= m + DEFER)) {
    const float mn = fmaxf(m, mx);
    l *= __builtin_amdgcn_exp2f((m - mn) * SCL2);
    m = mn;
  }
  const float nb = -m * SCL2;
  float ps = 0.f;
#pragma unroll
  for (int r = 0; r < 16; ++r) { s[r] = __builtin_amdgcn_exp2f(fmaf(s[r], SCL2, nb)); ps += s[r]; }
  l += ps;
#pragma unroll
  for (int i = 0; i < 4; ++i) { p0[i] = pack2(s[2 * i], s[2 * i + 1]); p1[i] = pack2(s[8 + 2 * i], s[9 + 2 * i]); }
}
__device__ __forceinline__ bf16x8 col_frag(LAS char* lds, unsigned vrd, int c, int koff) {
  const unsigned a = (vrd ^ ((c & 3) << 6)) + (c >> 2) * 256 + koff;
  const bf16x4 lo = __builtin_amdgcn_ds_read_tr16_b64_v4bf16((LAS bf16x4*)(lds + a));
  const bf16x4 hi = __builtin_amdgcn_ds_read_tr16_b64_v4bf16((LAS bf16x4*)(lds + a + 8 * ROWB));
  return __builtin_shufflevector(lo, hi, 0, 1, 2, 3, 4, 5, 6, 7);
}
__device__ __forceinline__ int tile_off(int u, int c) { return (4 * u + (c ^ (u >> 6))) * BLKB; }
__device__ __forceinline__ int ticket(unsigned w, int c) {
  const int n0 = w & 0x1ff, n1 = (w >> 9) & 0x1ff, tot = n0 + n1;
  const int mine = c ? n1 : n0;
  const bool own = mine < NDYN;
  const int off = tile_off(USTAT + (own ? mine : 2 * NDYN - 1 - mine), own ? c : c ^ 1);
  return tot < 2 * NDYN ? off + (tot >= RBOUND) : -1;
}

__global__ void __launch_bounds__(256, 1)
tree_attn(const char* __restrict__ Q, const char* __restrict__ K, const char* __restrict__ V, const char* __restrict__ KT,
          const char* __restrict__ VT, const char* __restrict__ MK, char* __restrict__ ws, unsigned short* __restrict__ out) {
  __shared__ __attribute__((aligned(1024))) char smem[LDS_BYTES];
  LAS char* lds = (LAS char*)smem;
  const unsigned lbase = (unsigned)(size_t)lds;
  const int tid = threadIdx.x, lane = tid & 63, wave = __builtin_amdgcn_readfirstlane(tid >> 6);
  const int wg = blockIdx.x, x8 = wg & 7;
  const int grp = (wg >> 3) * 2 + ((x8 >> 1) & 1), mem = (x8 >> 2) * 2 + (x8 & 1);
  const int r32 = lane & 31, hh = lane >> 5;
  const bool tree = (mem == 0) && (wave == 0);
  const bool leader = (mem == 1) && (wave == 0);
  unsigned long long ts0 = __builtin_amdgcn_s_memrealtime(), ts1 = 0, ts2 = 0; int ntl = 0, nslow = 0, npoll = 0;
  unsigned* cw = (unsigned*)(ws + WS_CTL + grp * 256);
  const unsigned vz = 0;
  ctl_add_ret(cw, vz, 0u);

  unsigned ko[8], vo[2];
#pragma unroll
  for (int i = 0; i < 8; ++i) ko[i] = hh * ROWB + ((r32 ^ ((2 * i + hh) & 15)) << 4);
#pragma unroll
  for (int i = 0; i < 2; ++i) vo[i] = hh * ROWB + ((r32 ^ (((2 * i + hh) & 3) << 2)) << 4);
  const unsigned ring = wave * RINGB;
  auto blk_k = [&](const char* src, unsigned dst) {
    dma4(src, lbase + dst, ko[0], ko[1], ko[2], ko[3]);
    dma4(src + 4096, lbase + dst + 4096, ko[4], ko[5], ko[6], ko[7]);
  };
  auto blk_v = [&](const char* src, unsigned dst) {
    dma4(src, lbase + dst, vo[0], vo[1], vo[0], vo[1]);
    dma4(src + 4096, lbase + dst + 4096, vo[0], vo[1], vo[0], vo[1]);
  };
  const char* Kg = K + (size_t)grp * CTX * ROWB;
  const char* Vg = V + (size_t)grp * CTX * ROWB;
  auto tile_k = [&](int off, unsigned dst) { blk_k(Kg + off, dst); blk_k(Kg + off + 2 * BLKB, dst + BLKB); };
  auto tile_v = [&](int off, unsigned dst) { blk_v(Vg + off, dst); blk_v(Vg + off + 2 * BLKB, dst + BLKB); };
  const int cls = wave & 1, i8 = mem * 2 + (wave >> 1);
  const unsigned inc = cls ? W_T1 : W_T0;

  {
    const unsigned x = (wave & 1) << 7;
    dma4(Q + (size_t)grp * TILEB + wave * 4096, lbase + Q_OFF + wave * 4096, ko[0] ^ x, ko[1] ^ x, ko[2] ^ x, ko[3] ^ x);
  }
  tile_k(tile_off(i8, cls), ring);
  const unsigned krd = r32 * ROWB + ((hh ^ (r32 & 15)) << 4);
  const int q4 = (lane & 15) >> 2;
  const unsigned vrd = (4 * hh + q4) * ROWB + (q4 << 6) + ((lane >> 4) & 1) * 32 + (lane & 3) * 8;
  acc_zero();
  WAIT_VM(16);
  const unsigned w0 = ctl_result();
  if (leader) { ctl_and(cw, vz, W_EPOCH_MASK); ctl_add(cw, vz, W_EPOCH); }
  BARRIER();
  bf16x8 q[16];
#pragma unroll
  for (int st = 0; st < 16; ++st) q[st] = row_frag(lds, Q_OFF + krd, st);
  BARRIER();
  if (tree) {
    blk_k(KT + (size_t)grp * BLKB, TREE_OFF); blk_v(VT + (size_t)grp * BLKB, TREE_OFF + BLKB);
    const unsigned mo = (r32 & 15) * (NT * 2) + 8 * hh;
    asm volatile("s_nop 4\n\tglobal_load_dwordx2 a[248:249], %0, %1\n\tglobal_load_dwordx2 a[250:251], %0, %1 offset:16"
                 :: "v"(mo), "s"(MK + (grp >> 3) * (NT * NT * 2)) : "memory", "a248", "a249", "a250", "a251");
  }
  tile_k(tile_off(i8 + 8, cls), ring + TILEB);

  float zm = -__builtin_inff(), zl = 0.f, om = -__builtin_inff();

  if (tree) {
    WAIT_VM(16);
    unsigned mk[4];
    asm volatile("v_accvgpr_read_b32 %0, a248\n\tv_accvgpr_read_b32 %1, a249\n\tv_accvgpr_read_b32 %2, a250\n\tv_accvgpr_read_b32 %3, a251"
                 : "=v"(mk[0]), "=v"(mk[1]), "=v"(mk[2]), "=v"(mk[3]) :: "memory");
    bf16x8 f[16];
#pragma unroll
    for (int st = 0; st < 16; ++st) f[st] = row_frag(lds, TREE_OFF + (r32 & 15) * ROWB + ((hh ^ (r32 & 15)) << 4), st);
    f32x16 s = mfma_qk(f, q);
#pragma unroll
    for (int r = 0; r < 8; ++r) {
      const float mv = __uint_as_float((r & 1) ? (mk[r >> 1] & 0xffff0000u) : (mk[r >> 1] << 16));
      s[r] = fmaf(1.0f - mv, MASKRAW, s[r]);
      s[r + 8] = -__builtin_inff();
    }
    u32x4 p0, p1;
    softmax_tile(s, zm, zl, p0, p1);
    om = zm;
#pragma unroll
    for (int c = 0; c < 8; ++c) f[c] = col_frag(lds, TREE_OFF + BLKB + vrd, c, 0);
    mfma_pv(f, p0);
  }

  int cur = 0, nx1 = 1;
  unsigned slot = ring;
  int offs = lane ? tile_off(i8 + 8, cls) : tile_off(i8, cls);
  int nk = 2, iv = -1, nst = 2;
  int held = -2;
  int pend = 0;
  bool ready = leader;
  unsigned chk = leader ? 0u : W_CHK;
  const unsigned mt = MT_OFF + wave * (CAP * 128) + r32 * 4;

  auto advance = [&]() {
    if (nst < NSTAT) { held = tile_off(i8 + 8 * nst, cls); ++nst; }
    if (iv >= nk) {
      if (held == -2) {
#pragma unroll 1
        for (int i = 0; i < SPIN_MAX && !ready; ++i) {
          ctl_add_ret(cw, vz, 0u); WAIT_VM(0);
          ready = ctl_result() != w0;
          if (!ready) __builtin_amdgcn_s_sleep(16);
        }
        ready = true;
        ctl_add_ret(cw, vz, inc + chk); chk = 0u; WAIT_VM(0);
        held = ticket(ctl_result(), cls);
        ++nslow;
      }
      if (held >= 0) { nk = 0; iv = -1; }
    }
    int nx2 = -1, off = 0;
    if (iv < 0 && held >= 0) {
      off = held & ~1;
      offs = lane == nk ? off : offs;
      nx2 = nk; ++nk;
      if ((held & 1) || nk == CAP) iv = 0;
      held = -2;
    } else {
      if (iv < 0) iv = 0;
      if (iv < nk) { off = __builtin_amdgcn_readlane(offs, iv); nx2 = 32 + iv; ++iv; }
    }
    if (!ready) { ctl_add_ret(cw, vz, 0u); pend = 1; }
    else if (held == -2 && nst >= NSTAT && (iv < 0 || iv >= nk)) {
      ctl_add_ret(cw, vz, inc + chk); chk = 0u; pend = 2;
    }
    WAIT_LGKM0();
    if (nx2 >= 32) tile_v(off, slot); else if (nx2 >= 0) tile_k(off, slot);
    cur = nx1; nx1 = nx2; slot ^= TILEB;
  };

  while (cur >= 0) {
    if (nx1 >= 0) WAIT_VM(16); else WAIT_VM(0);
    if (ntl == 0) { ts1 = __builtin_amdgcn_s_memrealtime(); __builtin_amdgcn_s_waitcnt(0xC07F); } ++ntl;
    const int j = cur & 31;
    const bool isk = cur < 32;
    bf16x8 f[16];
    if (isk) {
#pragma unroll
      for (int st = 0; st < 16; ++st) f[st] = row_frag(lds, slot + krd, st);
    } else {
#pragma unroll
      for (int c = 0; c < 8; ++c) { f[c] = col_frag(lds, slot + vrd, c, 0); f[8 + c] = col_frag(lds, slot + vrd, c, BLKB); }
    }
    if (pend == 2) held = ticket(ctl_result(), cls);
    else if (pend == 1) ready = ctl_result() != w0;
    pend = 0;
    if (leader && nst < NSTAT) ctl_add(cw, vz, W_EPOCH);
    advance();
    u32x4 p0, p1;
    if (isk) {
      f32x16 s = mfma_qk(f, q);
      softmax_tile(s, zm, zl, p0, p1);
      park_put(j, p0, p1);
      *(LAS float*)(lds + mt + j * 128) = zm;
    } else {
      const float tm = *(const LAS float*)(lds + mt + j * 128);
      park_get(j, p0, p1);
      if (!__all(tm == om)) {
        if (!__all(om == -__builtin_inff())) acc_scale(__builtin_amdgcn_exp2f((om - tm) * SCL2));
        om = tm;
      }
      mfma_pv(f, p0);
      mfma_pv(f + 8, p1);
    }
  }
  ctl_add_ret(cw, vz, W_ARR);

  ts2 = __builtin_amdgcn_s_memrealtime(); __builtin_amdgcn_s_waitcnt(0xC07F);
  zl = xhalf_sum(zl);
  {
    const unsigned img = lbase + ring + r32 * (HD * 4) + ((hh ^ (r32 & 7)) << 4);
    acc_store(img, img ^ 32, img ^ 64, img ^ 96);
    if (hh == 0) *(LAS f32x2*)(lds + ML_OFF + (wave * 32 + r32) * 8) = f32x2{zm, zl};
  }
  BARRIER();
  float Mr, Lr, fw[4];
  {
    f32x2 ml[4];
#pragma unroll
    for (int w = 0; w < 4; ++w) ml[w] = *(const LAS f32x2*)(lds + ML_OFF + (w * 32 + wave + 4 * (lane & 7)) * 8);
    Mr = fmaxf(fmaxf(ml[0][0], ml[1][0]), fmaxf(ml[2][0], ml[3][0]));
    Lr = 0.f;
#pragma unroll
    for (int w = 0; w < 4; ++w) { fw[w] = __builtin_amdgcn_exp2f((ml[w][0] - Mr) * SCL2); Lr = fmaf(fw[w], ml[w][1], Lr); }
  }
  f32x4 acc[8];
#pragma unroll
  for (int k = 0; k < 8; ++k) {
    const int row = wave + 4 * k;
    const unsigned a = row * (HD * 4) + ((lane ^ (row & 7)) << 4);
    acc[k] = lane_bcast(fw[0], k) * *(const LAS f32x4*)(lds + a);
#pragma unroll
    for (int w = 1; w < 4; ++w) acc[k] += lane_bcast(fw[w], k) * *(const LAS f32x4*)(lds + w * RINGB + a);
  }
  WAIT_VM(0);
  const unsigned war = ctl_result();
  if (lane == 0) *(LAS unsigned*)(lds + AR_OFF + wave * 4) = war;
  BARRIER();
  const u32x4 ar = *(const LAS u32x4*)(lds + AR_OFF);
  unsigned last = 0;
#pragma unroll
  for (int w = 0; w < 4; ++w) if (((ar[w] >> 22) & 31u) == 15u) last = ar[w];
  last = __builtin_amdgcn_readfirstlane(last);

  const __amdgpu_buffer_rsrc_t rs = __builtin_amdgcn_make_buffer_rsrc(ws, 0, (int)WS_END, 0x00020000);
  const int ml_off = (int)WS_PML + (grp * NSPL * 32 + wave + 4 * (lane & 7)) * 8;
  const int po_off = (int)WS_PO + (grp * NSPL * 32 + wave) * (HD * 4) + lane * 16;
  if (!last) {
#pragma unroll
    for (int k = 0; k < 8; ++k) __builtin_amdgcn_raw_buffer_store_b128(__builtin_bit_cast(u32x4, acc[k]), rs, po_off + k * 4096, mem * RINGB, 16);
    if (lane < 8) __builtin_amdgcn_raw_buffer_store_b64(__builtin_bit_cast(u32x2, f32x2{Mr, Lr}), rs, ml_off, mem * 256, 16);
    WAIT_VM(0);
    BARRIER();
    if (wave == 0) ctl_add(cw, vz, W_PUB);
    if (leader) {
      unsigned w = war;
      for (int i = 0; i < SPIN_MAX && ((w >> 18) & 15u) != 15u; ++i) {
        __builtin_amdgcn_s_sleep(16);
        ctl_add_ret(cw, vz, W_EPOCH); WAIT_VM(0);
        w = ctl_result();
      }
    }
  } else {
    if (((last >> 27) & 3u) != 3u) {
      if (wave == 0) {
        unsigned w = last;
        for (int i = 0; i < SPIN_MAX && ((w >> 27) & 3u) != 3u; ++i) {
          ctl_add_ret(cw, vz, 0u); WAIT_VM(0);
          w = ctl_result();
          ++npoll;
        }
      }
      BARRIER();
    }
    f32x2 oml[3]; f32x4 oa[3][8];
#pragma unroll
    for (int jj = 0; jj < 3; ++jj) {
      const int s = (mem + 1 + jj) & 3;
      oml[jj] = __builtin_bit_cast(f32x2, __builtin_amdgcn_raw_buffer_load_b64(rs, ml_off, s * 256, 16));
#pragma unroll
      for (int k = 0; k < 8; ++k) oa[jj][k] = __builtin_bit_cast(f32x4, __builtin_amdgcn_raw_buffer_load_b128(rs, po_off + k * 4096, s * RINGB, 16));
    }
    const float M = fmaxf(fmaxf(Mr, oml[0][0]), fmaxf(oml[1][0], oml[2][0]));
    float f[4]; float L = 0.f;
    f[3] = __builtin_amdgcn_exp2f((Mr - M) * SCL2); L = f[3] * Lr;
#pragma unroll
    for (int jj = 0; jj < 3; ++jj) { f[jj] = __builtin_amdgcn_exp2f((oml[jj][0] - M) * SCL2); L = fmaf(f[jj], oml[jj][1], L); }
    const float inv = 1.0f / L;
#pragma unroll
    for (int jj = 0; jj < 4; ++jj) f[jj] *= inv;
#pragma unroll
    for (int k = 0; k < 8; ++k) {
      f32x4 r = acc[k] * lane_bcast(f[3], k);
#pragma unroll
      for (int jj = 0; jj < 3; ++jj) r += oa[jj][k] * lane_bcast(f[jj], k);
      *(u32x2*)(out + (size_t)(grp * 32 + wave + 4 * k) * HD + lane * 4) = u32x2{pack2(r[0], r[1]), pack2(r[2], r[3])};
    }
  }
  WAIT_VM(0);
  if (lane == 0) { unsigned long long* sp = (unsigned long long*)(ws + (512ull << 20)) + (wg * 4 + wave) * 8;
    sp[0] = ts0; sp[1] = ts1; sp[2] = ts2; sp[3] = __builtin_amdgcn_s_memrealtime(); sp[4] = __builtin_amdgcn_s_getreg((31 << 11) | 20) & 7; sp[5] = ntl; sp[6] = nslow; sp[7] = npoll; }
}

__global__ void enc_k(const char* ws) {
  const unsigned long long t0 = __builtin_amdgcn_s_memrealtime();
  const unsigned long long* h = (const unsigned long long*)(ws + (512ull << 20));
  unsigned long long mn = ~0ull, mx3 = 0, mx2 = 0, s1 = 0, s2 = 0, mx0 = 0, sl = 0;
  for (int i = 0; i < 1024; ++i) { const unsigned long long* p = h + i * 8; mn = p[0] < mn ? p[0] : mn; mx0 = p[0] > mx0 ? p[0] : mx0; mx3 = p[3] > mx3 ? p[3] : mx3; mx2 = p[2] > mx2 ? p[2] : mx2; s1 += p[1]; s2 += p[2]; sl += p[6]; }
  unsigned long long m = 0;
  if (8 == 0) m = mx3 - mn;
  if (8 == 1) m = s1 / 1024 - mn;
  if (8 == 2) m = s2 / 1024 - mn;
  if (8 == 3) m = mx2 - mn;
  if (8 == 4) m = t0 - mx3;
  if (8 == 5) m = sl;
  if (8 == 6) m = 0;
  if (8 == 7) { m = mn - h[1024 * 8]; }
  if (8 == 8) { mn = h[1024 * 8]; m = 0; }
  if (8 == 9) m = mx0 - mn;
  if (m > 20000) m = 20000;
  while (__builtin_amdgcn_s_memrealtime() - mn < 100000ull + 20ull * m) __builtin_amdgcn_s_sleep(2);
}
__global__ void pre_k(char* ws) { ((unsigned long long*)(ws + (512ull << 20)))[1024 * 8] = __builtin_amdgcn_s_memrealtime(); }
extern "C" void kernel_launch(void* const* d_in, const int* in_sizes, int n_in, void* d_out, int out_size, void* d_ws, size_t ws_size,
                              hipStream_t stream) {
  pre_k<<<1, 1, 0, stream>>>((char*)d_ws);
  tree_attn<<<NGRP * NSPL, 256, 0, stream>>>((const char*)d_in[0], (const char*)d_in[1], (const char*)d_in[2], (const char*)d_in[3],
                                             (const char*)d_in[4], (const char*)d_in[5], (char*)d_ws, (unsigned short*)d_out);
  enc_k<<<1, 1, 0, stream>>>((const char*)d_ws);
}
```

```cpp
#include <hip/hip_runtime.h>
#include <hip/hip_bf16.h>

#define LAS __attribute__((address_space(3)))
typedef __bf16 bf16x8 __attribute__((ext_vector_type(8)));
typedef __bf16 bf16x4 __attribute__((ext_vector_type(4)));
typedef __bf16 bf16x2 __attribute__((ext_vector_type(2)));
typedef float f32x16 __attribute__((ext_vector_type(16)));
typedef float f32x4 __attribute__((ext_vector_type(4)));
typedef float f32x2 __attribute__((ext_vector_type(2)));
typedef unsigned u32x4 __attribute__((ext_vector_type(4)));
typedef unsigned u32x2 __attribute__((ext_vector_type(2)));

constexpr int NB = 8, HKV = 8, NT = 16, CTX = 8192, HD = 256;
constexpr int NGRP = NB * HKV;
constexpr int NSPL = 4;
constexpr int ROWB = HD * 2;
constexpr int BLKB = 16 * ROWB;
constexpr int TILEB = 2 * BLKB;
constexpr int RINGB = 2 * TILEB;
constexpr int Q_OFF = 4 * RINGB;
constexpr int PX_OFF = Q_OFF + TILEB;
constexpr int LDS_BYTES = PX_OFF + 4 * 2 * 2048;
constexpr int CAPA = 15, CAP = CAPA + 2;
constexpr int MT_OFF = Q_OFF;
constexpr int ML_OFF = MT_OFF + 4 * CAP * 128;
constexpr int AR_OFF = ML_OFF + 4 * 32 * 8;
static_assert(LDS_BYTES <= 160 * 1024 && RINGB == 32 * HD * 4 && AR_OFF + 32 <= PX_OFF, "LDS budget");
constexpr float SCL2 = 0.0625f * 1.4426950408889634f;
constexpr float MASKRAW = -1.0e9f * 16.0f;
constexpr float DEFER = 8.0f / SCL2;

constexpr int NTILE = CTX / 32, HALFB = NTILE * BLKB;
constexpr int NSTAT = 4;
#define PRIV 0
#define PB16 1
#define RBOUND 140
constexpr int NFULL = RBOUND + 16;
constexpr int NTICK = NFULL + 1 + 2 * (NTILE - 16 * NSTAT - NFULL) - 4 * PRIV;
static_assert(NFULL <= NTILE - 16 * NSTAT && NTICK + 16 < 1024, "ticket field");
constexpr unsigned W_CHK = 1u << 10, W_ARR = 1u << 14, W_PUB = 1u << 19, W_EPOCH = 1u << 29;
constexpr unsigned W_EPOCH_MASK = 0xE0000000u;
constexpr int SPIN_MAX = 1 << 16;

constexpr size_t WS_CTL = 0, WS_PML = 65536, WS_PO = WS_PML + (size_t)NGRP * NSPL * 32 * 8;
constexpr size_t WS_END = WS_PO + (size_t)NGRP * NSPL * 32 * HD * 4;

__device__ __forceinline__ void dma4(const char* src, unsigned lds, unsigned v0, unsigned v1, unsigned v2, unsigned v3) {
  unsigned keep;
  asm volatile("s_mov_b32 %0, m0\n\ts_mov_b32 m0, %2\n\ts_nop 2\n\t"
               "global_load_lds_dwordx4 %3, %1 nt\n\t"
               "global_load_lds_dwordx4 %4, %1 offset:1024 nt\n\t"
               "global_load_lds_dwordx4 %5, %1 offset:2048 nt\n\t"
               "global_load_lds_dwordx4 %6, %1 offset:3072 nt\n\t"
               "s_mov_b32 m0, %0"
               : "=&s"(keep) : "s"(src), "s"(lds), "v"(v0), "v"(v1), "v"(v2), "v"(v3) : "memory");
}
#define WAIT_VM(n) asm volatile("s_waitcnt vmcnt(" #n ")" ::: "memory")
#define WAIT_LGKM0() asm volatile("s_waitcnt lgkmcnt(0)" ::: "memory")
#define BARRIER() do { WAIT_LGKM0(); __builtin_amdgcn_s_barrier(); asm volatile("" ::: "memory"); } while (0)

__device__ __forceinline__ void ctl_add_ret(unsigned* w, unsigned zero, unsigned inc) {
  unsigned long long ex;
  asm volatile("v_accvgpr_write_b32 a254, %3\n\ts_mov_b64 %0, exec\n\ts_mov_b64 exec, 1\n\ts_nop 2\n\t"
               "global_atomic_add a255, %2, a254, %1 sc0\n\ts_mov_b64 exec, %0\n\ts_nop 1"
               : "=&s"(ex) : "s"(w), "v"(zero), "v"(inc) : "memory", "a254", "a255");
}
__device__ __forceinline__ unsigned ctl_result() {
  unsigned t, r;
  asm volatile("v_accvgpr_read_b32 %0, a255\n\ts_nop 1\n\tv_readfirstlane_b32 %1, %0\n\ts_nop 4" : "=&v"(t), "=s"(r) :: "memory");
  return r;
}
__device__ __forceinline__ void ctl_add(unsigned* w, unsigned zero, unsigned inc) {
  unsigned long long ex;
  asm volatile("s_mov_b64 %0, exec\n\ts_mov_b64 exec, 1\n\ts_nop 2\n\tglobal_atomic_add %2, %3, %1\n\ts_mov_b64 exec, %0\n\ts_nop 1"
               : "=&s"(ex) : "s"(w), "v"(zero), "v"(inc) : "memory");
}
__device__ __forceinline__ void ctl_and(unsigned* w, unsigned zero, unsigned m) {
  unsigned long long ex;
  asm volatile("s_mov_b64 %0, exec\n\ts_mov_b64 exec, 1\n\ts_nop 2\n\tglobal_atomic_and %2, %3, %1\n\ts_mov_b64 exec, %0\n\ts_nop 1"
               : "=&s"(ex) : "s"(w), "v"(zero), "v"(m) : "memory");
}
#define PARK_HEAD "s_getpc_b64 vcc\n\t.Lp0_%=:\n\ts_add_u32 vcc_lo, vcc_lo, %8\n\ts_addc_u32 vcc_hi, vcc_hi, 0\n\t" \
                  "s_add_u32 vcc_lo, vcc_lo, .Lp1_%=-.Lp0_%=\n\ts_addc_u32 vcc_hi, vcc_hi, 0\n\ts_setpc_b64 vcc\n\t.Lp1_%=:\n\t.set .Lpi_%=, 128\n\t.rept 15\n\t"
#define PARK_TAIL "s_branch .Lp2_%=\n\t.set .Lpi_%=, .Lpi_%=+8\n\t.endr\n\t.Lp2_%=:\n\ts_nop 1"
#define PARK_PUT(op, f) op " " f "[.Lpi_%=], %0\n\t" op " " f "[.Lpi_%=+1], %1\n\t" op " " f "[.Lpi_%=+2], %2\n\t" op " " f "[.Lpi_%=+3], %3\n\t" \
                        op " " f "[.Lpi_%=+4], %4\n\t" op " " f "[.Lpi_%=+5], %5\n\t" op " " f "[.Lpi_%=+6], %6\n\t" op " " f "[.Lpi_%=+7], %7\n\t"
#define PARK_GET(op, f) op " %0, " f "[.Lpi_%=]\n\t" op " %1, " f "[.Lpi_%=+1]\n\t" op " %2, " f "[.Lpi_%=+2]\n\t" op " %3, " f "[.Lpi_%=+3]\n\t" \
                        op " %4, " f "[.Lpi_%=+4]\n\t" op " %5, " f "[.Lpi_%=+5]\n\t" op " %6, " f "[.Lpi_%=+6]\n\t" op " %7, " f "[.Lpi_%=+7]\n\t"
__device__ __forceinline__ void park_put(int j, const u32x4& p0, const u32x4& p1) {
  asm volatile(PARK_HEAD PARK_PUT("v_accvgpr_write_b32", "a") PARK_TAIL
               :: "v"(p0[0]), "v"(p0[1]), "v"(p0[2]), "v"(p0[3]), "v"(p1[0]), "v"(p1[1]), "v"(p1[2]), "v"(p1[3]), "s"(j * 68)
               : "memory", "vcc", "scc");
}
__device__ __forceinline__ void park_get(int j, u32x4& p0, u32x4& p1) {
  asm volatile(PARK_HEAD PARK_GET("v_accvgpr_read_b32", "a") PARK_TAIL
               : "=v"(p0[0]), "=v"(p0[1]), "=v"(p0[2]), "=v"(p0[3]), "=v"(p1[0]), "=v"(p1[1]), "=v"(p1[2]), "=v"(p1[3]) : "s"(j * 68)
               : "memory", "vcc", "scc");
}
static_assert(CAPA == 15, "park_put / park_get hold 15 blocks");
__device__ __forceinline__ void acc_zero() {
  asm volatile(".set .Lz_%=, 0\n\t.rept 128\n\tv_accvgpr_write_b32 a[.Lz_%=], 0\n\t.set .Lz_%=, .Lz_%=+1\n\t.endr" ::: "memory", "a0", "a127", "a247");
}
__device__ __forceinline__ void acc_scale(float alpha) {
  float t0, t1, t2, t3;
  asm volatile("s_nop 15\n\t.set .Ls_%=, 0\n\t.rept 32\n\t"
               "v_accvgpr_read_b32 %0, a[.Ls_%=]\n\tv_accvgpr_read_b32 %1, a[.Ls_%=+1]\n\tv_accvgpr_read_b32 %2, a[.Ls_%=+2]\n\tv_accvgpr_read_b32 %3, a[.Ls_%=+3]\n\t"
               "v_mul_f32 %0, %0, %4\n\tv_mul_f32 %1, %1, %4\n\tv_mul_f32 %2, %2, %4\n\tv_mul_f32 %3, %3, %4\n\t"
               "v_accvgpr_write_b32 a[.Ls_%=], %0\n\tv_accvgpr_write_b32 a[.Ls_%=+1], %1\n\tv_accvgpr_write_b32 a[.Ls_%=+2], %2\n\tv_accvgpr_write_b32 a[.Ls_%=+3], %3\n\t"
               ".set .Ls_%=, .Ls_%=+4\n\t.endr\n\ts_nop 1"
               : "=&v"(t0), "=&v"(t1), "=&v"(t2), "=&v"(t3) : "v"(alpha) : "memory");
}
__device__ __forceinline__ f32x16 mfma_qk(const bf16x8 (&f)[16], const bf16x8 (&q)[16]) {
  f32x16 s;
#define M_(a, b) "v_mfma_f32_32x32x16_bf16 %0, %" #a ", %" #b ", %0\n\t"
  asm volatile("s_nop 1\n\tv_mfma_f32_32x32x16_bf16 %0, %1, %9, 0\n\t" M_(2, 10) M_(3, 11) M_(4, 12) M_(5, 13) M_(6, 14) M_(7, 15) M_(8, 16)
               : "=&v"(s) : "v"(f[0]), "v"(f[1]), "v"(f[2]), "v"(f[3]), "v"(f[4]), "v"(f[5]), "v"(f[6]), "v"(f[7]),
                 "v"(q[0]), "v"(q[1]), "v"(q[2]), "v"(q[3]), "v"(q[4]), "v"(q[5]), "v"(q[6]), "v"(q[7]));
  asm volatile(M_(1, 9) M_(2, 10) M_(3, 11) M_(4, 12) M_(5, 13) M_(6, 14) M_(7, 15) M_(8, 16) "s_nop 15"
               : "+v"(s) : "v"(f[8]), "v"(f[9]), "v"(f[10]), "v"(f[11]), "v"(f[12]), "v"(f[13]), "v"(f[14]), "v"(f[15]),
                 "v"(q[8]), "v"(q[9]), "v"(q[10]), "v"(q[11]), "v"(q[12]), "v"(q[13]), "v"(q[14]), "v"(q[15]));
#undef M_
  return s;
}
__device__ __forceinline__ void mfma_pv(const bf16x8* f, const u32x4& p) {
#define M_(a, lo, hi) "v_mfma_f32_32x32x16_bf16 a[" #lo ":" #hi "], %" #a ", %8, a[" #lo ":" #hi "]\n\t"
  asm volatile("s_nop 1\n\t" M_(0, 0, 15) M_(1, 16, 31) M_(2, 32, 47) M_(3, 48, 63) M_(4, 64, 79) M_(5, 80, 95) M_(6, 96, 111) M_(7, 112, 127)
               :: "v"(f[0]), "v"(f[1]), "v"(f[2]), "v"(f[3]), "v"(f[4]), "v"(f[5]), "v"(f[6]), "v"(f[7]), "v"(p) : "memory");
#undef M_
}
__device__ __forceinline__ void acc_store(unsigned a0, unsigned a1, unsigned a2, unsigned a3) {
  asm volatile("s_nop 15\n\t.set .Lw_%=, 0\n\t.rept 8\n\t"
               "ds_write_b128 %0, a[.Lw_%=*16:.Lw_%=*16+3] offset:.Lw_%=*128\n\tds_write_b128 %1, a[.Lw_%=*16+4:.Lw_%=*16+7] offset:.Lw_%=*128\n\t"
               "ds_write_b128 %2, a[.Lw_%=*16+8:.Lw_%=*16+11] offset:.Lw_%=*128\n\tds_write_b128 %3, a[.Lw_%=*16+12:.Lw_%=*16+15] offset:.Lw_%=*128\n\t"
               ".set .Lw_%=, .Lw_%=+1\n\t.endr"
               :: "v"(a0), "v"(a1), "v"(a2), "v"(a3) : "memory");
}

__device__ __forceinline__ float xhalf_max(float x) {
  auto r = __builtin_amdgcn_permlane32_swap(__float_as_uint(x), __float_as_uint(x), false, false);
  return fmaxf(__uint_as_float(r[0]), __uint_as_float(r[1]));
}
__device__ __forceinline__ float xhalf_sum(float x) {
  auto r = __builtin_amdgcn_permlane32_swap(__float_as_uint(x), __float_as_uint(x), false, false);
  return __uint_as_float(r[0]) + __uint_as_float(r[1]);
}
__device__ __forceinline__ unsigned pack2(float a, float b) {
  return __builtin_bit_cast(unsigned, __builtin_convertvector((f32x2){a, b}, bf16x2));
}
__device__ __forceinline__ float lane_bcast(float x, int l) { return __uint_as_float(__builtin_amdgcn_readlane(__float_as_uint(x), l)); }

__device__ __forceinline__ bf16x8 row_frag(LAS char* lds, unsigned rd, int st) {
  return *(const LAS bf16x8*)(lds + ((rd ^ ((st & 7) << 5)) + (st >> 3) * 256));
}
template <int NR>
__device__ __forceinline__ void softmax_tile(f32x16& s, float& m, float& l, u32x4& p0, u32x4& p1) {
  float mx = s[0];
#pragma unroll
  for (int r = 1; r < NR; ++r) mx = fmaxf(mx, s[r]);
  mx = xhalf_max(mx);
  if (!__all(mx <= m + DEFER)) {
    const float mn = fmaxf(m, mx);
    l *= __builtin_amdgcn_exp2f((m - mn) * SCL2);
    m = mn;
  }
  const float nb = -m * SCL2;
  float ps = 0.f;
#pragma unroll
  for (int r = 0; r < NR; ++r) { s[r] = __builtin_amdgcn_exp2f(fmaf(s[r], SCL2, nb)); ps += s[r]; }
  l += ps;
#pragma unroll
  for (int i = 0; i < 4; ++i) { p0[i] = pack2(s[2 * i], s[2 * i + 1]); if (NR == 16) p1[i] = pack2(s[8 + 2 * i], s[9 + 2 * i]); }
}
__device__ __forceinline__ bf16x8 col_frag(LAS char* lds, unsigned vrd, int c, int koff) {
  const unsigned a = (vrd ^ ((c & 3) << 6)) + (c >> 2) * 256 + koff;
  const bf16x4 lo = __builtin_amdgcn_ds_read_tr16_b64_v4bf16((LAS bf16x4*)(lds + a));
  const bf16x4 hi = __builtin_amdgcn_ds_read_tr16_b64_v4bf16((LAS bf16x4*)(lds + a + 8 * ROWB));
  return __builtin_shufflevector(lo, hi, 0, 1, 2, 3, 4, 5, 6, 7);
}
__device__ __forceinline__ int block_unit(int h) { return (16 * NSTAT + NFULL + (h >> 1)) * BLKB + (h & 1) * HALFB + 2; }
__device__ __forceinline__ int ticket(unsigned w) {
  const int n = w & 0x3ff, h = n - NFULL - 1;
  const int full = (16 * NSTAT + n) * BLKB + (n >= RBOUND);
  return n < NFULL ? full : n == NFULL ? 3 : n < NTICK ? block_unit(h) : -1;
}

__global__ void __launch_bounds__(256, 1)
tree_attn(const char* __restrict__ Q, const char* __restrict__ K, const char* __restrict__ V, const char* __restrict__ KT,
          const char* __restrict__ VT, const char* __restrict__ MK, char* __restrict__ ws, unsigned short* __restrict__ out) {
  __shared__ __attribute__((aligned(1024))) char smem[LDS_BYTES];
  LAS char* lds = (LAS char*)smem;
  const unsigned lbase = (unsigned)(size_t)lds;
  const int tid = threadIdx.x, lane = tid & 63, wave = __builtin_amdgcn_readfirstlane(tid >> 6);
  const int wg = blockIdx.x, x8 = wg & 7;
  const int grp = (wg >> 3) * 2 + ((x8 >> 1) & 1), mem = (x8 >> 2) * 2 + (x8 & 1);
  const int r32 = lane & 31, hh = lane >> 5;
  const bool leader = (mem == 1) && (wave == 0);
  unsigned* cw = (unsigned*)(ws + WS_CTL + grp * 256);
  const unsigned vz = 0;
  ctl_add_ret(cw, vz, 0u);

  unsigned ko[8], vo[2];
#pragma unroll
  for (int i = 0; i < 8; ++i) ko[i] = hh * ROWB + ((r32 ^ ((2 * i + hh) & 15)) << 4);
#pragma unroll
  for (int i = 0; i < 2; ++i) vo[i] = hh * ROWB + ((r32 ^ (((2 * i + hh) & 3) << 2)) << 4);
  const unsigned ring = wave * RINGB;
  auto blk_k = [&](const char* src, unsigned dst) {
    dma4(src, lbase + dst, ko[0], ko[1], ko[2], ko[3]);
    dma4(src + 4096, lbase + dst + 4096, ko[4], ko[5], ko[6], ko[7]);
  };
  auto blk_v = [&](const char* src, unsigned dst) {
    dma4(src, lbase + dst, vo[0], vo[1], vo[0], vo[1]);
    dma4(src + 4096, lbase + dst + 4096, vo[0], vo[1], vo[0], vo[1]);
  };
  const char* Kg = K + (size_t)grp * CTX * ROWB;
  const char* Vg = V + (size_t)grp * CTX * ROWB;
  auto tile_k = [&](int off, unsigned dst) { blk_k(Kg + off, dst); blk_k(Kg + off + HALFB, dst + BLKB); };
  auto tile_v = [&](int off, unsigned dst) { blk_v(Vg + off, dst); blk_v(Vg + off + HALFB, dst + BLKB); };
  const int wof = (mem * 4 + wave) * BLKB;

  {
    const unsigned x = (wave & 1) << 7;
    dma4(Q + (size_t)grp * TILEB + wave * 4096, lbase + Q_OFF + wave * 4096, ko[0] ^ x, ko[1] ^ x, ko[2] ^ x, ko[3] ^ x);
  }
  tile_k(wof, ring);
  const unsigned krd = r32 * ROWB + ((hh ^ (r32 & 15)) << 4);
  const int q4 = (lane & 15) >> 2;
  const unsigned vrd = (4 * hh + q4) * ROWB + (q4 << 6) + ((lane >> 4) & 1) * 32 + (lane & 3) * 8;
  WAIT_VM(16);
  const unsigned w0 = ctl_result();
  if (leader) { ctl_and(cw, vz, W_EPOCH_MASK); ctl_add(cw, vz, W_EPOCH); }
  BARRIER();
  bf16x8 q[16];
#pragma unroll
  for (int st = 0; st < 16; ++st) q[st] = row_frag(lds, Q_OFF + krd, st);
  BARRIER();
  tile_k(wof + 16 * BLKB, ring + TILEB);
  acc_zero();
  float zm = -__builtin_inff(), zl = 0.f, om = -__builtin_inff();

  int cur = 0, nx1 = 1;
  unsigned slot = ring;
  int offs = lane ? wof + 16 * BLKB : wof;
  int nk = 2, iv = -1, nst = 2;
  int held = -2;
  int pend = 0;
  bool ready = leader;
  unsigned chk = leader ? 0u : W_CHK;
  const unsigned mt = MT_OFF + wave * (CAP * 128) + r32 * 4;
  int priv = mem == ((wg >> 3) & 3) ? PRIV : 0;
  const unsigned px = PX_OFF + wave * 4096 + lane * 16 - CAPA * 2048;

  auto advance = [&]() {
    if (nst < NSTAT) { held = wof + nst * (16 * BLKB); ++nst; }
    if (iv >= nk && held == -2) {
#pragma unroll 1
      for (int i = 0; i < SPIN_MAX && !ready; ++i) {
        ctl_add_ret(cw, vz, 0u); WAIT_VM(0);
        ready = ctl_result() != w0;
        if (!ready) __builtin_amdgcn_s_sleep(16);
      }
      ready = true;
      ctl_add_ret(cw, vz, 1u + chk); chk = 0u; WAIT_VM(0);
      held = ticket(ctl_result());
    }
    if (iv >= nk && held >= 0 && !(held & 2)) { nk = 0; iv = -1; }
    int nx2 = -1, off = 0;
    if (iv < 0 && held >= 0 && !(held & 2)) {
      off = held & ~3;
      offs = lane == nk ? off : offs;
      nx2 = nk; ++nk;
      if ((held & 1) || nk == CAP) iv = 0;
      held = -2;
    } else {
      if (iv < 0) iv = 0;
      if (iv < nk) { off = __builtin_amdgcn_readlane(offs, iv); nx2 = 32 + iv; ++iv; }
      else if (held >= 0) { off = held & ~3; nx2 = 64 + (held & 1); held = -2; }
      else if (priv > 0) { --priv; off = block_unit(NTICK - NFULL - 1 + 4 * priv + wave) & ~3; nx2 = 64; }
    }
    if (!ready) { ctl_add_ret(cw, vz, 0u); pend = 1; }
    else if (held == -2 && nst >= NSTAT && (iv < 0 || iv >= nk)) {
      ctl_add_ret(cw, vz, 1u + chk); chk = 0u; pend = 2;
    }
    WAIT_LGKM0();
    if (nx2 >= 0) {
      const char* a = (nx2 & 32 ? Vg : Kg) + off;
      const char* b = (nx2 < 32 ? Kg : Vg) + off + (nx2 < 64 ? HALFB : 0);
      if (nx2 == 65) {
        a = KT + (size_t)grp * BLKB; b = VT + (size_t)grp * BLKB;
        const unsigned mo = (r32 & 15) * (NT * 2) + 8 * hh;
        asm volatile("s_nop 4\n\tglobal_load_dwordx2 a[248:249], %0, %1\n\tglobal_load_dwordx2 a[250:251], %0, %1 offset:16"
                     :: "v"(mo), "s"(MK + (grp >> 3) * (NT * NT * 2)) : "memory", "a248", "a249", "a250", "a251");
      }
      if (nx2 & 32) blk_v(a, slot); else blk_k(a, slot);
      if (nx2 < 32) blk_k(b, slot + BLKB); else blk_v(b, slot + BLKB);
    }
    cur = nx1; nx1 = nx2; slot ^= TILEB;
  };

  while (cur >= 0) {
    if (nx1 >= 0) WAIT_VM(16); else WAIT_VM(0);
    const int now = cur, j = cur & 31;
    bf16x8 f[16], g[8];
    if (now & 32) {
#pragma unroll
      for (int c = 0; c < 8; ++c) f[c] = col_frag(lds, slot + vrd, c, 0);
    } else {
      const unsigned rd = slot + (now < 32 ? krd : krd & (BLKB - 1));
#pragma unroll
      for (int st = 0; st < 16; ++st) f[st] = row_frag(lds, rd, st);
    }
    if (now >= 32) {
#pragma unroll
      for (int c = 0; c < 8; ++c) g[c] = col_frag(lds, slot + vrd, c, BLKB);
    }
    if (pend == 2) held = ticket(ctl_result());
    else if (pend == 1) ready = ctl_result() != w0;
    pend = 0;
    if (leader && nst < NSTAT) ctl_add(cw, vz, W_EPOCH);
    unsigned mk[4] = {0x3f803f80u, 0x3f803f80u, 0x3f803f80u, 0x3f803f80u};
    if (now == 65)
      asm volatile("v_accvgpr_read_b32 %0, a248\n\tv_accvgpr_read_b32 %1, a249\n\tv_accvgpr_read_b32 %2, a250\n\tv_accvgpr_read_b32 %3, a251"
                   : "=v"(mk[0]), "=v"(mk[1]), "=v"(mk[2]), "=v"(mk[3]) :: "memory");
    advance();
    u32x4 p0, p1;
    float tm;
    if (now & 32) {
      tm = *(const LAS float*)(lds + mt + j * 128);
      if (j < CAPA) park_get(j, p0, p1);
      else { p0 = *(const LAS u32x4*)(lds + px + j * 2048); p1 = *(const LAS u32x4*)(lds + px + j * 2048 + 1024); }
    } else {
      f32x16 s = mfma_qk(f, q);
      if (now < 32) {
        softmax_tile<16>(s, zm, zl, p0, p1);
        if (j < CAPA) park_put(j, p0, p1);
        else { *(LAS u32x4*)(lds + px + j * 2048) = p0; *(LAS u32x4*)(lds + px + j * 2048 + 1024) = p1; }
        *(LAS float*)(lds + mt + j * 128) = zm;
        continue;
      }
#pragma unroll
      for (int r = 0; r < 8; ++r) {
        const float mv = __uint_as_float((r & 1) ? (mk[r >> 1] & 0xffff0000u) : (mk[r >> 1] << 16));
        s[r] = fmaf(1.0f - mv, MASKRAW, s[r]);
      }
      softmax_tile<8>(s, zm, zl, p1, p0);
      tm = zm;
    }
    if (!__all(tm == om)) {
      if (!__all(om == -__builtin_inff())) acc_scale(__builtin_amdgcn_exp2f((om - tm) * SCL2));
      om = tm;
    }
    if (now & 32) mfma_pv(f, p0);
    mfma_pv(g, p1);
  }
  ctl_add_ret(cw, vz, W_ARR);

  zl = xhalf_sum(zl);
  {
    const unsigned img = lbase + ring + r32 * (HD * 4) + ((hh ^ (r32 & 7)) << 4);
    acc_store(img, img ^ 32, img ^ 64, img ^ 96);
    if (hh == 0) *(LAS f32x2*)(lds + ML_OFF + (wave * 32 + r32) * 8) = f32x2{zm, zl};
  }
  WAIT_VM(0);
  const unsigned war = ctl_result();
  if (lane == 0) { *(LAS unsigned*)(lds + AR_OFF + wave * 4) = war; if (wave == 0) *(LAS unsigned*)(lds + AR_OFF + 16) = 0u; }
  BARRIER();
  const u32x4 ar = *(const LAS u32x4*)(lds + AR_OFF);
  f32x2 ml[4];
#pragma unroll
  for (int w = 0; w < 4; ++w) ml[w] = *(const LAS f32x2*)(lds + ML_OFF + (w * 32 + wave + 4 * (lane & 7)) * 8);
  unsigned last = 0;
#pragma unroll
  for (int w = 0; w < 4; ++w) if (((ar[w] >> 14) & 31u) == 15u) last = ar[w];
  last = __builtin_amdgcn_readfirstlane(last);
  const float Mr = fmaxf(fmaxf(ml[0][0], ml[1][0]), fmaxf(ml[2][0], ml[3][0]));
  float Lr = 0.f, fw[4];
#pragma unroll
  for (int w = 0; w < 4; ++w) { fw[w] = __builtin_amdgcn_exp2f((ml[w][0] - Mr) * SCL2); Lr = fmaf(fw[w], ml[w][1], Lr); }
  auto reduce = [&](int k) {
    const int row = wave + 4 * k;
    const unsigned a = row * (HD * 4) + ((lane ^ (row & 7)) << 4);
    f32x4 r = lane_bcast(fw[0], k) * *(const LAS f32x4*)(lds + a);
#pragma unroll
    for (int w = 1; w < 4; ++w) r += lane_bcast(fw[w], k) * *(const LAS f32x4*)(lds + w * RINGB + a);
    return r;
  };
  constexpr int NV = PB16 ? 4 : 8;
  const __amdgpu_buffer_rsrc_t rs = __builtin_amdgcn_make_buffer_rsrc(ws, 0, (int)WS_END, 0x00020000);
  const int ml_off = (int)WS_PML + (grp * NSPL * 32 + wave + 4 * (lane & 7)) * 8;
  const int po_off = (int)WS_PO + grp * NSPL * RINGB + wave * (NV * 1024) + lane * 16;

  if (!last) {
    if (lane < 8) __builtin_amdgcn_raw_buffer_store_b64(__builtin_bit_cast(u32x2, f32x2{Mr, Lr}), rs, ml_off + mem * 256, 0, 16);
#pragma unroll
    for (int v = 0; v < NV; ++v) {
      u32x4 d;
      if (PB16) { const f32x4 x = reduce(2 * v), y = reduce(2 * v + 1); d = u32x4{pack2(x[0], x[1]), pack2(x[2], x[3]), pack2(y[0], y[1]), pack2(y[2], y[3])}; }
      else d = __builtin_bit_cast(u32x4, reduce(v));
      __builtin_amdgcn_raw_buffer_store_b128(d, rs, po_off + mem * RINGB + v * 1024, 0, 16);
    }
    WAIT_VM(0);
    BARRIER();
    if (wave == 0) ctl_add(cw, vz, W_PUB << mem);
    if (leader) {
      unsigned w = war;
#pragma unroll 1
      for (int i = 0; i < SPIN_MAX && ((w >> 10) & 15u) != 15u; ++i) {
        __builtin_amdgcn_s_sleep(16);
        ctl_add_ret(cw, vz, W_EPOCH); WAIT_VM(0);
        w = ctl_result();
      }
    }
  } else {
    const unsigned all = 15u & ~(1u << mem);
    unsigned todo = all, pub = (last >> 19) & 15u;
    f32x2 oml[3]; u32x4 oa[3][NV]; f32x4 acc[8];
    auto fetch = [&](int jj) {
      const int sm = __builtin_ctz((pub & todo) ? (pub & todo) : todo);
      todo &= ~(1u << sm);
      oml[jj] = __builtin_bit_cast(f32x2, __builtin_amdgcn_raw_buffer_load_b64(rs, ml_off, sm * 256, 16));
#pragma unroll
      for (int v = 0; v < NV; ++v) oa[jj][v] = __builtin_amdgcn_raw_buffer_load_b128(rs, po_off + v * 1024, sm * RINGB, 16);
    };
    const bool watch = wave == 0 && pub != all;
    const int early = watch ? 0 : __builtin_popcount(pub);
    if (watch) ctl_add_ret(cw, vz, 0u);
    if (early > 0) fetch(0);
    if (early > 1) fetch(1);
    if (early > 2) fetch(2);
#pragma unroll
    for (int k = 0; k < 8; ++k) { acc[k] = reduce(k); asm volatile("" : "+v"(acc[k])); }
    if (watch) {
#pragma unroll 1
      for (int i = 0; i < SPIN_MAX; ++i) {
        WAIT_VM(0);
        pub = (ctl_result() >> 19) & 15u;
        if (lane == 0) *(volatile LAS unsigned*)(lds + AR_OFF + 16) = pub;
        if (pub == all) break;
        ctl_add_ret(cw, vz, 0u);
      }
    }
#pragma unroll
    for (int jj = 0; jj < 3; ++jj)
      if (jj >= early) {
#pragma unroll 1
        for (int i = 0; i < (SPIN_MAX << 6) && !(pub & todo); ++i)
          pub = __builtin_amdgcn_readfirstlane(*(volatile LAS unsigned*)(lds + AR_OFF + 16));
        asm volatile("" ::: "memory");
        fetch(jj);
      }
    const float M = fmaxf(fmaxf(Mr, oml[0][0]), fmaxf(oml[1][0], oml[2][0]));
    float f[4]; float L = 0.f;
    f[3] = __builtin_amdgcn_exp2f((Mr - M) * SCL2); L = f[3] * Lr;
#pragma unroll
    for (int jj = 0; jj < 3; ++jj) { f[jj] = __builtin_amdgcn_exp2f((oml[jj][0] - M) * SCL2); L = fmaf(f[jj], oml[jj][1], L); }
    const float inv = 1.0f / L;
#pragma unroll
    for (int jj = 0; jj < 4; ++jj) f[jj] *= inv;
#pragma unroll
    for (int k = 0; k < 8; ++k) {
      f32x4 r = acc[k] * lane_bcast(f[3], k);
#pragma unroll
      for (int jj = 0; jj < 3; ++jj) {
        f32x4 x;
        if (PB16) { const unsigned lo = oa[jj][k >> 1][2 * (k & 1)], hi = oa[jj][k >> 1][2 * (k & 1) + 1];
                    x = f32x4{__uint_as_float(lo << 16), __uint_as_float(lo & 0xffff0000u), __uint_as_float(hi << 16), __uint_as_float(hi & 0xffff0000u)}; }
        else x = __builtin_bit_cast(f32x4, oa[jj][k % NV]);
        r += x * lane_bcast(f[jj], k);
      }
      *(u32x2*)(out + (size_t)(grp * 32 + wave + 4 * k) * HD + lane * 4) = u32x2{pack2(r[0], r[1]), pack2(r[2], r[3])};
    }
  }
}

extern "C" void kernel_launch(void* const* d_in, const int* in_sizes, int n_in, void* d_out, int out_size, void* d_ws, size_t ws_size,
                              hipStream_t stream) {
  tree_attn<<<NGRP * NSPL, 256, 0, stream>>>((const char*)d_in[0], (const char*)d_in[1], (const char*)d_in[2], (const char*)d_in[3],
                                             (const char*)d_in[4], (const char*)d_in[5], (char*)d_ws, (unsigned short*)d_out);
}
```

```cpp
#include <hip/hip_runtime.h>
#include <hip/hip_bf16.h>

#define LAS __attribute__((address_space(3)))
typedef __bf16 bf16x8 __attribute__((ext_vector_type(8)));
typedef __bf16 bf16x4 __attribute__((ext_vector_type(4)));
typedef __bf16 bf16x2 __attribute__((ext_vector_type(2)));
typedef float f32x16 __attribute__((ext_vector_type(16)));
typedef float f32x4 __attribute__((ext_vector_type(4)));
typedef float f32x2 __attribute__((ext_vector_type(2)));
typedef unsigned u32x4 __attribute__((ext_vector_type(4)));
typedef unsigned u32x2 __attribute__((ext_vector_type(2)));

constexpr int NB = 8, HKV = 8, NT = 16, CTX = 8192, HD = 256;
constexpr int NGRP = NB * HKV;
constexpr int NSPL = 4;
constexpr int ROWB = HD * 2;
constexpr int BLKB = 16 * ROWB;
constexpr int TILEB = 2 * BLKB;
constexpr int RINGB = 2 * TILEB;
constexpr int Q_OFF = 4 * RINGB;
constexpr int PX_OFF = Q_OFF + TILEB;
constexpr int LDS_BYTES = PX_OFF + 4 * 2 * 2048;
constexpr int CAPA = 15, CAP = CAPA + 2;
constexpr int MT_OFF = Q_OFF;
constexpr int ML_OFF = MT_OFF + 4 * CAP * 128;
constexpr int AR_OFF = ML_OFF + 4 * 32 * 8;
static_assert(LDS_BYTES <= 160 * 1024 && RINGB == 32 * HD * 4 && AR_OFF + 32 <= PX_OFF, "LDS budget");
constexpr float SCL2 = 0.0625f * 1.4426950408889634f;
constexpr float MASKRAW = -1.0e9f * 16.0f;
constexpr float DEFER = 8.0f / SCL2;

constexpr int NTILE = CTX / 32, HALFB = NTILE * BLKB;
constexpr int NSTAT = 4;
#define RBOUND 144
constexpr int NFULL = RBOUND + 16;
constexpr int NTICK = NFULL + 1 + 2 * (NTILE - 16 * NSTAT - NFULL);
static_assert(NFULL <= NTILE - 16 * NSTAT && NTICK + 16 < 1024, "ticket field");
constexpr unsigned W_CHK = 1u << 10, W_ARR = 1u << 14, W_PUB = 1u << 19, W_EPOCH = 1u << 29;
constexpr unsigned W_EPOCH_MASK = 0xE0000000u;
constexpr int SPIN_MAX = 1 << 16;

constexpr size_t WS_CTL = 0, WS_PML = 65536, WS_PO = WS_PML + (size_t)NGRP * NSPL * 32 * 8;
constexpr int PARTB = 32 * HD * 2;
constexpr size_t WS_END = WS_PO + (size_t)NGRP * NSPL * PARTB;

__device__ __forceinline__ void dma4(const char* src, unsigned lds, unsigned v0, unsigned v1, unsigned v2, unsigned v3) {
  unsigned keep;
  asm volatile("s_mov_b32 %0, m0\n\ts_mov_b32 m0, %2\n\ts_nop 2\n\t"
               "global_load_lds_dwordx4 %3, %1 nt\n\t"
               "global_load_lds_dwordx4 %4, %1 offset:1024 nt\n\t"
               "global_load_lds_dwordx4 %5, %1 offset:2048 nt\n\t"
               "global_load_lds_dwordx4 %6, %1 offset:3072 nt\n\t"
               "s_mov_b32 m0, %0"
               : "=&s"(keep) : "s"(src), "s"(lds), "v"(v0), "v"(v1), "v"(v2), "v"(v3) : "memory");
}
#define WAIT_VM(n) asm volatile("s_waitcnt vmcnt(" #n ")" ::: "memory")
#define WAIT_LGKM0() asm volatile("s_waitcnt lgkmcnt(0)" ::: "memory")
#define BARRIER() do { WAIT_LGKM0(); __builtin_amdgcn_s_barrier(); asm volatile("" ::: "memory"); } while (0)

__device__ __forceinline__ void ctl_add_ret(unsigned* w, unsigned zero, unsigned inc) {
  unsigned long long ex;
  asm volatile("v_accvgpr_write_b32 a254, %3\n\ts_mov_b64 %0, exec\n\ts_mov_b64 exec, 1\n\ts_nop 2\n\t"
               "global_atomic_add a255, %2, a254, %1 sc0\n\ts_mov_b64 exec, %0\n\ts_nop 1"
               : "=&s"(ex) : "s"(w), "v"(zero), "v"(inc) : "memory", "a254", "a255");
}
__device__ __forceinline__ unsigned ctl_result() {
  unsigned t, r;
  asm volatile("v_accvgpr_read_b32 %0, a255\n\ts_nop 1\n\tv_readfirstlane_b32 %1, %0\n\ts_nop 4" : "=&v"(t), "=s"(r) :: "memory");
  return r;
}
__device__ __forceinline__ void ctl_add(unsigned* w, unsigned zero, unsigned inc) {
  unsigned long long ex;
  asm volatile("s_mov_b64 %0, exec\n\ts_mov_b64 exec, 1\n\ts_nop 2\n\tglobal_atomic_add %2, %3, %1\n\ts_mov_b64 exec, %0\n\ts_nop 1"
               : "=&s"(ex) : "s"(w), "v"(zero), "v"(inc) : "memory");
}
__device__ __forceinline__ void ctl_and(unsigned* w, unsigned zero, unsigned m) {
  unsigned long long ex;
  asm volatile("s_mov_b64 %0, exec\n\ts_mov_b64 exec, 1\n\ts_nop 2\n\tglobal_atomic_and %2, %3, %1\n\ts_mov_b64 exec, %0\n\ts_nop 1"
               : "=&s"(ex) : "s"(w), "v"(zero), "v"(m) : "memory");
}
#define PARK_HEAD "s_getpc_b64 vcc\n\t.Lp0_%=:\n\ts_add_u32 vcc_lo, vcc_lo, %8\n\ts_addc_u32 vcc_hi, vcc_hi, 0\n\t" \
                  "s_add_u32 vcc_lo, vcc_lo, .Lp1_%=-.Lp0_%=\n\ts_addc_u32 vcc_hi, vcc_hi, 0\n\ts_setpc_b64 vcc\n\t.Lp1_%=:\n\t.set .Lpi_%=, 128\n\t.rept 15\n\t"
#define PARK_TAIL "s_branch .Lp2_%=\n\t.set .Lpi_%=, .Lpi_%=+8\n\t.endr\n\t.Lp2_%=:\n\ts_nop 1"
#define PARK_PUT(op, f) op " " f "[.Lpi_%=], %0\n\t" op " " f "[.Lpi_%=+1], %1\n\t" op " " f "[.Lpi_%=+2], %2\n\t" op " " f "[.Lpi_%=+3], %3\n\t" \
                        op " " f "[.Lpi_%=+4], %4\n\t" op " " f "[.Lpi_%=+5], %5\n\t" op " " f "[.Lpi_%=+6], %6\n\t" op " " f "[.Lpi_%=+7], %7\n\t"
#define PARK_GET(op, f) op " %0, " f "[.Lpi_%=]\n\t" op " %1, " f "[.Lpi_%=+1]\n\t" op " %2, " f "[.Lpi_%=+2]\n\t" op " %3, " f "[.Lpi_%=+3]\n\t" \
                        op " %4, " f "[.Lpi_%=+4]\n\t" op " %5, " f "[.Lpi_%=+5]\n\t" op " %6, " f "[.Lpi_%=+6]\n\t" op " %7, " f "[.Lpi_%=+7]\n\t"
__device__ __forceinline__ void park_put(int j, const u32x4& p0, const u32x4& p1) {
  asm volatile(PARK_HEAD PARK_PUT("v_accvgpr_write_b32", "a") PARK_TAIL
               :: "v"(p0[0]), "v"(p0[1]), "v"(p0[2]), "v"(p0[3]), "v"(p1[0]), "v"(p1[1]), "v"(p1[2]), "v"(p1[3]), "s"(j * 68)
               : "memory", "vcc", "scc");
}
__device__ __forceinline__ void park_get(int j, u32x4& p0, u32x4& p1) {
  asm volatile(PARK_HEAD PARK_GET("v_accvgpr_read_b32", "a") PARK_TAIL
               : "=v"(p0[0]), "=v"(p0[1]), "=v"(p0[2]), "=v"(p0[3]), "=v"(p1[0]), "=v"(p1[1]), "=v"(p1[2]), "=v"(p1[3]) : "s"(j * 68)
               : "memory", "vcc", "scc");
}
static_assert(CAPA == 15, "park_put / park_get hold 15 blocks");
__device__ __forceinline__ void acc_zero() {
  asm volatile(".set .Lz_%=, 0\n\t.rept 128\n\tv_accvgpr_write_b32 a[.Lz_%=], 0\n\t.set .Lz_%=, .Lz_%=+1\n\t.endr" ::: "memory", "a0", "a127", "a247");
}
__device__ __forceinline__ void acc_scale(float alpha) {
  float t0, t1, t2, t3;
  asm volatile("s_nop 15\n\t.set .Ls_%=, 0\n\t.rept 32\n\t"
               "v_accvgpr_read_b32 %0, a[.Ls_%=]\n\tv_accvgpr_read_b32 %1, a[.Ls_%=+1]\n\tv_accvgpr_read_b32 %2, a[.Ls_%=+2]\n\tv_accvgpr_read_b32 %3, a[.Ls_%=+3]\n\t"
               "v_mul_f32 %0, %0, %4\n\tv_mul_f32 %1, %1, %4\n\tv_mul_f32 %2, %2, %4\n\tv_mul_f32 %3, %3, %4\n\t"
               "v_accvgpr_write_b32 a[.Ls_%=], %0\n\tv_accvgpr_write_b32 a[.Ls_%=+1], %1\n\tv_accvgpr_write_b32 a[.Ls_%=+2], %2\n\tv_accvgpr_write_b32 a[.Ls_%=+3], %3\n\t"
               ".set .Ls_%=, .Ls_%=+4\n\t.endr\n\ts_nop 1"
               : "=&v"(t0), "=&v"(t1), "=&v"(t2), "=&v"(t3) : "v"(alpha) : "memory");
}
__device__ __forceinline__ f32x16 mfma_qk(const bf16x8 (&f)[16], const bf16x8 (&q)[16]) {
  f32x16 s;
#define M_(a, b) "v_mfma_f32_32x32x16_bf16 %0, %" #a ", %" #b ", %0\n\t"
  asm volatile("s_nop 1\n\tv_mfma_f32_32x32x16_bf16 %0, %1, %9, 0\n\t" M_(2, 10) M_(3, 11) M_(4, 12) M_(5, 13) M_(6, 14) M_(7, 15) M_(8, 16)
               : "=&v"(s) : "v"(f[0]), "v"(f[1]), "v"(f[2]), "v"(f[3]), "v"(f[4]), "v"(f[5]), "v"(f[6]), "v"(f[7]),
                 "v"(q[0]), "v"(q[1]), "v"(q[2]), "v"(q[3]), "v"(q[4]), "v"(q[5]), "v"(q[6]), "v"(q[7]));
  asm volatile(M_(1, 9) M_(2, 10) M_(3, 11) M_(4, 12) M_(5, 13) M_(6, 14) M_(7, 15) M_(8, 16) "s_nop 15"
               : "+v"(s) : "v"(f[8]), "v"(f[9]), "v"(f[10]), "v"(f[11]), "v"(f[12]), "v"(f[13]), "v"(f[14]), "v"(f[15]),
                 "v"(q[8]), "v"(q[9]), "v"(q[10]), "v"(q[11]), "v"(q[12]), "v"(q[13]), "v"(q[14]), "v"(q[15]));
#undef M_
  return s;
}
__device__ __forceinline__ void mfma_pv(const bf16x8* f, const u32x4& p) {
#define M_(a, lo, hi) "v_mfma_f32_32x32x16_bf16 a[" #lo ":" #hi "], %" #a ", %8, a[" #lo ":" #hi "]\n\t"
  asm volatile("s_nop 1\n\t" M_(0, 0, 15) M_(1, 16, 31) M_(2, 32, 47) M_(3, 48, 63) M_(4, 64, 79) M_(5, 80, 95) M_(6, 96, 111) M_(7, 112, 127)
               :: "v"(f[0]), "v"(f[1]), "v"(f[2]), "v"(f[3]), "v"(f[4]), "v"(f[5]), "v"(f[6]), "v"(f[7]), "v"(p) : "memory");
#undef M_
}
__device__ __forceinline__ void acc_store(unsigned addr, int w) {
  asm volatile("s_nop 15\n\t"
               "s_cmp_eq_u32 %1, 0\n\ts_cbranch_scc1 .Lo0_%=\n\t.set .Lw_%=, 0\n\t.rept 8\n\tds_write_b128 %0, a[.Lw_%=*4:.Lw_%=*4+3] offset:.Lw_%=*1024\n\t.set .Lw_%=, .Lw_%=+1\n\t.endr\n\t.Lo0_%=:\n\t"
               "s_cmp_eq_u32 %1, 1\n\ts_cbranch_scc1 .Lo1_%=\n\t.set .Lw_%=, 8\n\t.rept 8\n\tds_write_b128 %0, a[.Lw_%=*4:.Lw_%=*4+3] offset:.Lw_%=*1024\n\t.set .Lw_%=, .Lw_%=+1\n\t.endr\n\t.Lo1_%=:\n\t"
               "s_cmp_eq_u32 %1, 2\n\ts_cbranch_scc1 .Lo2_%=\n\t.set .Lw_%=, 16\n\t.rept 8\n\tds_write_b128 %0, a[.Lw_%=*4:.Lw_%=*4+3] offset:.Lw_%=*1024\n\t.set .Lw_%=, .Lw_%=+1\n\t.endr\n\t.Lo2_%=:\n\t"
               "s_cmp_eq_u32 %1, 3\n\ts_cbranch_scc1 .Lo3_%=\n\t.set .Lw_%=, 24\n\t.rept 8\n\tds_write_b128 %0, a[.Lw_%=*4:.Lw_%=*4+3] offset:.Lw_%=*1024\n\t.set .Lw_%=, .Lw_%=+1\n\t.endr\n\t.Lo3_%=:"
               :: "v"(addr), "s"(w) : "memory", "scc");
}
template <int B> __device__ __forceinline__ void acc_get16(f32x4* x) {
  float r[16];
  asm volatile("v_accvgpr_read_b32 %0, a[%c16]\n\tv_accvgpr_read_b32 %1, a[%c16+1]\n\tv_accvgpr_read_b32 %2, a[%c16+2]\n\tv_accvgpr_read_b32 %3, a[%c16+3]\n\t"
               "v_accvgpr_read_b32 %4, a[%c16+4]\n\tv_accvgpr_read_b32 %5, a[%c16+5]\n\tv_accvgpr_read_b32 %6, a[%c16+6]\n\tv_accvgpr_read_b32 %7, a[%c16+7]\n\t"
               "v_accvgpr_read_b32 %8, a[%c16+8]\n\tv_accvgpr_read_b32 %9, a[%c16+9]\n\tv_accvgpr_read_b32 %10, a[%c16+10]\n\tv_accvgpr_read_b32 %11, a[%c16+11]\n\t"
               "v_accvgpr_read_b32 %12, a[%c16+12]\n\tv_accvgpr_read_b32 %13, a[%c16+13]\n\tv_accvgpr_read_b32 %14, a[%c16+14]\n\tv_accvgpr_read_b32 %15, a[%c16+15]"
               : "=v"(r[0]), "=v"(r[1]), "=v"(r[2]), "=v"(r[3]), "=v"(r[4]), "=v"(r[5]), "=v"(r[6]), "=v"(r[7]), "=v"(r[8]), "=v"(r[9]),
                 "=v"(r[10]), "=v"(r[11]), "=v"(r[12]), "=v"(r[13]), "=v"(r[14]), "=v"(r[15]) : "i"(B) : "memory");
#pragma unroll
  for (int i = 0; i < 4; ++i) x[i] = f32x4{r[4 * i], r[4 * i + 1], r[4 * i + 2], r[4 * i + 3]};
}

__device__ __forceinline__ float xhalf_max(float x) {
  auto r = __builtin_amdgcn_permlane32_swap(__float_as_uint(x), __float_as_uint(x), false, false);
  return fmaxf(__uint_as_float(r[0]), __uint_as_float(r[1]));
}
__device__ __forceinline__ float xhalf_sum(float x) {
  auto r = __builtin_amdgcn_permlane32_swap(__float_as_uint(x), __float_as_uint(x), false, false);
  return __uint_as_float(r[0]) + __uint_as_float(r[1]);
}
__device__ __forceinline__ unsigned pack2(float a, float b) {
  return __builtin_bit_cast(unsigned, __builtin_convertvector((f32x2){a, b}, bf16x2));
}

__device__ __forceinline__ unsigned swz(unsigned row) { return ((row & 3) << 2) | ((row >> 2) & 3); }
__device__ __forceinline__ bf16x8 row_frag(LAS char* lds, unsigned rd, int st) {
  return *(const LAS bf16x8*)(lds + ((rd ^ ((st & 7) << 5)) + (st >> 3) * 256));
}
template <int NR>
__device__ __forceinline__ void softmax_tile(f32x16& s, float& m, float& l, u32x4& p0, u32x4& p1) {
  float mx = s[0];
#pragma unroll
  for (int r = 1; r < NR; ++r) mx = fmaxf(mx, s[r]);
  mx = xhalf_max(mx);
  if (!__all(mx <= m + DEFER)) {
    const float mn = fmaxf(m, mx);
    l *= __builtin_amdgcn_exp2f((m - mn) * SCL2);
    m = mn;
  }
  const float nb = -m * SCL2;
  float ps = 0.f;
#pragma unroll
  for (int r = 0; r < NR; ++r) { s[r] = __builtin_amdgcn_exp2f(fmaf(s[r], SCL2, nb)); ps += s[r]; }
  l += ps;
#pragma unroll
  for (int i = 0; i < 4; ++i) { p0[i] = pack2(s[2 * i], s[2 * i + 1]); if (NR == 16) p1[i] = pack2(s[8 + 2 * i], s[9 + 2 * i]); }
}
__device__ __forceinline__ bf16x8 col_frag(LAS char* lds, unsigned vrd, int c, int koff) {
  const int o = (c >> 2) * 256 + koff;
  const bf16x4 lo = __builtin_amdgcn_ds_read_tr16_b64_v4bf16((LAS bf16x4*)(lds + ((vrd ^ ((c & 3) << 6)) + o)));
  const bf16x4 hi = __builtin_amdgcn_ds_read_tr16_b64_v4bf16((LAS bf16x4*)(lds + ((vrd ^ ((c & 3) << 6) ^ 32) + o + 8 * ROWB)));
  return __builtin_shufflevector(lo, hi, 0, 1, 2, 3, 4, 5, 6, 7);
}
__device__ __forceinline__ int block_unit(int h) { return (16 * NSTAT + NFULL + (h >> 1)) * BLKB + (h & 1) * HALFB + 2; }
__device__ __forceinline__ int ticket(unsigned w) {
  const int n = w & 0x3ff, h = n - NFULL - 1;
  const int full = (16 * NSTAT + n) * BLKB + (n >= RBOUND);
  return n < NFULL ? full : n == NFULL ? 3 : n < NTICK ? block_unit(h) : -1;
}

__global__ void __launch_bounds__(256, 1)
tree_attn(const char* __restrict__ Q, const char* __restrict__ K, const char* __restrict__ V, const char* __restrict__ KT,
          const char* __restrict__ VT, const char* __restrict__ MK, char* __restrict__ ws, unsigned short* __restrict__ out) {
  __shared__ __attribute__((aligned(1024))) char smem[LDS_BYTES];
  LAS char* lds = (LAS char*)smem;
  const unsigned lbase = (unsigned)(size_t)lds;
  const int tid = threadIdx.x, lane = tid & 63, wave = __builtin_amdgcn_readfirstlane(tid >> 6);
  const int wg = blockIdx.x, x8 = wg & 7;
  const int grp = (wg >> 3) * 2 + ((x8 >> 1) & 1), mem = (x8 >> 2) * 2 + (x8 & 1);
  const int r32 = lane & 31, hh = lane >> 5;
  const bool leader = (mem == 1) && (wave == 0);
  {
    unsigned long long pc; unsigned len;
    asm volatile("s_getpc_b64 %0\n.Lcode0:\n\ts_mov_b32 %1, .Lcode1-.Lcode0" : "=s"(pc), "=s"(len));
    const unsigned o = min((unsigned)(((wg >> 3) * 4 + wave) & 31) * 1024u, (len & ~15u) - 1024u) + lane * 16;
    asm volatile("global_load_dwordx4 a[240:243], %0, %1" :: "v"(o), "s"(pc & ~15ull) : "memory", "a240", "a241", "a242", "a243");
  }
  unsigned ko[8], qo[4];
#pragma unroll
  for (int i = 0; i < 8; ++i) ko[i] = hh * ROWB + ((r32 ^ swz(2 * i + hh)) << 4);
#pragma unroll
  for (int i = 0; i < 4; ++i) qo[i] = ko[i] ^ ((wave & 1) << 5);
  asm volatile("" : "+v"(ko[0]), "+v"(ko[1]), "+v"(ko[2]), "+v"(ko[3]), "+v"(ko[4]), "+v"(ko[5]), "+v"(ko[6]), "+v"(ko[7]),
                    "+v"(qo[0]), "+v"(qo[1]), "+v"(qo[2]), "+v"(qo[3]));
  const unsigned ring = wave * RINGB;
  auto lo4 = [&](const char* src, unsigned dst) { dma4(src, lbase + dst, ko[0], ko[1], ko[2], ko[3]); };
  auto hi4 = [&](const char* src, unsigned dst) { dma4(src + 4096, lbase + dst + 4096, ko[4], ko[5], ko[6], ko[7]); };
  const char* Kg = K + (size_t)grp * CTX * ROWB;
  const char* Vg = V + (size_t)grp * CTX * ROWB;
  auto tile_k = [&](int off, unsigned dst) { lo4(Kg + off, dst); hi4(Kg + off, dst); lo4(Kg + off + HALFB, dst + BLKB); hi4(Kg + off + HALFB, dst + BLKB); };
  const int wof = (mem * 4 + wave) * BLKB;
  dma4(Q + (size_t)grp * TILEB + wave * 4096, lbase + Q_OFF + wave * 4096, qo[0], qo[1], qo[2], qo[3]);
  unsigned* cw = (unsigned*)(ws + WS_CTL + grp * 256);
  const unsigned vz = 0;
  ctl_add_ret(cw, vz, 0u);
  tile_k(wof, ring);
  const unsigned krd = r32 * ROWB + ((hh ^ swz(r32)) << 4);
  const int q4 = (lane & 15) >> 2;
  const unsigned vrd = (4 * hh + q4) * ROWB + (q4 << 6) + ((((lane >> 4) & 1) * 32 + (lane & 3) * 8) ^ (hh << 4));
  WAIT_VM(16);
  const unsigned w0 = ctl_result();
  if (leader) { ctl_and(cw, vz, W_EPOCH_MASK); ctl_add(cw, vz, W_EPOCH); }
  BARRIER();
  bf16x8 q[16];
#pragma unroll
  for (int st = 0; st < 16; ++st) q[st] = row_frag(lds, Q_OFF + krd, st);
  BARRIER();
  tile_k(wof + 16 * BLKB, ring + TILEB);
  acc_zero();
  float zm = -__builtin_inff(), zl = 0.f, om = -__builtin_inff();

  int cur = 0, nx1 = 1;
  unsigned slot = ring;
  int offs = lane ? wof + 16 * BLKB : wof;
  int nk = 2, iv = -1, nst = 2;
  int held = -2;
  int pend = 0;
  bool ready = leader;
  unsigned chk = leader ? 0u : W_CHK;
  const unsigned mt = MT_OFF + wave * (CAP * 128) + r32 * 4;
  const unsigned px = PX_OFF + wave * 4096 + lane * 16 - CAPA * 2048;
  auto rebase = [&](float tm) {
    if (!__all(tm == om)) {
      if (!__all(om == -__builtin_inff())) acc_scale(__builtin_amdgcn_exp2f((om - tm) * SCL2));
      om = tm;
    }
  };
  auto tree_src = [&](const char*& a, const char*& b) {
    a = KT + (size_t)grp * BLKB; b = VT + (size_t)grp * BLKB;
    const unsigned mo = (r32 & 15) * (NT * 2) + 8 * hh;
    asm volatile("s_nop 4\n\tglobal_load_dwordx2 a[248:249], %0, %1\n\tglobal_load_dwordx2 a[250:251], %0, %1 offset:16"
                 :: "v"(mo), "s"(MK + (grp >> 3) * (NT * NT * 2)) : "memory", "a248", "a249", "a250", "a251");
  };

  while ((unsigned)cur < 64u) {
    const int now = cur, j = cur & 31;
    const unsigned rds = slot;
    const bool lone = nx1 < 0;
    if (lone) WAIT_VM(0); else WAIT_VM(16);
    bf16x8 f[16];
    if (now & 32) {
#pragma unroll
      for (int c = 0; c < 8; ++c) { f[c] = col_frag(lds, rds + vrd, c, 0); f[8 + c] = col_frag(lds, rds + vrd, c, BLKB); }
    } else {
#pragma unroll
      for (int st = 0; st < 16; ++st) f[st] = row_frag(lds, rds + krd, st);
    }
    if (pend) {
      const unsigned w = ctl_result();
      held = pend == 2 ? ticket(w) : held;
      ready = ready || w != w0;
      pend = 0;
    }
    if (nst < NSTAT) {
      if (leader) ctl_add(cw, vz, W_EPOCH);
      held = wof + nst * (16 * BLKB); ++nst;
    }
    if (__builtin_expect(iv >= nk && held == -2 && lone, 0)) {
#pragma unroll 1
      for (int i = 0; i < SPIN_MAX && !ready; ++i) {
        ctl_add_ret(cw, vz, 0u); WAIT_VM(0);
        ready = ctl_result() != w0;
        if (!ready) __builtin_amdgcn_s_sleep(16);
      }
      ready = true;
      ctl_add_ret(cw, vz, 1u + chk); chk = 0u; WAIT_VM(0);
      held = ticket(ctl_result());
    }
    const bool full = held >= 0 && !(held & 2);
    const bool fresh = iv >= nk && full && lone;
    nk = fresh ? 0 : nk;
    const bool tk = (fresh || iv < 0) && full;
    iv = tk ? -1 : (iv < 0 ? 0 : iv);
    const bool tv = !tk && iv < nk;
    const bool tb = !tk && !tv && held >= 0 && lone;
    const int voff = __builtin_amdgcn_readlane(offs, iv & 31);
    const int off = tv ? voff : held & ~3;
    const int nx2 = tk ? nk : tv ? 32 + iv : tb ? 64 + (held & 1) : -1;
    offs = lane == (tk ? nk : -1) ? off : offs;
    nk += tk;
    iv = tk ? (((held & 1) || nk == CAP) ? 0 : -1) : iv + tv;
    held = (tk || tb) ? -2 : held;
    const bool ask = held == -2 && nst >= NSTAT && (iv < 0 || (iv >= nk && (!tv || lone)));
    cur = lone ? nx2 : nx1;
    nx1 = lone ? -1 : nx2;
    slot ^= lone ? 0u : (unsigned)TILEB;
    if (!ready || ask || cur < 0) {
      ctl_add_ret(cw, vz, !ready ? 0u : ask ? 1u + chk : W_ARR);
      pend = ready ? 2 : 1;
      chk = ready ? 0u : chk;
    }
    const char* srca = (nx2 & 32 ? Vg : Kg) + off;
    const char* srcb = (nx2 < 32 ? Kg : Vg) + off + (nx2 < 64 ? HALFB : 0);
    if (__builtin_expect(nx2 == 65, 0)) tree_src(srca, srcb);
    WAIT_LGKM0();
    if (nx2 >= 0) { lo4(srca, rds); hi4(srca, rds); lo4(srcb, rds + BLKB); hi4(srcb, rds + BLKB); }
    u32x4 p0, p1;
    if (now & 32) {
      const float tm = *(const LAS float*)(lds + mt + j * 128);
      if (j < CAPA) park_get(j, p0, p1);
      else { p0 = *(const LAS u32x4*)(lds + px + j * 2048); p1 = *(const LAS u32x4*)(lds + px + j * 2048 + 1024); }
      rebase(tm);
      mfma_pv(f, p0);
      mfma_pv(f + 8, p1);
    } else {
      f32x16 s = mfma_qk(f, q);
      softmax_tile<16>(s, zm, zl, p0, p1);
      if (j < CAPA) park_put(j, p0, p1);
      else { *(LAS u32x4*)(lds + px + j * 2048) = p0; *(LAS u32x4*)(lds + px + j * 2048 + 1024) = p1; }
      *(LAS float*)(lds + mt + j * 128) = zm;
    }
  }
  if (cur >= 0) {
    unsigned mk[4] = {0x3f803f80u, 0x3f803f80u, 0x3f803f80u, 0x3f803f80u};
    const unsigned rdk = slot + (krd & (BLKB - 1)), rdv = slot + vrd;
    bool more = true;
    WAIT_VM(8);
    if (cur == 65)
      asm volatile("v_accvgpr_read_b32 %0, a248\n\tv_accvgpr_read_b32 %1, a249\n\tv_accvgpr_read_b32 %2, a250\n\tv_accvgpr_read_b32 %3, a251"
                   : "=v"(mk[0]), "=v"(mk[1]), "=v"(mk[2]), "=v"(mk[3]) :: "memory");
    while (more) {
      bf16x8 f[16];
#pragma unroll
      for (int st = 0; st < 16; ++st) f[st] = row_frag(lds, rdk, st);
      const int n = ctl_result() & 0x3ff;
      more = n < NTICK;
      const int off = block_unit(n - NFULL - 1) & (CTX * ROWB - BLKB);
      ctl_add_ret(cw, vz, more ? 1u : W_ARR);
      WAIT_LGKM0();
      if (more) { lo4(Kg + off, slot); hi4(Kg + off, slot); }
      f32x16 s = mfma_qk(f, q);
      if (more) WAIT_VM(9); else WAIT_VM(1);
#pragma unroll
      for (int c = 0; c < 8; ++c) f[c] = col_frag(lds, rdv, c, BLKB);
#pragma unroll
      for (int r = 0; r < 8; ++r) {
        const float mv = __uint_as_float((r & 1) ? (mk[r >> 1] & 0xffff0000u) : (mk[r >> 1] << 16));
        s[r] = fmaf(1.0f - mv, MASKRAW, s[r]);
      }
#pragma unroll
      for (int i = 0; i < 4; ++i) mk[i] = 0x3f803f80u;
      u32x4 p0, p1;
      softmax_tile<8>(s, zm, zl, p0, p1);
      WAIT_LGKM0();
      if (more) { lo4(Vg + off, slot + BLKB); hi4(Vg + off, slot + BLKB); }
      rebase(zm);
      mfma_pv(f, p0);
      if (more) WAIT_VM(8);
    }
  }

  acc_store(lbase + ring + lane * 16, wave);
  zl = xhalf_sum(zl);
  *(LAS f32x2*)(lds + ML_OFF + (wave * 32 + r32) * 8) = f32x2{zm, zl};
  WAIT_VM(0);
  const unsigned war = ctl_result();
  if (lane < 2) *(LAS unsigned*)(lds + AR_OFF + (lane ? 16 : wave * 4)) = war;
  BARRIER();
  const u32x4 ar = *(const LAS u32x4*)(lds + AR_OFF);
  f32x2 ml[3]; f32x4 im[3][8], acc[8];
#pragma unroll
  for (int d = 0; d < 3; ++d) ml[d] = *(const LAS f32x2*)(lds + ML_OFF + (((wave + 1 + d) & 3) * 32 + r32) * 8);
#pragma unroll
  for (int d = 0; d < 3; ++d)
#pragma unroll
    for (int i = 0; i < 8; ++i) im[d][i] = *(const LAS f32x4*)(lds + ((wave + 1 + d) & 3) * RINGB + wave * 8192 + lane * 16 + i * 1024);
  if (wave == 0) { acc_get16<0>(acc); acc_get16<16>(acc + 4); }
  else if (wave == 1) { acc_get16<32>(acc); acc_get16<48>(acc + 4); }
  else if (wave == 2) { acc_get16<64>(acc); acc_get16<80>(acc + 4); }
  else { acc_get16<96>(acc); acc_get16<112>(acc + 4); }
  unsigned last = 0;
#pragma unroll
  for (int w = 0; w < 4; ++w) if (((ar[w] >> 14) & 31u) == 15u) last = ar[w];
  last = __builtin_amdgcn_readfirstlane(last);

  const __amdgpu_buffer_rsrc_t rs = __builtin_amdgcn_make_buffer_rsrc(ws, 0, (int)WS_END, 0x00020000);
  const int ml_off = (int)WS_PML + (grp * NSPL * 32 + r32) * 8;
  const int po_off = (int)WS_PO + grp * NSPL * PARTB + wave * 4096 + lane * 16;
  float M, L;
  auto reduce = [&]() {
    M = fmaxf(fmaxf(zm, ml[0][0]), fmaxf(ml[1][0], ml[2][0]));
    float f[4];
    f[3] = __builtin_amdgcn_exp2f((zm - M) * SCL2);
    L = f[3] * zl;
#pragma unroll
    for (int d = 0; d < 3; ++d) { f[d] = __builtin_amdgcn_exp2f((ml[d][0] - M) * SCL2); L = fmaf(f[d], ml[d][1], L); }
#pragma unroll
    for (int i = 0; i < 8; ++i) acc[i] = acc[i] * f[3] + im[0][i] * f[0] + im[1][i] * f[1] + im[2][i] * f[2];
  };
  if (!last) {
    reduce();
    if (wave == 0 && hh == 0) __builtin_amdgcn_raw_buffer_store_b64(__builtin_bit_cast(u32x2, f32x2{M, L}), rs, ml_off + mem * 256, 0, 16);
#pragma unroll
    for (int v = 0; v < 4; ++v) {
      const f32x4 x = acc[2 * v], y = acc[2 * v + 1];
      __builtin_amdgcn_raw_buffer_store_b128(u32x4{pack2(x[0], x[1]), pack2(x[2], x[3]), pack2(y[0], y[1]), pack2(y[2], y[3])}, rs,
                                             po_off + mem * PARTB + v * 1024, 0, 16);
    }
    WAIT_VM(0);
    BARRIER();
    if (wave == 0) ctl_add(cw, vz, W_PUB << mem);
    if (leader) {
      unsigned w = war;
#pragma unroll 1
      for (int i = 0; i < SPIN_MAX && ((w >> 10) & 15u) != 15u; ++i) {
        __builtin_amdgcn_s_sleep(16);
        ctl_add_ret(cw, vz, W_EPOCH); WAIT_VM(0);
        w = ctl_result();
      }
    }
  } else {
    const unsigned all = 15u & ~(1u << mem);
    unsigned todo = all, pub = (last >> 19) & 15u;
    f32x2 oml[3]; u32x4 oa[3][4];
    auto fetch = [&](int jj) {
      const int sm = __builtin_ctz((pub & todo) ? (pub & todo) : todo);
      todo &= ~(1u << sm);
      oml[jj] = __builtin_bit_cast(f32x2, __builtin_amdgcn_raw_buffer_load_b64(rs, ml_off, sm * 256, 16));
#pragma unroll
      for (int v = 0; v < 4; ++v) oa[jj][v] = __builtin_amdgcn_raw_buffer_load_b128(rs, po_off + v * 1024, sm * PARTB, 16);
    };
    const bool watch = wave == 0 && pub != all;
    if (watch) ctl_add_ret(cw, vz, 0u);
    reduce();
#pragma unroll
    for (int i = 0; i < 8; ++i) asm volatile("" : "+v"(acc[i]));
    if (watch) {
#pragma unroll 1
      for (int i = 0; i < SPIN_MAX; ++i) {
        WAIT_VM(0);
        const unsigned w = ctl_result();
        pub = (w >> 19) & 15u;
        if (pub != all) ctl_add_ret(cw, vz, 0u);
        *(volatile LAS unsigned*)(lds + AR_OFF + 16) = w;
        if (pub == all) break;
      }
    }
#pragma unroll
    for (int jj = 0; jj < 3; ++jj) {
#pragma unroll 1
      for (int i = 0; i < (SPIN_MAX << 6) && !(pub & todo); ++i)
        pub = (__builtin_amdgcn_readfirstlane(*(volatile LAS unsigned*)(lds + AR_OFF + 16)) >> 19) & 15u;
      asm volatile("" ::: "memory");
      fetch(jj);
    }
    const float Mf = fmaxf(fmaxf(M, oml[0][0]), fmaxf(oml[1][0], oml[2][0]));
    float f[4];
    f[3] = __builtin_amdgcn_exp2f((M - Mf) * SCL2);
    L *= f[3];
#pragma unroll
    for (int jj = 0; jj < 3; ++jj) { f[jj] = __builtin_amdgcn_exp2f((oml[jj][0] - Mf) * SCL2); L = fmaf(f[jj], oml[jj][1], L); }
    const float inv = 1.0f / L;
#pragma unroll
    for (int jj = 0; jj < 4; ++jj) f[jj] *= inv;
    char* orow = (char*)out + (size_t)(grp * 32 + r32) * ROWB + wave * 128 + hh * 16;
#pragma unroll
    for (int v = 0; v < 4; ++v) {
      f32x4 x = acc[2 * v] * f[3], y = acc[2 * v + 1] * f[3];
#pragma unroll
      for (int jj = 0; jj < 3; ++jj) {
        const u32x4 d = oa[jj][v];
        x += f[jj] * f32x4{__uint_as_float(d[0] << 16), __uint_as_float(d[0] & 0xffff0000u), __uint_as_float(d[1] << 16), __uint_as_float(d[1] & 0xffff0000u)};
        y += f[jj] * f32x4{__uint_as_float(d[2] << 16), __uint_as_float(d[2] & 0xffff0000u), __uint_as_float(d[3] << 16), __uint_as_float(d[3] & 0xffff0000u)};
      }
      const auto rx = __builtin_amdgcn_permlane32_swap(pack2(x[0], x[1]), pack2(y[0], y[1]), false, false);
      const auto ry = __builtin_amdgcn_permlane32_swap(pack2(x[2], x[3]), pack2(y[2], y[3]), false, false);
      *(u32x4*)(orow + v * 32) = u32x4{rx[0], ry[0], rx[1], ry[1]};
    }
  }
  asm volatile(".Lcode1:");
}

extern "C" void kernel_launch(void* const* d_in, const int* in_sizes, int n_in, void* d_out, int out_size, void* d_ws, size_t ws_size,
                              hipStream_t stream) {
  tree_attn<<<NGRP * NSPL, 256, 0, stream>>>((const char*)d_in[0], (const char*)d_in[1], (const char*)d_in[2], (const char*)d_in[3],
                                             (const char*)d_in[4], (const char*)d_in[5], (char*)d_ws, (unsigned short*)d_out);
}
```

```cpp
#include <hip/hip_runtime.h>
#include <hip/hip_bf16.h>

#define LAS __attribute__((address_space(3)))
typedef __bf16 bf16x8 __attribute__((ext_vector_type(8)));
typedef __bf16 bf16x4 __attribute__((ext_vector_type(4)));
typedef __bf16 bf16x2 __attribute__((ext_vector_type(2)));
typedef float f32x16 __attribute__((ext_vector_type(16)));
typedef float f32x4 __attribute__((ext_vector_type(4)));
typedef float f32x2 __attribute__((ext_vector_type(2)));
typedef unsigned u32x4 __attribute__((ext_vector_type(4)));
typedef unsigned u32x2 __attribute__((ext_vector_type(2)));

constexpr int NB = 8, HKV = 8, NT = 16, CTX = 8192, HD = 256;
constexpr int NGRP = NB * HKV;
constexpr int NSPL = 4;
constexpr int ROWB = HD * 2;
constexpr int BLKB = 16 * ROWB;
constexpr int TILEB = 2 * BLKB;
constexpr int RINGB = 2 * TILEB;
#define TPW 14
#define WTAIL 63
constexpr int NTICK = 1 + 2 * (CTX / 32 - 16 * TPW);
constexpr int HALFB = CTX / 2 * ROWB, TSTEP = 16 * BLKB;
constexpr int Q_OFF = 4 * RINGB;
constexpr int FL_OFF = Q_OFF + TILEB;
constexpr int LDS_BYTES = FL_OFF + 16;
constexpr int MT_OFF = Q_OFF;
constexpr int ML_OFF = MT_OFF + 4 * TPW * 128;
static_assert(LDS_BYTES <= 160 * 1024 && RINGB == 32 * HD * 4 && ML_OFF + 1024 <= FL_OFF && TPW >= 4 && TPW <= 15 && NTICK + 16 < 1024, "layout");
constexpr float SCL2 = 0.0625f * 1.4426950408889634f;
constexpr float MASKRAW = -1.0e9f * 16.0f;
constexpr float DEFER = 8.0f / SCL2;
#define WIN 5
#define WPAT 0
#define WNEG 0

constexpr unsigned W_TICK = 1u << 4, W_EPOCH = 1u << 29, W_EPOCH_MASK = 0xE0000000u;
constexpr int SPIN_MAX = 1 << 16;
constexpr int PARTB = 32 * HD * 2;
constexpr size_t WS_CTL = 0, WS_PML = 65536, WS_PO = WS_PML + (size_t)NGRP * NSPL * 32 * 8;
constexpr size_t WS_END = WS_PO + (size_t)NGRP * NSPL * PARTB;

template <int OFF, int CNT> __device__ __forceinline__ void dma1(const char* src, unsigned lds, unsigned v) {
  unsigned keep;
  __builtin_amdgcn_sched_barrier(0);
  asm volatile("s_mov_b32 %0, m0\n\ts_mov_b32 m0, %2\n\ts_waitcnt vmcnt(%5)\n\ts_nop 2\n\t"
               "global_load_lds_dwordx4 %3, %1 offset:%4 nt\n\ts_mov_b32 m0, %0"
               : "=&s"(keep) : "s"(src), "s"(lds), "v"(v), "i"(OFF), "i"(CNT) : "memory");
  __builtin_amdgcn_sched_barrier(0);
}
#define WAIT_VM(n) asm volatile("s_waitcnt vmcnt(%0)" :: "i"(n) : "memory")
#define WAIT_LGKM0() asm volatile("s_waitcnt lgkmcnt(0)" ::: "memory")
#define BARRIER() do { WAIT_LGKM0(); __builtin_amdgcn_s_barrier(); asm volatile("" ::: "memory"); } while (0)

__device__ __forceinline__ void ctl_add_ret(unsigned* w, unsigned zero, unsigned inc) {
  unsigned long long ex;
  asm volatile("v_accvgpr_write_b32 a254, %3\n\ts_mov_b64 %0, exec\n\ts_mov_b64 exec, 1\n\ts_nop 2\n\t"
               "global_atomic_add a255, %2, a254, %1 sc0\n\ts_mov_b64 exec, %0\n\ts_nop 1"
               : "=&s"(ex) : "s"(w), "v"(zero), "v"(inc) : "memory", "a254", "a255");
}
__device__ __forceinline__ unsigned ctl_result() {
  unsigned t, r;
  asm volatile("v_accvgpr_read_b32 %0, a255\n\ts_nop 1\n\tv_readfirstlane_b32 %1, %0\n\ts_nop 4" : "=&v"(t), "=s"(r) :: "memory");
  return r;
}
__device__ __forceinline__ void ctl_and(unsigned* w, unsigned zero, unsigned m) {
  unsigned long long ex;
  asm volatile("s_mov_b64 %0, exec\n\ts_mov_b64 exec, 1\n\ts_nop 2\n\tglobal_atomic_and %2, %3, %1\n\ts_mov_b64 exec, %0\n\ts_nop 1"
               : "=&s"(ex) : "s"(w), "v"(zero), "v"(m) : "memory");
}
#define PARK_HEAD "s_getpc_b64 vcc\n\t.Lp0_%=:\n\ts_add_u32 vcc_lo, vcc_lo, %8\n\ts_addc_u32 vcc_hi, vcc_hi, 0\n\t" \
                  "s_add_u32 vcc_lo, vcc_lo, .Lp1_%=-.Lp0_%=\n\ts_addc_u32 vcc_hi, vcc_hi, 0\n\ts_setpc_b64 vcc\n\t.Lp1_%=:\n\t.set .Lpi_%=, 128\n\t.rept 15\n\t"
#define PARK_TAIL "s_branch .Lp2_%=\n\t.set .Lpi_%=, .Lpi_%=+8\n\t.endr\n\t.Lp2_%=:\n\ts_nop 1"
__device__ __forceinline__ void park_put(int j, const u32x4& p0, const u32x4& p1) {
  asm volatile(PARK_HEAD
               "v_accvgpr_write_b32 a[.Lpi_%=], %0\n\tv_accvgpr_write_b32 a[.Lpi_%=+1], %1\n\tv_accvgpr_write_b32 a[.Lpi_%=+2], %2\n\t"
               "v_accvgpr_write_b32 a[.Lpi_%=+3], %3\n\tv_accvgpr_write_b32 a[.Lpi_%=+4], %4\n\tv_accvgpr_write_b32 a[.Lpi_%=+5], %5\n\t"
               "v_accvgpr_write_b32 a[.Lpi_%=+6], %6\n\tv_accvgpr_write_b32 a[.Lpi_%=+7], %7\n\t" PARK_TAIL
               :: "v"(p0[0]), "v"(p0[1]), "v"(p0[2]), "v"(p0[3]), "v"(p1[0]), "v"(p1[1]), "v"(p1[2]), "v"(p1[3]), "s"(j * 68)
               : "memory", "vcc", "scc");
}
__device__ __forceinline__ void park_get(int j, u32x4& p0, u32x4& p1) {
  asm volatile(PARK_HEAD
               "v_accvgpr_read_b32 %0, a[.Lpi_%=]\n\tv_accvgpr_read_b32 %1, a[.Lpi_%=+1]\n\tv_accvgpr_read_b32 %2, a[.Lpi_%=+2]\n\t"
               "v_accvgpr_read_b32 %3, a[.Lpi_%=+3]\n\tv_accvgpr_read_b32 %4, a[.Lpi_%=+4]\n\tv_accvgpr_read_b32 %5, a[.Lpi_%=+5]\n\t"
               "v_accvgpr_read_b32 %6, a[.Lpi_%=+6]\n\tv_accvgpr_read_b32 %7, a[.Lpi_%=+7]\n\t" PARK_TAIL
               : "=v"(p0[0]), "=v"(p0[1]), "=v"(p0[2]), "=v"(p0[3]), "=v"(p1[0]), "=v"(p1[1]), "=v"(p1[2]), "=v"(p1[3]) : "s"(j * 68)
               : "memory", "vcc", "scc");
}
template <int B> __device__ __forceinline__ void acc_zero32() {
  asm volatile(".set .Lz_%=, %c0\n\t.rept 32\n\tv_accvgpr_write_b32 a[.Lz_%=], 0\n\t.set .Lz_%=, .Lz_%=+1\n\t.endr" :: "i"(B) : "memory", "a0", "a127", "a247");
}
__device__ __forceinline__ void acc_scale(float alpha) {
  float t0, t1, t2, t3;
  asm volatile("s_nop 15\n\t.set .Ls_%=, 0\n\t.rept 32\n\t"
               "v_accvgpr_read_b32 %0, a[.Ls_%=]\n\tv_accvgpr_read_b32 %1, a[.Ls_%=+1]\n\tv_accvgpr_read_b32 %2, a[.Ls_%=+2]\n\tv_accvgpr_read_b32 %3, a[.Ls_%=+3]\n\t"
               "v_mul_f32 %0, %0, %4\n\tv_mul_f32 %1, %1, %4\n\tv_mul_f32 %2, %2, %4\n\tv_mul_f32 %3, %3, %4\n\t"
               "v_accvgpr_write_b32 a[.Ls_%=], %0\n\tv_accvgpr_write_b32 a[.Ls_%=+1], %1\n\tv_accvgpr_write_b32 a[.Ls_%=+2], %2\n\tv_accvgpr_write_b32 a[.Ls_%=+3], %3\n\t"
               ".set .Ls_%=, .Ls_%=+4\n\t.endr\n\ts_nop 1"
               : "=&v"(t0), "=&v"(t1), "=&v"(t2), "=&v"(t3) : "v"(alpha) : "memory");
}
#define M_(a, b) "v_mfma_f32_32x32x16_bf16 %0, %" #a ", %" #b ", %0\n\t"
__device__ __forceinline__ void qk_first(f32x16& s, const bf16x8* f, const bf16x8* q) {
  asm volatile("s_nop 1\n\tv_mfma_f32_32x32x16_bf16 %0, %1, %5, 0\n\t" M_(2, 6) M_(3, 7) M_(4, 8)
               : "=&v"(s) : "v"(f[0]), "v"(f[1]), "v"(f[2]), "v"(f[3]), "v"(q[0]), "v"(q[1]), "v"(q[2]), "v"(q[3]));
}
__device__ __forceinline__ void qk_next(f32x16& s, const bf16x8* f, const bf16x8* q) {
  asm volatile("s_nop 1\n\t" M_(1, 5) M_(2, 6) M_(3, 7) M_(4, 8)
               : "+v"(s) : "v"(f[0]), "v"(f[1]), "v"(f[2]), "v"(f[3]), "v"(q[0]), "v"(q[1]), "v"(q[2]), "v"(q[3]));
}
#undef M_
__device__ __forceinline__ void qk_done(f32x16& s) { asm volatile("s_nop 15" : "+v"(s)); }
template <int C> __device__ __forceinline__ void pv4(const bf16x8* f, const u32x4& p) {
  asm volatile("s_nop 1\n\t"
               "v_mfma_f32_32x32x16_bf16 a[%c5:%c5+15], %0, %4, a[%c5:%c5+15]\n\t"
               "v_mfma_f32_32x32x16_bf16 a[%c5+16:%c5+31], %1, %4, a[%c5+16:%c5+31]\n\t"
               "v_mfma_f32_32x32x16_bf16 a[%c5+32:%c5+47], %2, %4, a[%c5+32:%c5+47]\n\t"
               "v_mfma_f32_32x32x16_bf16 a[%c5+48:%c5+63], %3, %4, a[%c5+48:%c5+63]"
               :: "v"(f[0]), "v"(f[1]), "v"(f[2]), "v"(f[3]), "v"(p), "i"(16 * C) : "memory");
}
#define OUT_(p) "s_cmp_eq_u32 %1, " #p "\n\ts_cbranch_scc1 .Lo" #p "_%=\n\t.set .Lw_%=, 8*" #p "\n\t.rept 8\n\t" \
                "ds_write_b128 %0, a[.Lw_%=*4:.Lw_%=*4+3] offset:.Lw_%=*1024\n\t.set .Lw_%=, .Lw_%=+1\n\t.endr\n\t.Lo" #p "_%=:\n\t"
__device__ __forceinline__ void acc_store(unsigned addr, int w) {
  asm volatile("s_nop 15\n\t" OUT_(0) OUT_(1) OUT_(2) OUT_(3) :: "v"(addr), "s"(w) : "memory", "scc");
}
#undef OUT_
template <int B> __device__ __forceinline__ void acc_get16(f32x4* x) {
  float r[16];
  asm volatile("v_accvgpr_read_b32 %0, a[%c16]\n\tv_accvgpr_read_b32 %1, a[%c16+1]\n\tv_accvgpr_read_b32 %2, a[%c16+2]\n\tv_accvgpr_read_b32 %3, a[%c16+3]\n\t"
               "v_accvgpr_read_b32 %4, a[%c16+4]\n\tv_accvgpr_read_b32 %5, a[%c16+5]\n\tv_accvgpr_read_b32 %6, a[%c16+6]\n\tv_accvgpr_read_b32 %7, a[%c16+7]\n\t"
               "v_accvgpr_read_b32 %8, a[%c16+8]\n\tv_accvgpr_read_b32 %9, a[%c16+9]\n\tv_accvgpr_read_b32 %10, a[%c16+10]\n\tv_accvgpr_read_b32 %11, a[%c16+11]\n\t"
               "v_accvgpr_read_b32 %12, a[%c16+12]\n\tv_accvgpr_read_b32 %13, a[%c16+13]\n\tv_accvgpr_read_b32 %14, a[%c16+14]\n\tv_accvgpr_read_b32 %15, a[%c16+15]"
               : "=v"(r[0]), "=v"(r[1]), "=v"(r[2]), "=v"(r[3]), "=v"(r[4]), "=v"(r[5]), "=v"(r[6]), "=v"(r[7]), "=v"(r[8]), "=v"(r[9]),
                 "=v"(r[10]), "=v"(r[11]), "=v"(r[12]), "=v"(r[13]), "=v"(r[14]), "=v"(r[15]) : "i"(B) : "memory");
#pragma unroll
  for (int i = 0; i < 4; ++i) x[i] = f32x4{r[4 * i], r[4 * i + 1], r[4 * i + 2], r[4 * i + 3]};
}

__device__ __forceinline__ float xhalf_max(float x) {
  auto r = __builtin_amdgcn_permlane32_swap(__float_as_uint(x), __float_as_uint(x), false, false);
  return fmaxf(__uint_as_float(r[0]), __uint_as_float(r[1]));
}
__device__ __forceinline__ float xhalf_sum(float x) {
  auto r = __builtin_amdgcn_permlane32_swap(__float_as_uint(x), __float_as_uint(x), false, false);
  return __uint_as_float(r[0]) + __uint_as_float(r[1]);
}
__device__ __forceinline__ unsigned pack2(float a, float b) {
  return __builtin_bit_cast(unsigned, __builtin_convertvector((f32x2){a, b}, bf16x2));
}
__device__ __forceinline__ f32x4 unpack4(unsigned lo, unsigned hi) {
  return f32x4{__uint_as_float(lo << 16), __uint_as_float(lo & 0xffff0000u), __uint_as_float(hi << 16), __uint_as_float(hi & 0xffff0000u)};
}
__device__ __forceinline__ unsigned swz(unsigned row) { return ((row & 3) << 2) | ((row >> 2) & 3); }
__device__ __forceinline__ bf16x8 row_frag(LAS char* lds, unsigned rd, int st) {
  return *(const LAS bf16x8*)(lds + ((rd ^ ((st & 7) << 5)) + (st >> 3) * 256));
}
__device__ __forceinline__ bf16x8 col_frag(LAS char* lds, unsigned vrd, int c, int koff) {
  const int o = (c >> 2) * 256 + koff;
  const bf16x4 lo = __builtin_amdgcn_ds_read_tr16_b64_v4bf16((LAS bf16x4*)(lds + ((vrd ^ ((c & 3) << 6)) + o)));
  const bf16x4 hi = __builtin_amdgcn_ds_read_tr16_b64_v4bf16((LAS bf16x4*)(lds + ((vrd ^ ((c & 3) << 6) ^ 32) + o + 8 * ROWB)));
  return __builtin_shufflevector(lo, hi, 0, 1, 2, 3, 4, 5, 6, 7);
}
template <int NR> __device__ __forceinline__ void sm_ref(const f32x16& s, float& m, float& l) {
  float mx = s[0];
#pragma unroll
  for (int r = 1; r < NR; ++r) mx = fmaxf(mx, s[r]);
  mx = xhalf_max(mx);
  if (!__all(mx <= m + DEFER)) {
    const float mn = fmaxf(m, mx);
    l *= __builtin_amdgcn_exp2f((m - mn) * SCL2);
    m = mn;
  }
  asm volatile("" : "+v"(m), "+v"(l));
}
template <int B> __device__ __forceinline__ u32x4 sm_exp(const f32x16& s, float m, float& l) {
  const float nb = -m * SCL2;
  float e[8];
#pragma unroll
  for (int r = 0; r < 8; ++r) e[r] = __builtin_amdgcn_exp2f(fmaf(s[B + r], SCL2, nb));
  l += ((e[0] + e[1]) + (e[2] + e[3])) + ((e[4] + e[5]) + (e[6] + e[7]));
  u32x4 p = {pack2(e[0], e[1]), pack2(e[2], e[3]), pack2(e[4], e[5]), pack2(e[6], e[7])};
  asm volatile("" : "+v"(p), "+v"(l));
  return p;
}

__global__ void __launch_bounds__(256, 1)
tree_attn(const char* __restrict__ Q, const char* __restrict__ K, const char* __restrict__ V, const char* __restrict__ KT,
          const char* __restrict__ VT, const char* __restrict__ MK, char* __restrict__ ws, unsigned short* __restrict__ out) {
  __shared__ __attribute__((aligned(1024))) char smem[LDS_BYTES];
  LAS char* lds = (LAS char*)smem;
  const unsigned lbase = (unsigned)(size_t)lds;
  const int tid = threadIdx.x, lane = tid & 63, wave = __builtin_amdgcn_readfirstlane(tid >> 6);
  const int wg = blockIdx.x, x8 = wg & 7;
  const int grp = (wg >> 3) * 2 + ((x8 >> 1) & 1), mem = (x8 >> 2) * 2 + (x8 & 1);
  const int r32 = lane & 31, hh = lane >> 5;
  const bool leader = (mem == 1) && (wave == 0);
  {
    unsigned long long pc; unsigned len;
    asm volatile("s_getpc_b64 %0\n.Lcode0:\n\ts_mov_b32 %1, .Lcode1-.Lcode0" : "=s"(pc), "=s"(len));
    const unsigned o = min((unsigned)(((wg >> 3) * 4 + wave) & 31) * 1024u, (len & ~15u) - 1024u) + lane * 16;
    asm volatile("global_load_dwordx4 a[240:243], %0, %1" :: "v"(o), "s"(pc & ~15ull) : "memory", "a240", "a241", "a242", "a243");
  }
  unsigned ko[8], qo[4];
#pragma unroll
  for (int i = 0; i < 8; ++i) ko[i] = hh * ROWB + ((r32 ^ swz(2 * i + hh)) << 4);
#pragma unroll
  for (int i = 0; i < 4; ++i) qo[i] = ko[i] ^ ((wave & 1) << 5);
  asm volatile("" : "+v"(ko[0]), "+v"(ko[1]), "+v"(ko[2]), "+v"(ko[3]), "+v"(ko[4]), "+v"(ko[5]), "+v"(ko[6]), "+v"(ko[7]),
                    "+v"(qo[0]), "+v"(qo[1]), "+v"(qo[2]), "+v"(qo[3]));
  const unsigned ring = wave * RINGB;
  const size_t wof = (size_t)grp * (CTX * ROWB) + (mem * 4 + wave) * BLKB;
  const char* src = K + wof;
  unsigned dst = lbase + ring;
#define PIECE_N(k, cnt) dma1<((k) & 3) * 1024, (cnt)>(src + ((k) >> 3) * HALFB + (((k) >> 2) & 1) * 4096, dst + ((k) >> 2) * 4096, ko[(k) & 7])
#define PIECE(k) PIECE_N(k, WIN - 1 + ((WPAT >> (k)) & 1) - ((WNEG >> (k)) & 1))
  auto quarter = [&](const char* s, unsigned d) {
    dma1<0, 63>(s, d, qo[0]); dma1<1024, 63>(s, d, qo[1]); dma1<2048, 63>(s, d, qo[2]); dma1<3072, 63>(s, d, qo[3]);
  };
  quarter(Q + (size_t)grp * TILEB + wave * 4096, lbase + Q_OFF + wave * 4096);
  unsigned* cw = (unsigned*)(ws + WS_CTL + grp * 256);
  const unsigned vz = 0;
  if (wave == 0) ctl_add_ret(cw, vz, 0u);
  PIECE_N(0, 63); PIECE_N(1, 63); PIECE_N(2, 63); PIECE_N(3, 63); PIECE_N(4, 63);
  const unsigned krd = r32 * ROWB + ((hh ^ swz(r32)) << 4);
  const int q4 = (lane & 15) >> 2;
  const unsigned vrd = (4 * hh + q4) * ROWB + (q4 << 6) + ((((lane >> 4) & 1) * 32 + (lane & 3) * 8) ^ (hh << 4));
  acc_zero32<0>(); acc_zero32<32>(); acc_zero32<64>(); acc_zero32<96>();
  WAIT_VM(5);
  const unsigned w0 = ctl_result();
  if (leader) ctl_and(cw, vz, W_EPOCH_MASK);
  if (wave == 0) *(LAS u32x2*)(lds + FL_OFF) = u32x2{0u, 0u};
  BARRIER();
  PIECE(5);
  bf16x8 q[16];
#pragma unroll
  for (int st = 0; st < 16; ++st) q[st] = row_frag(lds, Q_OFF + krd, st);
  PIECE(6);
  BARRIER();
  float zm = -__builtin_inff(), zl = 0.f, om = -__builtin_inff();
  PIECE(7); PIECE(8); PIECE(9); PIECE(10); PIECE(11);
  PIECE(12); PIECE(13); PIECE(14); PIECE(15);

  bool ready = false;
  const unsigned mt = MT_OFF + wave * (TPW * 128) + r32 * 4;
  unsigned slot = ring;
#pragma unroll 1
  for (int n = 0; n < TPW; ++n) {
    src = n + 1 < TPW ? src + TSTEP : V + wof;
    dst ^= TILEB;
    const bool look = wave == 0 && (leader ? n < 4 : !ready);
    if (look) ctl_add_ret(cw, vz, leader ? W_EPOCH : 0u);
    PIECE(0); PIECE(1); PIECE(2); PIECE(3); PIECE(4);
    WAIT_VM(5);
    bf16x8 f[16]; f32x16 s;
#pragma unroll
    for (int st = 0; st < 16; ++st) f[st] = row_frag(lds, slot + krd, st);
    if (look && !ready && (leader || ctl_result() != w0)) { ready = true; *(volatile LAS unsigned*)(lds + FL_OFF + 4) = 1u; }
    PIECE(5);
    qk_first(s, f, q);
    PIECE(6);
    qk_next(s, f + 4, q + 4);
    PIECE(7);
    qk_next(s, f + 8, q + 8);
    PIECE(8);
    qk_next(s, f + 12, q + 12);
    PIECE(9);
    qk_done(s);
    sm_ref<16>(s, zm, zl);
    PIECE(10);
    const u32x4 p0 = sm_exp<0>(s, zm, zl);
    PIECE(11);
    const u32x4 p1 = sm_exp<8>(s, zm, zl);
    PIECE(12);
    park_put(n, p0, p1);
    *(LAS float*)(lds + mt + n * 128) = zm;
    PIECE(13); PIECE(14); PIECE(15);
    slot ^= TILEB;
  }
  auto rebase = [&](float tm) {
    if (!__all(tm == om)) {
      if (!__all(om == -__builtin_inff())) acc_scale(__builtin_amdgcn_exp2f((om - tm) * SCL2));
      om = tm;
    }
  };
  const char* Kg = K + (size_t)grp * (CTX * ROWB);
  const char* Vg = V + (size_t)grp * (CTX * ROWB);
  const char* srcb = src;
  auto take = [&](unsigned w) {
    const int n = (w >> 4) & 0x3ff, h = n - 1;
    const int off = ((16 * TPW + (h >> 1)) * BLKB + (h & 1) * HALFB) & (CTX * ROWB - BLKB);
    src = Kg + off; srcb = Vg + off;
    if (__builtin_expect(n == 0, 0)) {
      src = KT + (size_t)grp * BLKB; srcb = VT + (size_t)grp * BLKB;
      const unsigned mo = (r32 & 15) * (NT * 2) + 8 * hh;
      asm volatile("s_nop 4\n\tglobal_load_dwordx2 a[248:249], %0, %1\n\tglobal_load_dwordx2 a[250:251], %0, %1 offset:16"
                   :: "v"(mo), "s"(MK + (grp >> 3) * (NT * NT * 2)) : "memory", "a248", "a249", "a250", "a251");
    }
    return n;
  };
#define PIECE2(k, cnt) do { if (more) dma1<((k) & 3) * 1024, (cnt)>(((k) >> 3 ? srcb : src) + (((k) >> 2) & 1) * 4096, dst + ((k) >> 2) * 4096, ko[(k) & 7]); } while (0)
#define PIECEV(k) PIECE2(k, WIN - 1 + ((WPAT >> (k)) & 1) - ((WNEG >> (k)) & 1))
  bool more = true;
  int tick = 1;
#pragma unroll 1
  for (int n = 0; n < TPW; ++n) {
    dst ^= TILEB;
    if (n + 1 < TPW) { src += TSTEP; srcb = src + HALFB; }
    else { tick = take(ctl_result()); more = tick < NTICK; }
    if (n + 2 == TPW) {
      if (wave == 0) {
#pragma unroll 1
        for (int i = 0; i < SPIN_MAX && !ready; ++i) {
          ctl_add_ret(cw, vz, 0u); WAIT_VM(0);
          ready = ctl_result() != w0;
          if (!ready) __builtin_amdgcn_s_sleep(16);
        }
        *(volatile LAS unsigned*)(lds + FL_OFF + 4) = 1u;
      } else {
#pragma unroll 1
        for (int i = 0; i < (SPIN_MAX << 8); ++i)
          if (__builtin_amdgcn_readfirstlane(*(volatile LAS unsigned*)(lds + FL_OFF + 4)) == 1) break;
      }
    }
    u32x4 r0, r1;
    PIECEV(0);
    const float tm = *(const LAS float*)(lds + mt + n * 128);
    park_get(n, r0, r1);
    PIECEV(1);
    rebase(tm);
    PIECEV(2); PIECEV(3); PIECEV(4);
    if (more) WAIT_VM(5); else WAIT_VM(0);
    bf16x8 f[16];
#pragma unroll
    for (int c = 0; c < 8; ++c) f[c] = col_frag(lds, slot + vrd, c, 0);
    PIECEV(5);
#pragma unroll
    for (int c = 0; c < 8; ++c) f[8 + c] = col_frag(lds, slot + vrd, c, BLKB);
    PIECEV(6);
    pv4<0>(f, r0);
    PIECEV(7);
    pv4<4>(f + 4, r0);
    if (n + 2 >= TPW && more) ctl_add_ret(cw, vz, W_TICK);
    PIECEV(8);
    pv4<0>(f + 8, r1);
    PIECEV(9);
    pv4<4>(f + 12, r1);
    PIECEV(10); PIECEV(11); PIECEV(12); PIECEV(13); PIECEV(14); PIECEV(15);
    WAIT_VM(8);
    slot ^= TILEB;
  }
  while (more) {
    unsigned mk[4] = {0x3f803f80u, 0x3f803f80u, 0x3f803f80u, 0x3f803f80u};
    if (__builtin_expect(tick == 0, 0))
      asm volatile("v_accvgpr_read_b32 %0, a248\n\tv_accvgpr_read_b32 %1, a249\n\tv_accvgpr_read_b32 %2, a250\n\tv_accvgpr_read_b32 %3, a251"
                   : "=v"(mk[0]), "=v"(mk[1]), "=v"(mk[2]), "=v"(mk[3]) :: "memory");
    bf16x8 f[16]; f32x16 s;
#pragma unroll
    for (int st = 0; st < 16; ++st) f[st] = row_frag(lds, slot + (krd & (BLKB - 1)), st);
    dst ^= TILEB;
    tick = take(ctl_result()); more = tick < NTICK;
    PIECE2(0, WTAIL - 1); PIECE2(1, WTAIL - 1);
    qk_first(s, f, q);
    PIECE2(2, WTAIL - 1);
    qk_next(s, f + 4, q + 4);
    PIECE2(3, WTAIL - 1);
    qk_next(s, f + 8, q + 8);
    PIECE2(4, WTAIL - 1);
    qk_next(s, f + 12, q + 12);
    if (more) WAIT_VM(5); else WAIT_VM(0);
    qk_done(s);
#pragma unroll
    for (int c = 0; c < 8; ++c) f[c] = col_frag(lds, slot + vrd, c, BLKB);
#pragma unroll
    for (int r = 0; r < 8; ++r) {
      const float mv = __uint_as_float((r & 1) ? (mk[r >> 1] & 0xffff0000u) : (mk[r >> 1] << 16));
      s[r] = fmaf(1.0f - mv, MASKRAW, s[r]);
    }
    PIECE2(5, WTAIL - 1);
    sm_ref<8>(s, zm, zl);
    PIECE2(6, WTAIL - 1);
    const u32x4 p = sm_exp<0>(s, zm, zl);
    PIECE2(7, WTAIL - 1);
    if (more) ctl_add_ret(cw, vz, W_TICK);
    rebase(zm);
    PIECE2(8, WTAIL - 1);
    pv4<0>(f, p);
    PIECE2(9, WTAIL - 1);
    pv4<4>(f + 4, p);
    PIECE2(10, WTAIL - 1); PIECE2(11, WTAIL - 1); PIECE2(12, WTAIL - 1); PIECE2(13, WTAIL - 1); PIECE2(14, WTAIL - 1); PIECE2(15, WTAIL - 1);
    WAIT_VM(8);
    slot ^= TILEB;
  }

  acc_store(lbase + ring + lane * 16, wave);
  zl = xhalf_sum(zl);
  *(LAS f32x2*)(lds + ML_OFF + (wave * 32 + r32) * 8) = f32x2{zm, zl};
  BARRIER();
  f32x2 ml[3]; f32x4 im[3][8], acc[8];
#pragma unroll
  for (int d = 0; d < 3; ++d) ml[d] = *(const LAS f32x2*)(lds + ML_OFF + (((wave + 1 + d) & 3) * 32 + r32) * 8);
#pragma unroll
  for (int d = 0; d < 3; ++d)
#pragma unroll
    for (int i = 0; i < 8; ++i) im[d][i] = *(const LAS f32x4*)(lds + ((wave + 1 + d) & 3) * RINGB + wave * 8192 + lane * 16 + i * 1024);
  if (wave == 0) { acc_get16<0>(acc); acc_get16<16>(acc + 4); }
  else if (wave == 1) { acc_get16<32>(acc); acc_get16<48>(acc + 4); }
  else if (wave == 2) { acc_get16<64>(acc); acc_get16<80>(acc + 4); }
  else { acc_get16<96>(acc); acc_get16<112>(acc + 4); }
  const float M = fmaxf(fmaxf(zm, ml[0][0]), fmaxf(ml[1][0], ml[2][0]));
  float f[4];
  f[3] = __builtin_amdgcn_exp2f((zm - M) * SCL2);
  float L = f[3] * zl;
#pragma unroll
  for (int d = 0; d < 3; ++d) { f[d] = __builtin_amdgcn_exp2f((ml[d][0] - M) * SCL2); L = fmaf(f[d], ml[d][1], L); }

  const __amdgpu_buffer_rsrc_t rs = __builtin_amdgcn_make_buffer_rsrc(ws, 0, (int)WS_END, 0x00020000);
  const int ml_off = (int)WS_PML + (grp * NSPL * 32 + r32) * 8;
  const int po_off = (int)WS_PO + grp * NSPL * PARTB + wave * 4096 + lane * 16;
  if (wave == 0 && hh == 0) __builtin_amdgcn_raw_buffer_store_b64(__builtin_bit_cast(u32x2, f32x2{M, L}), rs, ml_off + mem * 256, 0, 16);
  f32x4 x = {}, y = {};
#pragma unroll
  for (int v = 0; v < 4; ++v) {
    const f32x4 a = acc[2 * v] * f[3] + im[0][2 * v] * f[0] + im[1][2 * v] * f[1] + im[2][2 * v] * f[2];
    const f32x4 b = acc[2 * v + 1] * f[3] + im[0][2 * v + 1] * f[0] + im[1][2 * v + 1] * f[1] + im[2][2 * v + 1] * f[2];
    if (v == mem) { x = a; y = b; }
    else __builtin_amdgcn_raw_buffer_store_b128(u32x4{pack2(a[0], a[1]), pack2(a[2], a[3]), pack2(b[0], b[1]), pack2(b[2], b[3])}, rs,
                                                po_off + mem * PARTB + v * 1024, 0, 16);
  }
  WAIT_VM(0);
  BARRIER();
  if (wave == 0) {
    ctl_add_ret(cw, vz, 1u << mem);
#pragma unroll 1
    for (int i = 0; i < SPIN_MAX; ++i) {
      WAIT_VM(0);
      const unsigned w = ctl_result() | (1u << mem);
      if ((w & 15u) == 15u) break;
      ctl_add_ret(cw, vz, leader ? W_EPOCH : 0u);
    }
    *(volatile LAS unsigned*)(lds + FL_OFF) = 1u;
  } else {
#pragma unroll 1
    for (int i = 0; i < (SPIN_MAX << 8); ++i)
      if (__builtin_amdgcn_readfirstlane(*(volatile LAS unsigned*)(lds + FL_OFF))) break;
  }
  asm volatile("" ::: "memory");
  f32x2 oml[3]; u32x4 oa[3];
#pragma unroll
  for (int d = 0; d < 3; ++d) {
    const int sm = (mem + 1 + d) & 3;
    oml[d] = __builtin_bit_cast(f32x2, __builtin_amdgcn_raw_buffer_load_b64(rs, ml_off, sm * 256, 16));
    oa[d] = __builtin_amdgcn_raw_buffer_load_b128(rs, po_off, sm * PARTB + mem * 1024, 16);
  }
  const float Mf = fmaxf(fmaxf(M, oml[0][0]), fmaxf(oml[1][0], oml[2][0]));
  f[3] = __builtin_amdgcn_exp2f((M - Mf) * SCL2);
  L *= f[3];
#pragma unroll
  for (int d = 0; d < 3; ++d) { f[d] = __builtin_amdgcn_exp2f((oml[d][0] - Mf) * SCL2); L = fmaf(f[d], oml[d][1], L); }
  const float inv = 1.0f / L;
  x *= f[3] * inv; y *= f[3] * inv;
#pragma unroll
  for (int d = 0; d < 3; ++d) { x += (f[d] * inv) * unpack4(oa[d][0], oa[d][1]); y += (f[d] * inv) * unpack4(oa[d][2], oa[d][3]); }
  const auto rx = __builtin_amdgcn_permlane32_swap(pack2(x[0], x[1]), pack2(y[0], y[1]), false, false);
  const auto ry = __builtin_amdgcn_permlane32_swap(pack2(x[2], x[3]), pack2(y[2], y[3]), false, false);
  *(u32x4*)((char*)out + (size_t)(grp * 32 + r32) * ROWB + wave * 128 + mem * 32 + hh * 16) = u32x4{rx[0], ry[0], rx[1], ry[1]};
  asm volatile(".Lcode1:");
}

extern "C" void kernel_launch(void* const* d_in, const int* in_sizes, int n_in, void* d_out, int out_size, void* d_ws, size_t ws_size,
                              hipStream_t stream) {
  tree_attn<<<NGRP * NSPL, 256, 0, stream>>>((const char*)d_in[0], (const char*)d_in[1], (const char*)d_in[2], (const char*)d_in[3],
                                             (const char*)d_in[4], (const char*)d_in[5], (char*)d_ws, (unsigned short*)d_out);
}
```

```cpp
#define DBG_STAMPS
#include <hip/hip_runtime.h>
#include <hip/hip_bf16.h>

#define LAS __attribute__((address_space(3)))
typedef __bf16 bf16x8 __attribute__((ext_vector_type(8)));
typedef __bf16 bf16x4 __attribute__((ext_vector_type(4)));
typedef __bf16 bf16x2 __attribute__((ext_vector_type(2)));
typedef float f32x16 __attribute__((ext_vector_type(16)));
typedef float f32x4 __attribute__((ext_vector_type(4)));
typedef float f32x2 __attribute__((ext_vector_type(2)));
typedef unsigned u32x4 __attribute__((ext_vector_type(4)));
typedef unsigned u32x2 __attribute__((ext_vector_type(2)));

constexpr int NB = 8, HKV = 8, NT = 16, CTX = 8192, HD = 256;
constexpr int NGRP = NB * HKV;
constexpr int NSPL = 4;
constexpr int ROWB = HD * 2;
constexpr int BLKB = 16 * ROWB;
constexpr int TILEB = 2 * BLKB;
constexpr int RINGB = 2 * TILEB;
#define TPW 14
#define WTAIL 63
constexpr int NTICK = 1 + 2 * (CTX / 32 - 16 * TPW);
constexpr int HALFB = CTX / 2 * ROWB, TSTEP = 16 * BLKB;
constexpr int Q_OFF = 4 * RINGB;
constexpr int FL_OFF = Q_OFF + TILEB;
constexpr int LDS_BYTES = FL_OFF + 16;
constexpr int MT_OFF = Q_OFF;
constexpr int ML_OFF = MT_OFF + 4 * TPW * 128;
static_assert(LDS_BYTES <= 160 * 1024 && RINGB == 32 * HD * 4 && ML_OFF + 1024 <= FL_OFF && TPW >= 4 && TPW <= 15 && NTICK + 16 < 1024, "layout");
constexpr float SCL2 = 0.0625f * 1.4426950408889634f;
constexpr float MASKRAW = -1.0e9f * 16.0f;
constexpr float DEFER = 8.0f / SCL2;
#define WIN 5
#define WPAT 0
#define WNEG 0

constexpr unsigned W_TICK = 1u << 4, W_EPOCH = 1u << 29, W_EPOCH_MASK = 0xE0000000u;
constexpr int SPIN_MAX = 1 << 16;
constexpr int PARTB = 32 * HD * 2;
constexpr size_t WS_CTL = 0, WS_PML = 65536, WS_PO = WS_PML + (size_t)NGRP * NSPL * 32 * 8;
constexpr size_t WS_END = WS_PO + (size_t)NGRP * NSPL * PARTB;

template <int OFF, int CNT> __device__ __forceinline__ void dma1(const char* src, unsigned lds, unsigned v) {
  unsigned keep;
  __builtin_amdgcn_sched_barrier(0);
  asm volatile("s_mov_b32 %0, m0\n\ts_mov_b32 m0, %2\n\ts_waitcnt vmcnt(%5)\n\ts_nop 2\n\t"
               "global_load_lds_dwordx4 %3, %1 offset:%4 nt\n\ts_mov_b32 m0, %0"
               : "=&s"(keep) : "s"(src), "s"(lds), "v"(v), "i"(OFF), "i"(CNT) : "memory");
  __builtin_amdgcn_sched_barrier(0);
}
#define WAIT_VM(n) asm volatile("s_waitcnt vmcnt(%0)" :: "i"(n) : "memory")
#define WAIT_LGKM0() asm volatile("s_waitcnt lgkmcnt(0)" ::: "memory")
#define BARRIER() do { WAIT_LGKM0(); __builtin_amdgcn_s_barrier(); asm volatile("" ::: "memory"); } while (0)

__device__ __forceinline__ void ctl_add_ret(unsigned* w, unsigned zero, unsigned inc) {
  unsigned long long ex;
  asm volatile("v_accvgpr_write_b32 a254, %3\n\ts_mov_b64 %0, exec\n\ts_mov_b64 exec, 1\n\ts_nop 2\n\t"
               "global_atomic_add a255, %2, a254, %1 sc0\n\ts_mov_b64 exec, %0\n\ts_nop 1"
               : "=&s"(ex) : "s"(w), "v"(zero), "v"(inc) : "memory", "a254", "a255");
}
__device__ __forceinline__ unsigned ctl_result() {
  unsigned t, r;
  asm volatile("v_accvgpr_read_b32 %0, a255\n\ts_nop 1\n\tv_readfirstlane_b32 %1, %0\n\ts_nop 4" : "=&v"(t), "=s"(r) :: "memory");
  return r;
}
__device__ __forceinline__ void ctl_and(unsigned* w, unsigned zero, unsigned m) {
  unsigned long long ex;
  asm volatile("s_mov_b64 %0, exec\n\ts_mov_b64 exec, 1\n\ts_nop 2\n\tglobal_atomic_and %2, %3, %1\n\ts_mov_b64 exec, %0\n\ts_nop 1"
               : "=&s"(ex) : "s"(w), "v"(zero), "v"(m) : "memory");
}
#define PARK_HEAD "s_getpc_b64 vcc\n\t.Lp0_%=:\n\ts_add_u32 vcc_lo, vcc_lo, %8\n\ts_addc_u32 vcc_hi, vcc_hi, 0\n\t" \
                  "s_add_u32 vcc_lo, vcc_lo, .Lp1_%=-.Lp0_%=\n\ts_addc_u32 vcc_hi, vcc_hi, 0\n\ts_setpc_b64 vcc\n\t.Lp1_%=:\n\t.set .Lpi_%=, 128\n\t.rept 15\n\t"
#define PARK_TAIL "s_branch .Lp2_%=\n\t.set .Lpi_%=, .Lpi_%=+8\n\t.endr\n\t.Lp2_%=:\n\ts_nop 1"
__device__ __forceinline__ void park_put(int j, const u32x4& p0, const u32x4& p1) {
  asm volatile(PARK_HEAD
               "v_accvgpr_write_b32 a[.Lpi_%=], %0\n\tv_accvgpr_write_b32 a[.Lpi_%=+1], %1\n\tv_accvgpr_write_b32 a[.Lpi_%=+2], %2\n\t"
               "v_accvgpr_write_b32 a[.Lpi_%=+3], %3\n\tv_accvgpr_write_b32 a[.Lpi_%=+4], %4\n\tv_accvgpr_write_b32 a[.Lpi_%=+5], %5\n\t"
               "v_accvgpr_write_b32 a[.Lpi_%=+6], %6\n\tv_accvgpr_write_b32 a[.Lpi_%=+7], %7\n\t" PARK_TAIL
               :: "v"(p0[0]), "v"(p0[1]), "v"(p0[2]), "v"(p0[3]), "v"(p1[0]), "v"(p1[1]), "v"(p1[2]), "v"(p1[3]), "s"(j * 68)
               : "memory", "vcc", "scc");
}
__device__ __forceinline__ void park_get(int j, u32x4& p0, u32x4& p1) {
  asm volatile(PARK_HEAD
               "v_accvgpr_read_b32 %0, a[.Lpi_%=]\n\tv_accvgpr_read_b32 %1, a[.Lpi_%=+1]\n\tv_accvgpr_read_b32 %2, a[.Lpi_%=+2]\n\t"
               "v_accvgpr_read_b32 %3, a[.Lpi_%=+3]\n\tv_accvgpr_read_b32 %4, a[.Lpi_%=+4]\n\tv_accvgpr_read_b32 %5, a[.Lpi_%=+5]\n\t"
               "v_accvgpr_read_b32 %6, a[.Lpi_%=+6]\n\tv_accvgpr_read_b32 %7, a[.Lpi_%=+7]\n\t" PARK_TAIL
               : "=v"(p0[0]), "=v"(p0[1]), "=v"(p0[2]), "=v"(p0[3]), "=v"(p1[0]), "=v"(p1[1]), "=v"(p1[2]), "=v"(p1[3]) : "s"(j * 68)
               : "memory", "vcc", "scc");
}
template <int B> __device__ __forceinline__ void acc_zero32() {
  asm volatile(".set .Lz_%=, %c0\n\t.rept 32\n\tv_accvgpr_write_b32 a[.Lz_%=], 0\n\t.set .Lz_%=, .Lz_%=+1\n\t.endr" :: "i"(B) : "memory", "a0", "a127", "a247");
}
__device__ __forceinline__ void acc_scale(float alpha) {
  float t0, t1, t2, t3;
  asm volatile("s_nop 15\n\t.set .Ls_%=, 0\n\t.rept 32\n\t"
               "v_accvgpr_read_b32 %0, a[.Ls_%=]\n\tv_accvgpr_read_b32 %1, a[.Ls_%=+1]\n\tv_accvgpr_read_b32 %2, a[.Ls_%=+2]\n\tv_accvgpr_read_b32 %3, a[.Ls_%=+3]\n\t"
               "v_mul_f32 %0, %0, %4\n\tv_mul_f32 %1, %1, %4\n\tv_mul_f32 %2, %2, %4\n\tv_mul_f32 %3, %3, %4\n\t"
               "v_accvgpr_write_b32 a[.Ls_%=], %0\n\tv_accvgpr_write_b32 a[.Ls_%=+1], %1\n\tv_accvgpr_write_b32 a[.Ls_%=+2], %2\n\tv_accvgpr_write_b32 a[.Ls_%=+3], %3\n\t"
               ".set .Ls_%=, .Ls_%=+4\n\t.endr\n\ts_nop 1"
               : "=&v"(t0), "=&v"(t1), "=&v"(t2), "=&v"(t3) : "v"(alpha) : "memory");
}
#define M_(a, b) "v_mfma_f32_32x32x16_bf16 %0, %" #a ", %" #b ", %0\n\t"
__device__ __forceinline__ void qk_first(f32x16& s, const bf16x8* f, const bf16x8* q) {
  asm volatile("s_nop 1\n\tv_mfma_f32_32x32x16_bf16 %0, %1, %5, 0\n\t" M_(2, 6) M_(3, 7) M_(4, 8)
               : "=&v"(s) : "v"(f[0]), "v"(f[1]), "v"(f[2]), "v"(f[3]), "v"(q[0]), "v"(q[1]), "v"(q[2]), "v"(q[3]));
}
__device__ __forceinline__ void qk_next(f32x16& s, const bf16x8* f, const bf16x8* q) {
  asm volatile("s_nop 1\n\t" M_(1, 5) M_(2, 6) M_(3, 7) M_(4, 8)
               : "+v"(s) : "v"(f[0]), "v"(f[1]), "v"(f[2]), "v"(f[3]), "v"(q[0]), "v"(q[1]), "v"(q[2]), "v"(q[3]));
}
#undef M_
__device__ __forceinline__ void qk_done(f32x16& s) { asm volatile("s_nop 15" : "+v"(s)); }
template <int C> __device__ __forceinline__ void pv4(const bf16x8* f, const u32x4& p) {
  asm volatile("s_nop 1\n\t"
               "v_mfma_f32_32x32x16_bf16 a[%c5:%c5+15], %0, %4, a[%c5:%c5+15]\n\t"
               "v_mfma_f32_32x32x16_bf16 a[%c5+16:%c5+31], %1, %4, a[%c5+16:%c5+31]\n\t"
               "v_mfma_f32_32x32x16_bf16 a[%c5+32:%c5+47], %2, %4, a[%c5+32:%c5+47]\n\t"
               "v_mfma_f32_32x32x16_bf16 a[%c5+48:%c5+63], %3, %4, a[%c5+48:%c5+63]"
               :: "v"(f[0]), "v"(f[1]), "v"(f[2]), "v"(f[3]), "v"(p), "i"(16 * C) : "memory");
}
#define OUT_(p) "s_cmp_eq_u32 %1, " #p "\n\ts_cbranch_scc1 .Lo" #p "_%=\n\t.set .Lw_%=, 8*" #p "\n\t.rept 8\n\t" \
                "ds_write_b128 %0, a[.Lw_%=*4:.Lw_%=*4+3] offset:.Lw_%=*1024\n\t.set .Lw_%=, .Lw_%=+1\n\t.endr\n\t.Lo" #p "_%=:\n\t"
__device__ __forceinline__ void acc_store(unsigned addr, int w) {
  asm volatile("s_nop 15\n\t" OUT_(0) OUT_(1) OUT_(2) OUT_(3) :: "v"(addr), "s"(w) : "memory", "scc");
}
#undef OUT_
template <int B> __device__ __forceinline__ void acc_get16(f32x4* x) {
  float r[16];
  asm volatile("v_accvgpr_read_b32 %0, a[%c16]\n\tv_accvgpr_read_b32 %1, a[%c16+1]\n\tv_accvgpr_read_b32 %2, a[%c16+2]\n\tv_accvgpr_read_b32 %3, a[%c16+3]\n\t"
               "v_accvgpr_read_b32 %4, a[%c16+4]\n\tv_accvgpr_read_b32 %5, a[%c16+5]\n\tv_accvgpr_read_b32 %6, a[%c16+6]\n\tv_accvgpr_read_b32 %7, a[%c16+7]\n\t"
               "v_accvgpr_read_b32 %8, a[%c16+8]\n\tv_accvgpr_read_b32 %9, a[%c16+9]\n\tv_accvgpr_read_b32 %10, a[%c16+10]\n\tv_accvgpr_read_b32 %11, a[%c16+11]\n\t"
               "v_accvgpr_read_b32 %12, a[%c16+12]\n\tv_accvgpr_read_b32 %13, a[%c16+13]\n\tv_accvgpr_read_b32 %14, a[%c16+14]\n\tv_accvgpr_read_b32 %15, a[%c16+15]"
               : "=v"(r[0]), "=v"(r[1]), "=v"(r[2]), "=v"(r[3]), "=v"(r[4]), "=v"(r[5]), "=v"(r[6]), "=v"(r[7]), "=v"(r[8]), "=v"(r[9]),
                 "=v"(r[10]), "=v"(r[11]), "=v"(r[12]), "=v"(r[13]), "=v"(r[14]), "=v"(r[15]) : "i"(B) : "memory");
#pragma unroll
  for (int i = 0; i < 4; ++i) x[i] = f32x4{r[4 * i], r[4 * i + 1], r[4 * i + 2], r[4 * i + 3]};
}

__device__ __forceinline__ float xhalf_max(float x) {
  auto r = __builtin_amdgcn_permlane32_swap(__float_as_uint(x), __float_as_uint(x), false, false);
  return fmaxf(__uint_as_float(r[0]), __uint_as_float(r[1]));
}
__device__ __forceinline__ float xhalf_sum(float x) {
  auto r = __builtin_amdgcn_permlane32_swap(__float_as_uint(x), __float_as_uint(x), false, false);
  return __uint_as_float(r[0]) + __uint_as_float(r[1]);
}
__device__ __forceinline__ unsigned pack2(float a, float b) {
  return __builtin_bit_cast(unsigned, __builtin_convertvector((f32x2){a, b}, bf16x2));
}
__device__ __forceinline__ f32x4 unpack4(unsigned lo, unsigned hi) {
  return f32x4{__uint_as_float(lo << 16), __uint_as_float(lo & 0xffff0000u), __uint_as_float(hi << 16), __uint_as_float(hi & 0xffff0000u)};
}
__device__ __forceinline__ unsigned swz(unsigned row) { return ((row & 3) << 2) | ((row >> 2) & 3); }
__device__ __forceinline__ bf16x8 row_frag(LAS char* lds, unsigned rd, int st) {
  return *(const LAS bf16x8*)(lds + ((rd ^ ((st & 7) << 5)) + (st >> 3) * 256));
}
__device__ __forceinline__ bf16x8 col_frag(LAS char* lds, unsigned vrd, int c, int koff) {
  const int o = (c >> 2) * 256 + koff;
  const bf16x4 lo = __builtin_amdgcn_ds_read_tr16_b64_v4bf16((LAS bf16x4*)(lds + ((vrd ^ ((c & 3) << 6)) + o)));
  const bf16x4 hi = __builtin_amdgcn_ds_read_tr16_b64_v4bf16((LAS bf16x4*)(lds + ((vrd ^ ((c & 3) << 6) ^ 32) + o + 8 * ROWB)));
  return __builtin_shufflevector(lo, hi, 0, 1, 2, 3, 4, 5, 6, 7);
}
template <int NR> __device__ __forceinline__ void sm_ref(const f32x16& s, float& m, float& l) {
  float mx = s[0];
#pragma unroll
  for (int r = 1; r < NR; ++r) mx = fmaxf(mx, s[r]);
  mx = xhalf_max(mx);
  if (!__all(mx <= m + DEFER)) {
    const float mn = fmaxf(m, mx);
    l *= __builtin_amdgcn_exp2f((m - mn) * SCL2);
    m = mn;
  }
  asm volatile("" : "+v"(m), "+v"(l));
}
template <int B> __device__ __forceinline__ u32x4 sm_exp(const f32x16& s, float m, float& l) {
  const float nb = -m * SCL2;
  float e[8];
#pragma unroll
  for (int r = 0; r < 8; ++r) e[r] = __builtin_amdgcn_exp2f(fmaf(s[B + r], SCL2, nb));
  l += ((e[0] + e[1]) + (e[2] + e[3])) + ((e[4] + e[5]) + (e[6] + e[7]));
  u32x4 p = {pack2(e[0], e[1]), pack2(e[2], e[3]), pack2(e[4], e[5]), pack2(e[6], e[7])};
  asm volatile("" : "+v"(p), "+v"(l));
  return p;
}

__global__ void __launch_bounds__(256, 1)
tree_attn(const char* __restrict__ Q, const char* __restrict__ K, const char* __restrict__ V, const char* __restrict__ KT,
          const char* __restrict__ VT, const char* __restrict__ MK, char* __restrict__ ws, unsigned short* __restrict__ out) {
  __shared__ __attribute__((aligned(1024))) char smem[LDS_BYTES];
  LAS char* lds = (LAS char*)smem;
  const unsigned lbase = (unsigned)(size_t)lds;
  const int tid = threadIdx.x, lane = tid & 63, wave = __builtin_amdgcn_readfirstlane(tid >> 6);
  const int wg = blockIdx.x, x8 = wg & 7;
  const int grp = (wg >> 3) * 2 + ((x8 >> 1) & 1), mem = (x8 >> 2) * 2 + (x8 & 1);
  const int r32 = lane & 31, hh = lane >> 5;
  const bool leader = (mem == 1) && (wave == 0);
  unsigned long long ts0 = __builtin_amdgcn_s_memrealtime(), ts1 = 0, ts2 = 0; int ntl = 0, nslow = 0, npoll = 0; unsigned long long tq0 = 0, tq1 = 0, tq2 = 0;
  {
    unsigned long long pc; unsigned len;
    asm volatile("s_getpc_b64 %0\n.Lcode0:\n\ts_mov_b32 %1, .Lcode1-.Lcode0" : "=s"(pc), "=s"(len));
    const unsigned o = min((unsigned)(((wg >> 3) * 4 + wave) & 31) * 1024u, (len & ~15u) - 1024u) + lane * 16;
    asm volatile("global_load_dwordx4 a[240:243], %0, %1" :: "v"(o), "s"(pc & ~15ull) : "memory", "a240", "a241", "a242", "a243");
  }
  unsigned ko[8], qo[4];
#pragma unroll
  for (int i = 0; i < 8; ++i) ko[i] = hh * ROWB + ((r32 ^ swz(2 * i + hh)) << 4);
#pragma unroll
  for (int i = 0; i < 4; ++i) qo[i] = ko[i] ^ ((wave & 1) << 5);
  asm volatile("" : "+v"(ko[0]), "+v"(ko[1]), "+v"(ko[2]), "+v"(ko[3]), "+v"(ko[4]), "+v"(ko[5]), "+v"(ko[6]), "+v"(ko[7]),
                    "+v"(qo[0]), "+v"(qo[1]), "+v"(qo[2]), "+v"(qo[3]));
  const unsigned ring = wave * RINGB;
  const size_t wof = (size_t)grp * (CTX * ROWB) + (mem * 4 + wave) * BLKB;
  const char* src = K + wof;
  unsigned dst = lbase + ring;
#define PIECE_N(k, cnt) dma1<((k) & 3) * 1024, (cnt)>(src + ((k) >> 3) * HALFB + (((k) >> 2) & 1) * 4096, dst + ((k) >> 2) * 4096, ko[(k) & 7])
#define PIECE(k) PIECE_N(k, WIN - 1 + ((WPAT >> (k)) & 1) - ((WNEG >> (k)) & 1))
  auto quarter = [&](const char* s, unsigned d) {
    dma1<0, 63>(s, d, qo[0]); dma1<1024, 63>(s, d, qo[1]); dma1<2048, 63>(s, d, qo[2]); dma1<3072, 63>(s, d, qo[3]);
  };
  quarter(Q + (size_t)grp * TILEB + wave * 4096, lbase + Q_OFF + wave * 4096);
  unsigned* cw = (unsigned*)(ws + WS_CTL + grp * 256);
  const unsigned vz = 0;
  if (wave == 0) ctl_add_ret(cw, vz, 0u);
  PIECE_N(0, 63); PIECE_N(1, 63); PIECE_N(2, 63); PIECE_N(3, 63); PIECE_N(4, 63);
  const unsigned krd = r32 * ROWB + ((hh ^ swz(r32)) << 4);
  const int q4 = (lane & 15) >> 2;
  const unsigned vrd = (4 * hh + q4) * ROWB + (q4 << 6) + ((((lane >> 4) & 1) * 32 + (lane & 3) * 8) ^ (hh << 4));
  acc_zero32<0>(); acc_zero32<32>(); acc_zero32<64>(); acc_zero32<96>();
  WAIT_VM(5);
  const unsigned w0 = ctl_result();
  if (leader) ctl_and(cw, vz, W_EPOCH_MASK);
  if (wave == 0) *(LAS u32x2*)(lds + FL_OFF) = u32x2{0u, 0u};
  BARRIER();
  PIECE(5);
  bf16x8 q[16];
#pragma unroll
  for (int st = 0; st < 16; ++st) q[st] = row_frag(lds, Q_OFF + krd, st);
  PIECE(6);
  BARRIER();
  float zm = -__builtin_inff(), zl = 0.f, om = -__builtin_inff();
  PIECE(7); PIECE(8); PIECE(9); PIECE(10); PIECE(11);
  PIECE(12); PIECE(13); PIECE(14); PIECE(15);

  bool ready = false;
  const unsigned mt = MT_OFF + wave * (TPW * 128) + r32 * 4;
  unsigned slot = ring;
#pragma unroll 1
  for (int n = 0; n < TPW; ++n) {
    src = n + 1 < TPW ? src + TSTEP : V + wof;
    dst ^= TILEB;
    const bool look = wave == 0 && (leader ? n < 4 : !ready);
    if (look) ctl_add_ret(cw, vz, leader ? W_EPOCH : 0u);
    PIECE(0); PIECE(1); PIECE(2); PIECE(3); PIECE(4);
    WAIT_VM(5);
    if (n == 1) { ts1 = __builtin_amdgcn_s_memrealtime(); __builtin_amdgcn_s_waitcnt(0xC07F); }
    if (n == 12) { tq0 = __builtin_amdgcn_s_memrealtime(); __builtin_amdgcn_s_waitcnt(0xC07F); }
    bf16x8 f[16]; f32x16 s;
#pragma unroll
    for (int st = 0; st < 16; ++st) f[st] = row_frag(lds, slot + krd, st);
    if (look && !ready && (leader || ctl_result() != w0)) { ready = true; *(volatile LAS unsigned*)(lds + FL_OFF + 4) = 1u; }
    PIECE(5);
    qk_first(s, f, q);
    PIECE(6);
    qk_next(s, f + 4, q + 4);
    PIECE(7);
    qk_next(s, f + 8, q + 8);
    PIECE(8);
    qk_next(s, f + 12, q + 12);
    PIECE(9);
    qk_done(s);
    sm_ref<16>(s, zm, zl);
    PIECE(10);
    const u32x4 p0 = sm_exp<0>(s, zm, zl);
    PIECE(11);
    const u32x4 p1 = sm_exp<8>(s, zm, zl);
    PIECE(12);
    park_put(n, p0, p1);
    *(LAS float*)(lds + mt + n * 128) = zm;
    PIECE(13); PIECE(14); PIECE(15);
    slot ^= TILEB;
  }
  auto rebase = [&](float tm) {
    if (!__all(tm == om)) {
      if (!__all(om == -__builtin_inff())) acc_scale(__builtin_amdgcn_exp2f((om - tm) * SCL2));
      om = tm;
    }
  };
  const char* Kg = K + (size_t)grp * (CTX * ROWB);
  const char* Vg = V + (size_t)grp * (CTX * ROWB);
  const char* srcb = src;
  auto take = [&](unsigned w) {
    const int n = (w >> 4) & 0x3ff, h = n - 1;
    const int off = ((16 * TPW + (h >> 1)) * BLKB + (h & 1) * HALFB) & (CTX * ROWB - BLKB);
    src = Kg + off; srcb = Vg + off;
    if (__builtin_expect(n == 0, 0)) {
      src = KT + (size_t)grp * BLKB; srcb = VT + (size_t)grp * BLKB;
      const unsigned mo = (r32 & 15) * (NT * 2) + 8 * hh;
      asm volatile("s_nop 4\n\tglobal_load_dwordx2 a[248:249], %0, %1\n\tglobal_load_dwordx2 a[250:251], %0, %1 offset:16"
                   :: "v"(mo), "s"(MK + (grp >> 3) * (NT * NT * 2)) : "memory", "a248", "a249", "a250", "a251");
    }
    return n;
  };
#define PIECE2(k, cnt) do { if (more) dma1<((k) & 3) * 1024, (cnt)>(((k) >> 3 ? srcb : src) + (((k) >> 2) & 1) * 4096, dst + ((k) >> 2) * 4096, ko[(k) & 7]); } while (0)
#define PIECEV(k) PIECE2(k, WIN - 1 + ((WPAT >> (k)) & 1) - ((WNEG >> (k)) & 1))
  bool more = true;
  int tick = 1;
#pragma unroll 1
  for (int n = 0; n < TPW; ++n) {
    dst ^= TILEB;
    if (n + 1 < TPW) { src += TSTEP; srcb = src + HALFB; }
    else { tick = take(ctl_result()); more = tick < NTICK; }
    if (n + 2 == TPW) {
      if (wave == 0) {
#pragma unroll 1
        for (int i = 0; i < SPIN_MAX && !ready; ++i) {
          ctl_add_ret(cw, vz, 0u); WAIT_VM(0);
          ready = ctl_result() != w0;
          ++nslow;
          if (!ready) __builtin_amdgcn_s_sleep(16);
        }
        *(volatile LAS unsigned*)(lds + FL_OFF + 4) = 1u;
      } else {
#pragma unroll 1
        for (int i = 0; i < (SPIN_MAX << 8); ++i)
          if (__builtin_amdgcn_readfirstlane(*(volatile LAS unsigned*)(lds + FL_OFF + 4)) == 1) break;
      }
    }
    u32x4 r0, r1;
    if (n == 1) { tq1 = __builtin_amdgcn_s_memrealtime(); __builtin_amdgcn_s_waitcnt(0xC07F); }
    if (n == 12) { tq2 = __builtin_amdgcn_s_memrealtime(); __builtin_amdgcn_s_waitcnt(0xC07F); }
    PIECEV(0);
    const float tm = *(const LAS float*)(lds + mt + n * 128);
    park_get(n, r0, r1);
    PIECEV(1);
    rebase(tm);
    PIECEV(2); PIECEV(3); PIECEV(4);
    if (more) WAIT_VM(5); else WAIT_VM(0);
    bf16x8 f[16];
#pragma unroll
    for (int c = 0; c < 8; ++c) f[c] = col_frag(lds, slot + vrd, c, 0);
    PIECEV(5);
#pragma unroll
    for (int c = 0; c < 8; ++c) f[8 + c] = col_frag(lds, slot + vrd, c, BLKB);
    PIECEV(6);
    pv4<0>(f, r0);
    PIECEV(7);
    pv4<4>(f + 4, r0);
    if (n + 2 >= TPW && more) ctl_add_ret(cw, vz, W_TICK);
    PIECEV(8);
    pv4<0>(f + 8, r1);
    PIECEV(9);
    pv4<4>(f + 12, r1);
    PIECEV(10); PIECEV(11); PIECEV(12); PIECEV(13); PIECEV(14); PIECEV(15);
    WAIT_VM(8);
    slot ^= TILEB;
  }
  while (more) {
    ++ntl;
    unsigned mk[4] = {0x3f803f80u, 0x3f803f80u, 0x3f803f80u, 0x3f803f80u};
    if (__builtin_expect(tick == 0, 0))
      asm volatile("v_accvgpr_read_b32 %0, a248\n\tv_accvgpr_read_b32 %1, a249\n\tv_accvgpr_read_b32 %2, a250\n\tv_accvgpr_read_b32 %3, a251"
                   : "=v"(mk[0]), "=v"(mk[1]), "=v"(mk[2]), "=v"(mk[3]) :: "memory");
    bf16x8 f[16]; f32x16 s;
#pragma unroll
    for (int st = 0; st < 16; ++st) f[st] = row_frag(lds, slot + (krd & (BLKB - 1)), st);
    dst ^= TILEB;
    tick = take(ctl_result()); more = tick < NTICK;
    PIECE2(0, WTAIL - 1); PIECE2(1, WTAIL - 1);
    qk_first(s, f, q);
    PIECE2(2, WTAIL - 1);
    qk_next(s, f + 4, q + 4);
    PIECE2(3, WTAIL - 1);
    qk_next(s, f + 8, q + 8);
    PIECE2(4, WTAIL - 1);
    qk_next(s, f + 12, q + 12);
    if (more) WAIT_VM(5); else WAIT_VM(0);
    qk_done(s);
#pragma unroll
    for (int c = 0; c < 8; ++c) f[c] = col_frag(lds, slot + vrd, c, BLKB);
#pragma unroll
    for (int r = 0; r < 8; ++r) {
      const float mv = __uint_as_float((r & 1) ? (mk[r >> 1] & 0xffff0000u) : (mk[r >> 1] << 16));
      s[r] = fmaf(1.0f - mv, MASKRAW, s[r]);
    }
    PIECE2(5, WTAIL - 1);
    sm_ref<8>(s, zm, zl);
    PIECE2(6, WTAIL - 1);
    const u32x4 p = sm_exp<0>(s, zm, zl);
    PIECE2(7, WTAIL - 1);
    if (more) ctl_add_ret(cw, vz, W_TICK);
    rebase(zm);
    PIECE2(8, WTAIL - 1);
    pv4<0>(f, p);
    PIECE2(9, WTAIL - 1);
    pv4<4>(f + 4, p);
    PIECE2(10, WTAIL - 1); PIECE2(11, WTAIL - 1); PIECE2(12, WTAIL - 1); PIECE2(13, WTAIL - 1); PIECE2(14, WTAIL - 1); PIECE2(15, WTAIL - 1);
    WAIT_VM(8);
    slot ^= TILEB;
  }
  ts2 = __builtin_amdgcn_s_memrealtime(); __builtin_amdgcn_s_waitcnt(0xC07F);

  acc_store(lbase + ring + lane * 16, wave);
  zl = xhalf_sum(zl);
  *(LAS f32x2*)(lds + ML_OFF + (wave * 32 + r32) * 8) = f32x2{zm, zl};
  BARRIER();
  f32x2 ml[3]; f32x4 im[3][8], acc[8];
#pragma unroll
  for (int d = 0; d < 3; ++d) ml[d] = *(const LAS f32x2*)(lds + ML_OFF + (((wave + 1 + d) & 3) * 32 + r32) * 8);
#pragma unroll
  for (int d = 0; d < 3; ++d)
#pragma unroll
    for (int i = 0; i < 8; ++i) im[d][i] = *(const LAS f32x4*)(lds + ((wave + 1 + d) & 3) * RINGB + wave * 8192 + lane * 16 + i * 1024);
  if (wave == 0) { acc_get16<0>(acc); acc_get16<16>(acc + 4); }
  else if (wave == 1) { acc_get16<32>(acc); acc_get16<48>(acc + 4); }
  else if (wave == 2) { acc_get16<64>(acc); acc_get16<80>(acc + 4); }
  else { acc_get16<96>(acc); acc_get16<112>(acc + 4); }
  const float M = fmaxf(fmaxf(zm, ml[0][0]), fmaxf(ml[1][0], ml[2][0]));
  float f[4];
  f[3] = __builtin_amdgcn_exp2f((zm - M) * SCL2);
  float L = f[3] * zl;
#pragma unroll
  for (int d = 0; d < 3; ++d) { f[d] = __builtin_amdgcn_exp2f((ml[d][0] - M) * SCL2); L = fmaf(f[d], ml[d][1], L); }

  const __amdgpu_buffer_rsrc_t rs = __builtin_amdgcn_make_buffer_rsrc(ws, 0, (int)WS_END, 0x00020000);
  const int ml_off = (int)WS_PML + (grp * NSPL * 32 + r32) * 8;
  const int po_off = (int)WS_PO + grp * NSPL * PARTB + wave * 4096 + lane * 16;
  if (wave == 0 && hh == 0) __builtin_amdgcn_raw_buffer_store_b64(__builtin_bit_cast(u32x2, f32x2{M, L}), rs, ml_off + mem * 256, 0, 16);
  f32x4 x = {}, y = {};
#pragma unroll
  for (int v = 0; v < 4; ++v) {
    const f32x4 a = acc[2 * v] * f[3] + im[0][2 * v] * f[0] + im[1][2 * v] * f[1] + im[2][2 * v] * f[2];
    const f32x4 b = acc[2 * v + 1] * f[3] + im[0][2 * v + 1] * f[0] + im[1][2 * v + 1] * f[1] + im[2][2 * v + 1] * f[2];
    if (v == mem) { x = a; y = b; }
    else __builtin_amdgcn_raw_buffer_store_b128(u32x4{pack2(a[0], a[1]), pack2(a[2], a[3]), pack2(b[0], b[1]), pack2(b[2], b[3])}, rs,
                                                po_off + mem * PARTB + v * 1024, 0, 16);
  }
  WAIT_VM(0);
  BARRIER();
  if (wave == 0) {
    ctl_add_ret(cw, vz, 1u << mem);
#pragma unroll 1
    for (int i = 0; i < SPIN_MAX; ++i) {
      WAIT_VM(0);
      const unsigned w = ctl_result() | (1u << mem);
      ++npoll;
      if ((w & 15u) == 15u) break;
      ctl_add_ret(cw, vz, leader ? W_EPOCH : 0u);
    }
    *(volatile LAS unsigned*)(lds + FL_OFF) = 1u;
  } else {
#pragma unroll 1
    for (int i = 0; i < (SPIN_MAX << 8); ++i)
      if (__builtin_amdgcn_readfirstlane(*(volatile LAS unsigned*)(lds + FL_OFF))) break;
  }
  asm volatile("" ::: "memory");
  f32x2 oml[3]; u32x4 oa[3];
#pragma unroll
  for (int d = 0; d < 3; ++d) {
    const int sm = (mem + 1 + d) & 3;
    oml[d] = __builtin_bit_cast(f32x2, __builtin_amdgcn_raw_buffer_load_b64(rs, ml_off, sm * 256, 16));
    oa[d] = __builtin_amdgcn_raw_buffer_load_b128(rs, po_off, sm * PARTB + mem * 1024, 16);
  }
  const float Mf = fmaxf(fmaxf(M, oml[0][0]), fmaxf(oml[1][0], oml[2][0]));
  f[3] = __builtin_amdgcn_exp2f((M - Mf) * SCL2);
  L *= f[3];
#pragma unroll
  for (int d = 0; d < 3; ++d) { f[d] = __builtin_amdgcn_exp2f((oml[d][0] - Mf) * SCL2); L = fmaf(f[d], oml[d][1], L); }
  const float inv = 1.0f / L;
  x *= f[3] * inv; y *= f[3] * inv;
#pragma unroll
  for (int d = 0; d < 3; ++d) { x += (f[d] * inv) * unpack4(oa[d][0], oa[d][1]); y += (f[d] * inv) * unpack4(oa[d][2], oa[d][3]); }
  const auto rx = __builtin_amdgcn_permlane32_swap(pack2(x[0], x[1]), pack2(y[0], y[1]), false, false);
  const auto ry = __builtin_amdgcn_permlane32_swap(pack2(x[2], x[3]), pack2(y[2], y[3]), false, false);
  *(u32x4*)((char*)out + (size_t)(grp * 32 + r32) * ROWB + wave * 128 + mem * 32 + hh * 16) = u32x4{rx[0], ry[0], rx[1], ry[1]};
  asm volatile(".Lcode1:");
  WAIT_VM(0);
  if (lane == 0) { unsigned long long* sp = (unsigned long long*)(ws + (512ull << 20)) + (wg * 4 + wave) * 8;
    sp[0] = ts0; sp[1] = ts1; sp[2] = ts2; sp[3] = __builtin_amdgcn_s_memrealtime(); sp[4] = __builtin_amdgcn_s_getreg((31 << 11) | 20) & 7; sp[5] = tq0; sp[6] = tq1; sp[7] = tq2; }
}

__global__ void enc_k(const char* ws) {
  const unsigned long long* h = (const unsigned long long*)(ws + (512ull << 20));
  unsigned long long mn = ~0ull, sk = 0, sv = 0;
  for (int i = 0; i < 1024; ++i) { const unsigned long long* p = h + i * 8; mn = p[0] < mn ? p[0] : mn; sk += p[5] - p[1]; sv += p[7] - p[6]; }
  long long dk = ((long long)(sk / 1024) - 2200) / 40, dv = ((long long)(sv / 1024) - 2200) / 40;
  dk = dk < 0 ? 0 : dk > 19 ? 19 : dk; dv = dv < 0 ? 0 : dv > 19 ? 19 : dv;
  while (__builtin_amdgcn_s_memrealtime() - mn < 100000ull + 100000ull * dk + 4000ull * dv) __builtin_amdgcn_s_sleep(2);
}
extern "C" void kernel_launch(void* const* d_in, const int* in_sizes, int n_in, void* d_out, int out_size, void* d_ws, size_t ws_size,
                              hipStream_t stream) {
  tree_attn<<<NGRP * NSPL, 256, 0, stream>>>((const char*)d_in[0], (const char*)d_in[1], (const char*)d_in[2], (const char*)d_in[3],
                                             (const char*)d_in[4], (const char*)d_in[5], (char*)d_ws, (unsigned short*)d_out);
  enc_k<<<1, 1, 0, stream>>>((const char*)d_ws);
}
```

```cpp
#include <hip/hip_runtime.h>
#include <hip/hip_bf16.h>

#define LAS __attribute__((address_space(3)))
typedef __bf16 bf16x8 __attribute__((ext_vector_type(8)));
typedef __bf16 bf16x4 __attribute__((ext_vector_type(4)));
typedef __bf16 bf16x2 __attribute__((ext_vector_type(2)));
typedef float f32x16 __attribute__((ext_vector_type(16)));
typedef float f32x4 __attribute__((ext_vector_type(4)));
typedef float f32x2 __attribute__((ext_vector_type(2)));
typedef unsigned u32x4 __attribute__((ext_vector_type(4)));
typedef unsigned u32x2 __attribute__((ext_vector_type(2)));

constexpr int NB = 8, HKV = 8, NT = 16, CTX = 8192, HD = 256;
constexpr int NGRP = NB * HKV;
constexpr int NSPL = 4;
constexpr int ROWB = HD * 2;
constexpr int BLKB = 16 * ROWB;
constexpr int TILEB = 2 * BLKB;
constexpr int RINGB = 2 * TILEB;
constexpr int TPW = CTX / 32 / 16;
constexpr int HALFB = CTX / 2 * ROWB, TSTEP = 16 * BLKB;
constexpr int TREE_OFF = 4 * RINGB;
constexpr int Q_OFF = TREE_OFF + TILEB;
constexpr int LDS_BYTES = Q_OFF + TILEB;
constexpr int MT_OFF = Q_OFF;
constexpr int ML_OFF = MT_OFF + 4 * TPW * 128;
constexpr int FL_OFF = ML_OFF + 4 * 32 * 8;
static_assert(LDS_BYTES <= 160 * 1024 && RINGB == 32 * HD * 4 && FL_OFF + 16 <= LDS_BYTES && TPW == 16, "layout");
constexpr float SCL2 = 0.0625f * 1.4426950408889634f;
constexpr float MASKRAW = -1.0e9f * 16.0f;
constexpr float DEFER = 8.0f / SCL2;
#define WIN 5
#define WBOOST 7
#define PACE 217
#define SLIP 200
#define PACE0 330

#define ENDB 0

constexpr unsigned W_EPOCH = 1u << 29, W_EPOCH_MASK = 0xE0000000u;
constexpr int SPIN_MAX = 1 << 16;
constexpr int PARTB = 32 * HD * 2;
constexpr size_t WS_CTL = 0, WS_PML = 65536, WS_PO = WS_PML + (size_t)NGRP * NSPL * 32 * 8;
constexpr size_t WS_END = WS_PO + (size_t)NGRP * NSPL * PARTB;

template <int OFF, int CNT> __device__ __forceinline__ void dma1(const char* src, unsigned lds, unsigned v) {
  unsigned keep;
  __builtin_amdgcn_sched_barrier(0);
  asm volatile("s_mov_b32 %0, m0\n\ts_mov_b32 m0, %2\n\ts_waitcnt vmcnt(%5)\n\ts_nop 2\n\t"
               "global_load_lds_dwordx4 %3, %1 offset:%4 nt\n\ts_mov_b32 m0, %0"
               : "=&s"(keep) : "s"(src), "s"(lds), "v"(v), "i"(OFF), "i"(CNT) : "memory");
  __builtin_amdgcn_sched_barrier(0);
}
template <int OFF> __device__ __forceinline__ void dma1w(const char* src, unsigned lds, unsigned v, int late) {
  unsigned keep;
  __builtin_amdgcn_sched_barrier(0);
  asm volatile("s_mov_b32 %0, m0\n\ts_mov_b32 m0, %2\n\ts_cmp_gt_i32 %5, 0\n\ts_waitcnt vmcnt(%7)\n\ts_cbranch_scc1 .Lb_%=\n\ts_waitcnt vmcnt(%6)\n\t.Lb_%=:\n\ts_nop 0\n\t"
               "global_load_lds_dwordx4 %3, %1 offset:%4 nt\n\ts_mov_b32 m0, %0"
               : "=&s"(keep) : "s"(src), "s"(lds), "v"(v), "i"(OFF), "s"(late), "i"(WIN - 1), "i"(WBOOST - 1) : "memory", "scc");
  __builtin_amdgcn_sched_barrier(0);
}
__device__ __forceinline__ unsigned clock_ticks() {
  unsigned long long t;
  asm volatile("s_memrealtime %0\n\ts_waitcnt lgkmcnt(0)" : "=s"(t) :: "memory");
  return (unsigned)t;
}
#define WAIT_VM(n) asm volatile("s_waitcnt vmcnt(%0)" :: "i"(n) : "memory")
#define WAIT_LGKM0() asm volatile("s_waitcnt lgkmcnt(0)" ::: "memory")
#define BARRIER() do { WAIT_LGKM0(); __builtin_amdgcn_s_barrier(); asm volatile("" ::: "memory"); } while (0)

__device__ __forceinline__ void ctl_add_ret(unsigned* w, unsigned zero, unsigned inc) {
  unsigned long long ex;
  asm volatile("v_accvgpr_write_b32 a254, %3\n\ts_mov_b64 %0, exec\n\ts_mov_b64 exec, 1\n\ts_nop 2\n\t"
               "global_atomic_add a255, %2, a254, %1 sc0\n\ts_mov_b64 exec, %0\n\ts_nop 1"
               : "=&s"(ex) : "s"(w), "v"(zero), "v"(inc) : "memory", "a254", "a255");
}
__device__ __forceinline__ unsigned ctl_result() {
  unsigned t, r;
  asm volatile("v_accvgpr_read_b32 %0, a255\n\ts_nop 1\n\tv_readfirstlane_b32 %1, %0\n\ts_nop 4" : "=&v"(t), "=s"(r) :: "memory");
  return r;
}
__device__ __forceinline__ void ctl_and(unsigned* w, unsigned zero, unsigned m) {
  unsigned long long ex;
  asm volatile("s_mov_b64 %0, exec\n\ts_mov_b64 exec, 1\n\ts_nop 2\n\tglobal_atomic_and %2, %3, %1\n\ts_mov_b64 exec, %0\n\ts_nop 1"
               : "=&s"(ex) : "s"(w), "v"(zero), "v"(m) : "memory");
}
#define PARK_HEAD "s_getpc_b64 vcc\n\t.Lp0_%=:\n\ts_add_u32 vcc_lo, vcc_lo, %8\n\ts_addc_u32 vcc_hi, vcc_hi, 0\n\t" \
                  "s_add_u32 vcc_lo, vcc_lo, .Lp1_%=-.Lp0_%=\n\ts_addc_u32 vcc_hi, vcc_hi, 0\n\ts_setpc_b64 vcc\n\t.Lp1_%=:\n\t.set .Lpi_%=, 128\n\t.rept 15\n\t"
#define PARK_TAIL "s_branch .Lp2_%=\n\t.set .Lpi_%=, .Lpi_%=+8\n\t.endr\n\t.Lp2_%=:\n\ts_nop 1"
__device__ __forceinline__ void park_put(int j, const u32x4& p0, const u32x4& p1) {
  asm volatile(PARK_HEAD
               "v_accvgpr_write_b32 a[.Lpi_%=], %0\n\tv_accvgpr_write_b32 a[.Lpi_%=+1], %1\n\tv_accvgpr_write_b32 a[.Lpi_%=+2], %2\n\t"
               "v_accvgpr_write_b32 a[.Lpi_%=+3], %3\n\tv_accvgpr_write_b32 a[.Lpi_%=+4], %4\n\tv_accvgpr_write_b32 a[.Lpi_%=+5], %5\n\t"
               "v_accvgpr_write_b32 a[.Lpi_%=+6], %6\n\tv_accvgpr_write_b32 a[.Lpi_%=+7], %7\n\t" PARK_TAIL
               :: "v"(p0[0]), "v"(p0[1]), "v"(p0[2]), "v"(p0[3]), "v"(p1[0]), "v"(p1[1]), "v"(p1[2]), "v"(p1[3]), "s"(j * 68)
               : "memory", "vcc", "scc");
}
__device__ __forceinline__ void park_get(int j, u32x4& p0, u32x4& p1) {
  asm volatile(PARK_HEAD
               "v_accvgpr_read_b32 %0, a[.Lpi_%=]\n\tv_accvgpr_read_b32 %1, a[.Lpi_%=+1]\n\tv_accvgpr_read_b32 %2, a[.Lpi_%=+2]\n\t"
               "v_accvgpr_read_b32 %3, a[.Lpi_%=+3]\n\tv_accvgpr_read_b32 %4, a[.Lpi_%=+4]\n\tv_accvgpr_read_b32 %5, a[.Lpi_%=+5]\n\t"
               "v_accvgpr_read_b32 %6, a[.Lpi_%=+6]\n\tv_accvgpr_read_b32 %7, a[.Lpi_%=+7]\n\t" PARK_TAIL
               : "=v"(p0[0]), "=v"(p0[1]), "=v"(p0[2]), "=v"(p0[3]), "=v"(p1[0]), "=v"(p1[1]), "=v"(p1[2]), "=v"(p1[3]) : "s"(j * 68)
               : "memory", "vcc", "scc");
}
template <int B> __device__ __forceinline__ void acc_zero32() {
  asm volatile(".set .Lz_%=, %c0\n\t.rept 32\n\tv_accvgpr_write_b32 a[.Lz_%=], 0\n\t.set .Lz_%=, .Lz_%=+1\n\t.endr" :: "i"(B) : "memory", "a0", "a127", "a247");
}
__device__ __forceinline__ void acc_scale(float alpha) {
  float t0, t1, t2, t3;
  asm volatile("s_nop 15\n\t.set .Ls_%=, 0\n\t.rept 32\n\t"
               "v_accvgpr_read_b32 %0, a[.Ls_%=]\n\tv_accvgpr_read_b32 %1, a[.Ls_%=+1]\n\tv_accvgpr_read_b32 %2, a[.Ls_%=+2]\n\tv_accvgpr_read_b32 %3, a[.Ls_%=+3]\n\t"
               "v_mul_f32 %0, %0, %4\n\tv_mul_f32 %1, %1, %4\n\tv_mul_f32 %2, %2, %4\n\tv_mul_f32 %3, %3, %4\n\t"
               "v_accvgpr_write_b32 a[.Ls_%=], %0\n\tv_accvgpr_write_b32 a[.Ls_%=+1], %1\n\tv_accvgpr_write_b32 a[.Ls_%=+2], %2\n\tv_accvgpr_write_b32 a[.Ls_%=+3], %3\n\t"
               ".set .Ls_%=, .Ls_%=+4\n\t.endr\n\ts_nop 1"
               : "=&v"(t0), "=&v"(t1), "=&v"(t2), "=&v"(t3) : "v"(alpha) : "memory");
}
#define M_(a, b) "v_mfma_f32_32x32x16_bf16 %0, %" #a ", %" #b ", %0\n\t"
__device__ __forceinline__ void qk_first(f32x16& s, const bf16x8* f, const bf16x8* q) {
  asm volatile("s_nop 1\n\tv_mfma_f32_32x32x16_bf16 %0, %1, %5, 0\n\t" M_(2, 6) M_(3, 7) M_(4, 8)
               : "=&v"(s) : "v"(f[0]), "v"(f[1]), "v"(f[2]), "v"(f[3]), "v"(q[0]), "v"(q[1]), "v"(q[2]), "v"(q[3]));
}
__device__ __forceinline__ void qk_next(f32x16& s, const bf16x8* f, const bf16x8* q) {
  asm volatile("s_nop 1\n\t" M_(1, 5) M_(2, 6) M_(3, 7) M_(4, 8)
               : "+v"(s) : "v"(f[0]), "v"(f[1]), "v"(f[2]), "v"(f[3]), "v"(q[0]), "v"(q[1]), "v"(q[2]), "v"(q[3]));
}
#undef M_
__device__ __forceinline__ void qk_done(f32x16& s) { asm volatile("s_nop 15" : "+v"(s)); }
template <int C> __device__ __forceinline__ void pv4(const bf16x8* f, const u32x4& p) {
  asm volatile("s_nop 1\n\t"
               "v_mfma_f32_32x32x16_bf16 a[%c5:%c5+15], %0, %4, a[%c5:%c5+15]\n\t"
               "v_mfma_f32_32x32x16_bf16 a[%c5+16:%c5+31], %1, %4, a[%c5+16:%c5+31]\n\t"
               "v_mfma_f32_32x32x16_bf16 a[%c5+32:%c5+47], %2, %4, a[%c5+32:%c5+47]\n\t"
               "v_mfma_f32_32x32x16_bf16 a[%c5+48:%c5+63], %3, %4, a[%c5+48:%c5+63]"
               :: "v"(f[0]), "v"(f[1]), "v"(f[2]), "v"(f[3]), "v"(p), "i"(16 * C) : "memory");
}
#define OUT_(p) "s_cmp_eq_u32 %1, " #p "\n\ts_cbranch_scc1 .Lo" #p "_%=\n\t.set .Lw_%=, 8*" #p "\n\t.rept 8\n\t" \
                "ds_write_b128 %0, a[.Lw_%=*4:.Lw_%=*4+3] offset:.Lw_%=*1024\n\t.set .Lw_%=, .Lw_%=+1\n\t.endr\n\t.Lo" #p "_%=:\n\t"
__device__ __forceinline__ void acc_store(unsigned addr, int w) {
  asm volatile("s_nop 15\n\t" OUT_(0) OUT_(1) OUT_(2) OUT_(3) :: "v"(addr), "s"(w) : "memory", "scc");
}
#undef OUT_
template <int B> __device__ __forceinline__ void acc_get16(f32x4* x) {
  float r[16];
  asm volatile("v_accvgpr_read_b32 %0, a[%c16]\n\tv_accvgpr_read_b32 %1, a[%c16+1]\n\tv_accvgpr_read_b32 %2, a[%c16+2]\n\tv_accvgpr_read_b32 %3, a[%c16+3]\n\t"
               "v_accvgpr_read_b32 %4, a[%c16+4]\n\tv_accvgpr_read_b32 %5, a[%c16+5]\n\tv_accvgpr_read_b32 %6, a[%c16+6]\n\tv_accvgpr_read_b32 %7, a[%c16+7]\n\t"
               "v_accvgpr_read_b32 %8, a[%c16+8]\n\tv_accvgpr_read_b32 %9, a[%c16+9]\n\tv_accvgpr_read_b32 %10, a[%c16+10]\n\tv_accvgpr_read_b32 %11, a[%c16+11]\n\t"
               "v_accvgpr_read_b32 %12, a[%c16+12]\n\tv_accvgpr_read_b32 %13, a[%c16+13]\n\tv_accvgpr_read_b32 %14, a[%c16+14]\n\tv_accvgpr_read_b32 %15, a[%c16+15]"
               : "=v"(r[0]), "=v"(r[1]), "=v"(r[2]), "=v"(r[3]), "=v"(r[4]), "=v"(r[5]), "=v"(r[6]), "=v"(r[7]), "=v"(r[8]), "=v"(r[9]),
                 "=v"(r[10]), "=v"(r[11]), "=v"(r[12]), "=v"(r[13]), "=v"(r[14]), "=v"(r[15]) : "i"(B) : "memory");
#pragma unroll
  for (int i = 0; i < 4; ++i) x[i] = f32x4{r[4 * i], r[4 * i + 1], r[4 * i + 2], r[4 * i + 3]};
}

__device__ __forceinline__ float xhalf_max(float x) {
  auto r = __builtin_amdgcn_permlane32_swap(__float_as_uint(x), __float_as_uint(x), false, false);
  return fmaxf(__uint_as_float(r[0]), __uint_as_float(r[1]));
}
__device__ __forceinline__ float xhalf_sum(float x) {
  auto r = __builtin_amdgcn_permlane32_swap(__float_as_uint(x), __float_as_uint(x), false, false);
  return __uint_as_float(r[0]) + __uint_as_float(r[1]);
}
__device__ __forceinline__ unsigned pack2(float a, float b) {
  return __builtin_bit_cast(unsigned, __builtin_convertvector((f32x2){a, b}, bf16x2));
}
__device__ __forceinline__ f32x4 unpack4(unsigned lo, unsigned hi) {
  return f32x4{__uint_as_float(lo << 16), __uint_as_float(lo & 0xffff0000u), __uint_as_float(hi << 16), __uint_as_float(hi & 0xffff0000u)};
}
__device__ __forceinline__ unsigned swz(unsigned row) { return ((row & 3) << 2) | ((row >> 2) & 3); }
__device__ __forceinline__ bf16x8 row_frag(LAS char* lds, unsigned rd, int st) {
  return *(const LAS bf16x8*)(lds + ((rd ^ ((st & 7) << 5)) + (st >> 3) * 256));
}
__device__ __forceinline__ bf16x8 col_frag(LAS char* lds, unsigned vrd, int c, int koff) {
  const int o = (c >> 2) * 256 + koff;
  const bf16x4 lo = __builtin_amdgcn_ds_read_tr16_b64_v4bf16((LAS bf16x4*)(lds + ((vrd ^ ((c & 3) << 6)) + o)));
  const bf16x4 hi = __builtin_amdgcn_ds_read_tr16_b64_v4bf16((LAS bf16x4*)(lds + ((vrd ^ ((c & 3) << 6) ^ 32) + o + 8 * ROWB)));
  return __builtin_shufflevector(lo, hi, 0, 1, 2, 3, 4, 5, 6, 7);
}
template <int NR> __device__ __forceinline__ void sm_ref(const f32x16& s, float& m, float& l) {
  float mx = s[0];
#pragma unroll
  for (int r = 1; r < NR; ++r) mx = fmaxf(mx, s[r]);
  mx = xhalf_max(mx);
  if (!__all(mx <= m + DEFER)) {
    const float mn = fmaxf(m, mx);
    l *= __builtin_amdgcn_exp2f((m - mn) * SCL2);
    m = mn;
  }
  asm volatile("" : "+v"(m), "+v"(l));
}
template <int B> __device__ __forceinline__ u32x4 sm_exp(const f32x16& s, float m, float& l) {
  const float nb = -m * SCL2;
  float e[8];
#pragma unroll
  for (int r = 0; r < 8; ++r) e[r] = __builtin_amdgcn_exp2f(fmaf(s[B + r], SCL2, nb));
  l += ((e[0] + e[1]) + (e[2] + e[3])) + ((e[4] + e[5]) + (e[6] + e[7]));
  u32x4 p = {pack2(e[0], e[1]), pack2(e[2], e[3]), pack2(e[4], e[5]), pack2(e[6], e[7])};
  asm volatile("" : "+v"(p), "+v"(l));
  return p;
}

__global__ void __launch_bounds__(256, 1)
tree_attn(const char* __restrict__ Q, const char* __restrict__ K, const char* __restrict__ V, const char* __restrict__ KT,
          const char* __restrict__ VT, const char* __restrict__ MK, char* __restrict__ ws, unsigned short* __restrict__ out) {
  __shared__ __attribute__((aligned(1024))) char smem[LDS_BYTES];
  LAS char* lds = (LAS char*)smem;
  const unsigned lbase = (unsigned)(size_t)lds;
  const int tid = threadIdx.x, lane = tid & 63, wave = __builtin_amdgcn_readfirstlane(tid >> 6);
  const int wg = blockIdx.x, x8 = wg & 7;
  const int grp = (wg >> 3) * 2 + ((x8 >> 1) & 1), mem = (x8 >> 2) * 2 + (x8 & 1);
  const int r32 = lane & 31, hh = lane >> 5;
  const bool leader = (mem == 1) && (wave == 0);
  {
    unsigned long long pc; unsigned len;
    asm volatile("s_getpc_b64 %0\n.Lcode0:\n\ts_mov_b32 %1, .Lcode1-.Lcode0" : "=s"(pc), "=s"(len));
    const unsigned o = min((unsigned)(((wg >> 3) * 4 + wave) & 31) * 1024u, (len & ~15u) - 1024u) + lane * 16;
    asm volatile("global_load_dwordx4 a[240:243], %0, %1" :: "v"(o), "s"(pc & ~15ull) : "memory", "a240", "a241", "a242", "a243");
  }
  unsigned ko[8], qo[4];
#pragma unroll
  for (int i = 0; i < 8; ++i) ko[i] = hh * ROWB + ((r32 ^ swz(2 * i + hh)) << 4);
#pragma unroll
  for (int i = 0; i < 4; ++i) qo[i] = ko[i] ^ ((wave & 1) << 5);
  asm volatile("" : "+v"(ko[0]), "+v"(ko[1]), "+v"(ko[2]), "+v"(ko[3]), "+v"(ko[4]), "+v"(ko[5]), "+v"(ko[6]), "+v"(ko[7]),
                    "+v"(qo[0]), "+v"(qo[1]), "+v"(qo[2]), "+v"(qo[3]));
  const unsigned ring = wave * RINGB;
  const size_t wof = (size_t)grp * (CTX * ROWB) + (mem * 4 + wave) * BLKB;
  const char* src = K + wof;
  unsigned dst = lbase + ring;
#define PIECE_N(k, cnt) dma1<((k) & 3) * 1024, (cnt)>(src + ((k) >> 3) * HALFB + (((k) >> 2) & 1) * 4096, dst + ((k) >> 2) * 4096, ko[(k) & 7])
#define PIECE(k) dma1w<((k) & 3) * 1024>(src + ((k) >> 3) * HALFB + (((k) >> 2) & 1) * 4096, dst + ((k) >> 2) * 4096, ko[(k) & 7], late)
  int late = 0;
  auto quarter = [&](const char* s, unsigned d) {
    dma1<0, 63>(s, d, qo[0]); dma1<1024, 63>(s, d, qo[1]); dma1<2048, 63>(s, d, qo[2]); dma1<3072, 63>(s, d, qo[3]);
  };
  quarter(Q + (size_t)grp * TILEB + wave * 4096, lbase + Q_OFF + wave * 4096);
  if (mem == 0) {
    quarter((wave & 2 ? VT : KT) + (size_t)grp * BLKB + (wave & 1) * 4096, lbase + TREE_OFF + wave * 4096);
    const unsigned mo = (r32 & 15) * (NT * 2) + 8 * hh;
    asm volatile("s_nop 4\n\tglobal_load_dwordx2 a[248:249], %0, %1\n\tglobal_load_dwordx2 a[250:251], %0, %1 offset:16"
                 :: "v"(mo), "s"(MK + (grp >> 3) * (NT * NT * 2)) : "memory", "a248", "a249", "a250", "a251");
  }
  unsigned* cw = (unsigned*)(ws + WS_CTL + grp * 256);
  const unsigned vz = 0;
  if (wave == 0) ctl_add_ret(cw, vz, 0u);
  PIECE_N(0, 63); PIECE_N(1, 63); PIECE_N(2, 63); PIECE_N(3, 63); PIECE_N(4, 63);
  unsigned due = clock_ticks() + PACE0;
  const unsigned krd = r32 * ROWB + ((hh ^ swz(r32)) << 4);
  const int q4 = (lane & 15) >> 2;
  const unsigned vrd = (4 * hh + q4) * ROWB + (q4 << 6) + ((((lane >> 4) & 1) * 32 + (lane & 3) * 8) ^ (hh << 4));
  acc_zero32<0>(); acc_zero32<32>(); acc_zero32<64>(); acc_zero32<96>();
  WAIT_VM(5);
  const unsigned w0 = ctl_result();
  if (leader) ctl_and(cw, vz, W_EPOCH_MASK);
  BARRIER();
  PIECE(5);
  bf16x8 q[16];
#pragma unroll
  for (int st = 0; st < 16; ++st) q[st] = row_frag(lds, Q_OFF + krd, st);
  PIECE(6);
  BARRIER();
  float zm = -__builtin_inff(), zl = 0.f, om = -__builtin_inff();
  if (mem == 0 && wave == 1) {
    unsigned mk[4];
    asm volatile("v_accvgpr_read_b32 %0, a248\n\tv_accvgpr_read_b32 %1, a249\n\tv_accvgpr_read_b32 %2, a250\n\tv_accvgpr_read_b32 %3, a251"
                 : "=v"(mk[0]), "=v"(mk[1]), "=v"(mk[2]), "=v"(mk[3]) :: "memory");
    bf16x8 f[16]; f32x16 s;
#pragma unroll
    for (int st = 0; st < 16; ++st) f[st] = row_frag(lds, TREE_OFF + (krd & (BLKB - 1)), st);
    PIECE(7);
    qk_first(s, f, q); qk_next(s, f + 4, q + 4);
    PIECE(8);
    qk_next(s, f + 8, q + 8); qk_next(s, f + 12, q + 12);
    PIECE(9);
    qk_done(s);
#pragma unroll
    for (int c = 0; c < 8; ++c) f[c] = col_frag(lds, TREE_OFF + vrd, c, BLKB);
#pragma unroll
    for (int r = 0; r < 8; ++r) {
      const float mv = __uint_as_float((r & 1) ? (mk[r >> 1] & 0xffff0000u) : (mk[r >> 1] << 16));
      s[r] = fmaf(1.0f - mv, MASKRAW, s[r]);
    }
    sm_ref<8>(s, zm, zl);
    PIECE(10);
    const u32x4 p = sm_exp<0>(s, zm, zl);
    om = zm;
    PIECE(11);
    pv4<0>(f, p); pv4<4>(f + 4, p);
  } else {
    PIECE(7); PIECE(8); PIECE(9); PIECE(10); PIECE(11);
  }
  PIECE(12); PIECE(13); PIECE(14); PIECE(15);

  bool ready = leader;
  const unsigned mt = MT_OFF + wave * (TPW * 128) + r32 * 4;
  unsigned slot = ring;
  u32x4 p0, p1;
#pragma unroll 1
  for (int n = 0; n < TPW; ++n) {
    src = n + 1 < TPW ? src + TSTEP : V + wof;
    dst ^= TILEB;
    const bool look = wave == 0 && (leader ? n < 4 : !ready);
    if (look) ctl_add_ret(cw, vz, leader ? W_EPOCH : 0u);
    PIECE(0);
    late = (int)(clock_ticks() - due); due += PACE + max(late - SLIP, 0);
    PIECE(1); PIECE(2); PIECE(3); PIECE(4);
    WAIT_VM(5);
    bf16x8 f[16]; f32x16 s;
#pragma unroll
    for (int st = 0; st < 16; ++st) f[st] = row_frag(lds, slot + krd, st);
    if (look) ready = ready || ctl_result() != w0;
    PIECE(5);
    qk_first(s, f, q);
    PIECE(6);
    qk_next(s, f + 4, q + 4);
    PIECE(7);
    qk_next(s, f + 8, q + 8);
    PIECE(8);
    qk_next(s, f + 12, q + 12);
    PIECE(9);
    qk_done(s);
    sm_ref<16>(s, zm, zl);
    PIECE(10);
    p0 = sm_exp<0>(s, zm, zl);
    PIECE(11);
    p1 = sm_exp<8>(s, zm, zl);
    PIECE(12);
    if (n < TPW - 1) park_put(n, p0, p1);
    *(LAS float*)(lds + mt + n * 128) = zm;
    PIECE(13); PIECE(14); PIECE(15);
    slot ^= TILEB;
  }
  auto rebase = [&](float tm) {
    if (!__all(tm == om)) {
      if (!__all(om == -__builtin_inff())) acc_scale(__builtin_amdgcn_exp2f((om - tm) * SCL2));
      om = tm;
    }
  };
#pragma unroll 1
  for (int n = 0; n < TPW - 1; ++n) {
    src += TSTEP;
    dst ^= TILEB;
    u32x4 r0, r1;
    PIECE(0);
    late = (int)(clock_ticks() - due); due += PACE + max(late - SLIP, 0);
    if (n >= TPW - 1 - ENDB) late = 1;
    const float tm = *(const LAS float*)(lds + mt + n * 128);
    park_get(n, r0, r1);
    PIECE(1);
    rebase(tm);
    PIECE(2); PIECE(3); PIECE(4);
    WAIT_VM(5);
    bf16x8 f[16];
#pragma unroll
    for (int c = 0; c < 8; ++c) f[c] = col_frag(lds, slot + vrd, c, 0);
    PIECE(5);
#pragma unroll
    for (int c = 0; c < 8; ++c) f[8 + c] = col_frag(lds, slot + vrd, c, BLKB);
    PIECE(6);
    pv4<0>(f, r0);
    PIECE(7);
    pv4<4>(f + 4, r0);
    PIECE(8);
    pv4<0>(f + 8, r1);
    PIECE(9);
    pv4<4>(f + 12, r1);
    PIECE(10); PIECE(11); PIECE(12); PIECE(13); PIECE(14); PIECE(15);
    slot ^= TILEB;
  }
  {
    rebase(zm);
    WAIT_VM(8);
    bf16x8 f[16];
#pragma unroll
    for (int c = 0; c < 8; ++c) f[c] = col_frag(lds, slot + vrd, c, 0);
    pv4<0>(f, p0); pv4<4>(f + 4, p0);
    WAIT_VM(0);
#pragma unroll
    for (int c = 0; c < 8; ++c) f[8 + c] = col_frag(lds, slot + vrd, c, BLKB);
    pv4<0>(f + 8, p1); pv4<4>(f + 12, p1);
  }

  acc_store(lbase + ring + lane * 16, wave);
  zl = xhalf_sum(zl);
  *(LAS f32x2*)(lds + ML_OFF + (wave * 32 + r32) * 8) = f32x2{zm, zl};
  if (wave == 0) *(LAS unsigned*)(lds + FL_OFF) = 0u;
  BARRIER();
  f32x2 ml[3]; f32x4 im[3][8], acc[8];
#pragma unroll
  for (int d = 0; d < 3; ++d) ml[d] = *(const LAS f32x2*)(lds + ML_OFF + (((wave + 1 + d) & 3) * 32 + r32) * 8);
#pragma unroll
  for (int d = 0; d < 3; ++d)
#pragma unroll
    for (int i = 0; i < 8; ++i) im[d][i] = *(const LAS f32x4*)(lds + ((wave + 1 + d) & 3) * RINGB + wave * 8192 + lane * 16 + i * 1024);
  if (wave == 0) { acc_get16<0>(acc); acc_get16<16>(acc + 4); }
  else if (wave == 1) { acc_get16<32>(acc); acc_get16<48>(acc + 4); }
  else if (wave == 2) { acc_get16<64>(acc); acc_get16<80>(acc + 4); }
  else { acc_get16<96>(acc); acc_get16<112>(acc + 4); }
  const float M = fmaxf(fmaxf(zm, ml[0][0]), fmaxf(ml[1][0], ml[2][0]));
  float f[4];
  f[3] = __builtin_amdgcn_exp2f((zm - M) * SCL2);
  float L = f[3] * zl;
#pragma unroll
  for (int d = 0; d < 3; ++d) { f[d] = __builtin_amdgcn_exp2f((ml[d][0] - M) * SCL2); L = fmaf(f[d], ml[d][1], L); }

  const __amdgpu_buffer_rsrc_t rs = __builtin_amdgcn_make_buffer_rsrc(ws, 0, (int)WS_END, 0x00020000);
  const int ml_off = (int)WS_PML + (grp * NSPL * 32 + r32) * 8;
  const int po_off = (int)WS_PO + grp * NSPL * PARTB + wave * 4096 + lane * 16;
  if (wave == 0 && hh == 0) __builtin_amdgcn_raw_buffer_store_b64(__builtin_bit_cast(u32x2, f32x2{M, L}), rs, ml_off + mem * 256, 0, 16);
  f32x4 x = {}, y = {};
#pragma unroll
  for (int v = 0; v < 4; ++v) {
    const f32x4 a = acc[2 * v] * f[3] + im[0][2 * v] * f[0] + im[1][2 * v] * f[1] + im[2][2 * v] * f[2];
    const f32x4 b = acc[2 * v + 1] * f[3] + im[0][2 * v + 1] * f[0] + im[1][2 * v + 1] * f[1] + im[2][2 * v + 1] * f[2];
    if (v == mem) { x = a; y = b; }
    else __builtin_amdgcn_raw_buffer_store_b128(u32x4{pack2(a[0], a[1]), pack2(a[2], a[3]), pack2(b[0], b[1]), pack2(b[2], b[3])}, rs,
                                                po_off + mem * PARTB + v * 1024, 0, 16);
  }
  WAIT_VM(0);
  BARRIER();
  if (wave == 0) {
#pragma unroll 1
    for (int i = 0; i < SPIN_MAX && !ready; ++i) {
      ctl_add_ret(cw, vz, 0u); WAIT_VM(0);
      ready = ctl_result() != w0;
      if (!ready) __builtin_amdgcn_s_sleep(16);
    }
    ctl_add_ret(cw, vz, 1u << mem);
#pragma unroll 1
    for (int i = 0; i < SPIN_MAX; ++i) {
      WAIT_VM(0);
      const unsigned w = ctl_result() | (1u << mem);
      if ((w & 15u) == 15u) break;
      ctl_add_ret(cw, vz, leader ? W_EPOCH : 0u);
    }
    *(volatile LAS unsigned*)(lds + FL_OFF) = 1u;
  } else {
#pragma unroll 1
    for (int i = 0; i < (SPIN_MAX << 8); ++i)
      if (__builtin_amdgcn_readfirstlane(*(volatile LAS unsigned*)(lds + FL_OFF))) break;
  }
  asm volatile("" ::: "memory");
  f32x2 oml[3]; u32x4 oa[3];
#pragma unroll
  for (int d = 0; d < 3; ++d) {
    const int sm = (mem + 1 + d) & 3;
    oml[d] = __builtin_bit_cast(f32x2, __builtin_amdgcn_raw_buffer_load_b64(rs, ml_off, sm * 256, 16));
    oa[d] = __builtin_amdgcn_raw_buffer_load_b128(rs, po_off, sm * PARTB + mem * 1024, 16);
  }
  const float Mf = fmaxf(fmaxf(M, oml[0][0]), fmaxf(oml[1][0], oml[2][0]));
  f[3] = __builtin_amdgcn_exp2f((M - Mf) * SCL2);
  L *= f[3];
#pragma unroll
  for (int d = 0; d < 3; ++d) { f[d] = __builtin_amdgcn_exp2f((oml[d][0] - Mf) * SCL2); L = fmaf(f[d], oml[d][1], L); }
  const float inv = 1.0f / L;
  x *= f[3] * inv; y *= f[3] * inv;
#pragma unroll
  for (int d = 0; d < 3; ++d) { x += (f[d] * inv) * unpack4(oa[d][0], oa[d][1]); y += (f[d] * inv) * unpack4(oa[d][2], oa[d][3]); }
  const auto rx = __builtin_amdgcn_permlane32_swap(pack2(x[0], x[1]), pack2(y[0], y[1]), false, false);
  const auto ry = __builtin_amdgcn_permlane32_swap(pack2(x[2], x[3]), pack2(y[2], y[3]), false, false);
  *(u32x4*)((char*)out + (size_t)(grp * 32 + r32) * ROWB + wave * 128 + mem * 32 + hh * 16) = u32x4{rx[0], ry[0], rx[1], ry[1]};
  asm volatile(".Lcode1:");
}

extern "C" void kernel_launch(void* const* d_in, const int* in_sizes, int n_in, void* d_out, int out_size, void* d_ws, size_t ws_size,
                              hipStream_t stream) {
  tree_attn<<<NGRP * NSPL, 256, 0, stream>>>((const char*)d_in[0], (const char*)d_in[1], (const char*)d_in[2], (const char*)d_in[3],
                                             (const char*)d_in[4], (const char*)d_in[5], (char*)d_ws, (unsigned short*)d_out);
}
```

```cpp
#include <hip/hip_runtime.h>
#include <hip/hip_bf16.h>

#define LAS __attribute__((address_space(3)))
typedef __bf16 bf16x8 __attribute__((ext_vector_type(8)));
typedef __bf16 bf16x4 __attribute__((ext_vector_type(4)));
typedef __bf16 bf16x2 __attribute__((ext_vector_type(2)));
typedef float f32x16 __attribute__((ext_vector_type(16)));
typedef float f32x4 __attribute__((ext_vector_type(4)));
typedef float f32x2 __attribute__((ext_vector_type(2)));
typedef unsigned u32x4 __attribute__((ext_vector_type(4)));
typedef unsigned u32x2 __attribute__((ext_vector_type(2)));

constexpr int NB = 8, HKV = 8, NT = 16, CTX = 8192, HD = 256;
constexpr int NGRP = NB * HKV;
constexpr int NSPL = 4;
constexpr int ROWB = HD * 2;
constexpr int BLKB = 16 * ROWB;
constexpr int TILEB = 2 * BLKB;
constexpr int RINGB = 2 * TILEB;
constexpr int TPW = CTX / 32 / 16;
constexpr int HALFB = CTX / 2 * ROWB, TSTEP = 16 * BLKB;
constexpr int TREE_OFF = 4 * RINGB;
constexpr int Q_OFF = TREE_OFF + TILEB;
constexpr int LDS_BYTES = Q_OFF + TILEB;
constexpr int MT_OFF = Q_OFF;
constexpr int ML_OFF = MT_OFF + 4 * TPW * 128;
static_assert(LDS_BYTES <= 160 * 1024 && ML_OFF + 1024 <= LDS_BYTES && TPW == 16, "layout");
constexpr float SCL2 = 0.0625f * 1.4426950408889634f;
constexpr float MASKRAW = -1.0e9f * 16.0f;
constexpr float DEFER = 8.0f / SCL2;

constexpr int WIN = 5;
constexpr int WEND = 10;
constexpr int PACE = 215, PACE0 = 250;
constexpr int SLIP = 200, JUMP = 212;
constexpr int LV0 = -100, LV1 = -50, LV2 = 0, LV3 = 40, LV4 = 90;

constexpr unsigned W_EPOCH = 1u << 29, W_EPOCH_MASK = 0xE0000000u;
constexpr int SPIN_MAX = 1 << 16;
constexpr int PARTB = 32 * HD * 2;
constexpr size_t WS_CTL = 0, WS_PML = 65536, WS_PO = WS_PML + (size_t)NGRP * NSPL * 32 * 8;
constexpr size_t WS_END = WS_PO + (size_t)NGRP * NSPL * PARTB;

template <int OFF, int CNT> __device__ __forceinline__ void dma1(const char* src, unsigned lds, unsigned v) {
  unsigned keep;
  __builtin_amdgcn_sched_barrier(0);
  asm volatile("s_mov_b32 %0, m0\n\ts_mov_b32 m0, %2\n\ts_waitcnt vmcnt(%5)\n\ts_nop 2\n\t"
               "global_load_lds_dwordx4 %3, %1 offset:%4 nt\n\ts_mov_b32 m0, %0"
               : "=&s"(keep) : "s"(src), "s"(lds), "v"(v), "i"(OFF), "i"(CNT) : "memory");
  __builtin_amdgcn_sched_barrier(0);
}
template <int OFF> __device__ __forceinline__ void dma1w(const char* src, unsigned lds, unsigned v, int lvl) {
  unsigned keep;
  __builtin_amdgcn_sched_barrier(0);
  asm volatile("s_mov_b32 %0, m0\n\ts_mov_b32 m0, %2\n\t"
               "s_cmp_gt_i32 %5, 4\n\ts_waitcnt vmcnt(%6+3)\n\ts_cbranch_scc1 .Lb_%=\n\t"
               "s_cmp_gt_i32 %5, 3\n\ts_waitcnt vmcnt(%6+2)\n\ts_cbranch_scc1 .Lb_%=\n\t"
               "s_cmp_gt_i32 %5, 2\n\ts_waitcnt vmcnt(%6+1)\n\ts_cbranch_scc1 .Lb_%=\n\t"
               "s_cmp_gt_i32 %5, 1\n\ts_waitcnt vmcnt(%6)\n\ts_cbranch_scc1 .Lb_%=\n\t"
               "s_cmp_gt_i32 %5, 0\n\ts_nop 7\n\ts_cbranch_scc1 .Lb_%=\n\t"
               "s_waitcnt vmcnt(%6-1)\n\t.Lb_%=:\n\t"
               "global_load_lds_dwordx4 %3, %1 offset:%4 nt\n\ts_mov_b32 m0, %0"
               : "=&s"(keep) : "s"(src), "s"(lds), "v"(v), "i"(OFF), "s"(lvl), "i"(WIN - 1) : "memory", "scc");
  __builtin_amdgcn_sched_barrier(0);
}
__device__ __forceinline__ int level_of(int late) {
  int l;
  asm volatile("s_mov_b32 %0, 0\n\ts_cmp_gt_i32 %1, %2\n\ts_addc_u32 %0, %0, 0\n\ts_cmp_gt_i32 %1, %3\n\ts_addc_u32 %0, %0, 0\n\ts_cmp_gt_i32 %1, %4\n\ts_addc_u32 %0, %0, 0\n\t"
               "s_cmp_gt_i32 %1, %5\n\ts_addc_u32 %0, %0, 0\n\ts_cmp_gt_i32 %1, %6\n\ts_addc_u32 %0, %0, 0"
               : "=&s"(l) : "s"(late), "i"(LV0), "i"(LV1), "i"(LV2), "i"(LV3), "i"(LV4) : "scc");
  return l;
}
__device__ __forceinline__ unsigned clock_ticks() {
  unsigned long long t;
  asm volatile("s_memrealtime %0\n\ts_waitcnt lgkmcnt(0)" : "=s"(t) :: "memory");
  return (unsigned)t;
}
#define WAIT_VM(n) asm volatile("s_waitcnt vmcnt(%0)" :: "i"(n) : "memory")
#define WAIT_LGKM0() asm volatile("s_waitcnt lgkmcnt(0)" ::: "memory")
#define BARRIER() do { WAIT_LGKM0(); __builtin_amdgcn_s_barrier(); asm volatile("" ::: "memory"); } while (0)

__device__ __forceinline__ void ctl_add_ret(unsigned* w, unsigned zero, unsigned inc) {
  unsigned long long ex;
  asm volatile("v_accvgpr_write_b32 a254, %3\n\ts_mov_b64 %0, exec\n\ts_mov_b64 exec, 1\n\ts_nop 2\n\t"
               "global_atomic_add a255, %2, a254, %1 sc0\n\ts_mov_b64 exec, %0\n\ts_nop 1"
               : "=&s"(ex) : "s"(w), "v"(zero), "v"(inc) : "memory", "a254", "a255");
}
__device__ __forceinline__ unsigned ctl_result() {
  unsigned t, r;
  asm volatile("v_accvgpr_read_b32 %0, a255\n\ts_nop 1\n\tv_readfirstlane_b32 %1, %0\n\ts_nop 4" : "=&v"(t), "=s"(r) :: "memory");
  return r;
}
__device__ __forceinline__ void ctl_and(unsigned* w, unsigned zero, unsigned m) {
  unsigned long long ex;
  asm volatile("s_mov_b64 %0, exec\n\ts_mov_b64 exec, 1\n\ts_nop 2\n\tglobal_atomic_and %2, %3, %1\n\ts_mov_b64 exec, %0\n\ts_nop 1"
               : "=&s"(ex) : "s"(w), "v"(zero), "v"(m) : "memory");
}
#define PARK_HEAD "s_getpc_b64 vcc\n\t.Lp0_%=:\n\ts_add_u32 vcc_lo, vcc_lo, %8\n\ts_addc_u32 vcc_hi, vcc_hi, 0\n\t" \
                  "s_add_u32 vcc_lo, vcc_lo, .Lp1_%=-.Lp0_%=\n\ts_addc_u32 vcc_hi, vcc_hi, 0\n\ts_setpc_b64 vcc\n\t.Lp1_%=:\n\t.set .Lpi_%=, 128\n\t.rept 15\n\t"
#define PARK_TAIL "s_branch .Lp2_%=\n\t.set .Lpi_%=, .Lpi_%=+8\n\t.endr\n\t.Lp2_%=:\n\ts_nop 1"
__device__ __forceinline__ void park_put(int j, const u32x4& p0, const u32x4& p1) {
  asm volatile(PARK_HEAD
               "v_accvgpr_write_b32 a[.Lpi_%=], %0\n\tv_accvgpr_write_b32 a[.Lpi_%=+1], %1\n\tv_accvgpr_write_b32 a[.Lpi_%=+2], %2\n\t"
               "v_accvgpr_write_b32 a[.Lpi_%=+3], %3\n\tv_accvgpr_write_b32 a[.Lpi_%=+4], %4\n\tv_accvgpr_write_b32 a[.Lpi_%=+5], %5\n\t"
               "v_accvgpr_write_b32 a[.Lpi_%=+6], %6\n\tv_accvgpr_write_b32 a[.Lpi_%=+7], %7\n\t" PARK_TAIL
               :: "v"(p0[0]), "v"(p0[1]), "v"(p0[2]), "v"(p0[3]), "v"(p1[0]), "v"(p1[1]), "v"(p1[2]), "v"(p1[3]), "s"(j * 68)
               : "memory", "vcc", "scc");
}
__device__ __forceinline__ void park_get(int j, u32x4& p0, u32x4& p1) {
  asm volatile(PARK_HEAD
               "v_accvgpr_read_b32 %0, a[.Lpi_%=]\n\tv_accvgpr_read_b32 %1, a[.Lpi_%=+1]\n\tv_accvgpr_read_b32 %2, a[.Lpi_%=+2]\n\t"
               "v_accvgpr_read_b32 %3, a[.Lpi_%=+3]\n\tv_accvgpr_read_b32 %4, a[.Lpi_%=+4]\n\tv_accvgpr_read_b32 %5, a[.Lpi_%=+5]\n\t"
               "v_accvgpr_read_b32 %6, a[.Lpi_%=+6]\n\tv_accvgpr_read_b32 %7, a[.Lpi_%=+7]\n\t" PARK_TAIL
               : "=v"(p0[0]), "=v"(p0[1]), "=v"(p0[2]), "=v"(p0[3]), "=v"(p1[0]), "=v"(p1[1]), "=v"(p1[2]), "=v"(p1[3]) : "s"(j * 68)
               : "memory", "vcc", "scc");
}
template <int B> __device__ __forceinline__ void acc_zero32() {
  asm volatile(".set .Lz_%=, %c0\n\t.rept 32\n\tv_accvgpr_write_b32 a[.Lz_%=], 0\n\t.set .Lz_%=, .Lz_%=+1\n\t.endr" :: "i"(B) : "memory", "a0", "a127", "a247");
}
__device__ __forceinline__ void acc_scale(float alpha) {
  float t0, t1, t2, t3;
  asm volatile("s_nop 15\n\t.set .Ls_%=, 0\n\t.rept 32\n\t"
               "v_accvgpr_read_b32 %0, a[.Ls_%=]\n\tv_accvgpr_read_b32 %1, a[.Ls_%=+1]\n\tv_accvgpr_read_b32 %2, a[.Ls_%=+2]\n\tv_accvgpr_read_b32 %3, a[.Ls_%=+3]\n\t"
               "v_mul_f32 %0, %0, %4\n\tv_mul_f32 %1, %1, %4\n\tv_mul_f32 %2, %2, %4\n\tv_mul_f32 %3, %3, %4\n\t"
               "v_accvgpr_write_b32 a[.Ls_%=], %0\n\tv_accvgpr_write_b32 a[.Ls_%=+1], %1\n\tv_accvgpr_write_b32 a[.Ls_%=+2], %2\n\tv_accvgpr_write_b32 a[.Ls_%=+3], %3\n\t"
               ".set .Ls_%=, .Ls_%=+4\n\t.endr\n\ts_nop 1"
               : "=&v"(t0), "=&v"(t1), "=&v"(t2), "=&v"(t3) : "v"(alpha) : "memory");
}
#define M_(a, b) "v_mfma_f32_32x32x16_bf16 %0, %" #a ", %" #b ", %0\n\t"
__device__ __forceinline__ void qk_first(f32x16& s, const bf16x8* f, const bf16x8* q) {
  asm volatile("s_nop 1\n\tv_mfma_f32_32x32x16_bf16 %0, %1, %5, 0\n\t" M_(2, 6) M_(3, 7) M_(4, 8)
               : "=&v"(s) : "v"(f[0]), "v"(f[1]), "v"(f[2]), "v"(f[3]), "v"(q[0]), "v"(q[1]), "v"(q[2]), "v"(q[3]));
}
__device__ __forceinline__ void qk_next(f32x16& s, const bf16x8* f, const bf16x8* q) {
  asm volatile("s_nop 1\n\t" M_(1, 5) M_(2, 6) M_(3, 7) M_(4, 8)
               : "+v"(s) : "v"(f[0]), "v"(f[1]), "v"(f[2]), "v"(f[3]), "v"(q[0]), "v"(q[1]), "v"(q[2]), "v"(q[3]));
}
#undef M_
__device__ __forceinline__ void qk_done(f32x16& s) { asm volatile("s_nop 15" : "+v"(s)); }
template <int C> __device__ __forceinline__ void pv4(const bf16x8* f, const u32x4& p) {
  asm volatile("s_nop 1\n\t"
               "v_mfma_f32_32x32x16_bf16 a[%c5:%c5+15], %0, %4, a[%c5:%c5+15]\n\t"
               "v_mfma_f32_32x32x16_bf16 a[%c5+16:%c5+31], %1, %4, a[%c5+16:%c5+31]\n\t"
               "v_mfma_f32_32x32x16_bf16 a[%c5+32:%c5+47], %2, %4, a[%c5+32:%c5+47]\n\t"
               "v_mfma_f32_32x32x16_bf16 a[%c5+48:%c5+63], %3, %4, a[%c5+48:%c5+63]"
               :: "v"(f[0]), "v"(f[1]), "v"(f[2]), "v"(f[3]), "v"(p), "i"(16 * C) : "memory");
}
__device__ __forceinline__ void pv2(f32x16& x, f32x16& y, const bf16x8& v0, const bf16x8& v1, const u32x4& p) {
  asm volatile("s_nop 1\n\tv_mfma_f32_32x32x16_bf16 %0, %2, %4, %0\n\tv_mfma_f32_32x32x16_bf16 %1, %3, %4, %1"
               : "+v"(x), "+v"(y) : "v"(v0), "v"(v1), "v"(p));
}
template <int B> __device__ __forceinline__ f32x16 acc_get16() {
  float r[16];
  asm volatile("v_accvgpr_read_b32 %0, a[%c16]\n\tv_accvgpr_read_b32 %1, a[%c16+1]\n\tv_accvgpr_read_b32 %2, a[%c16+2]\n\tv_accvgpr_read_b32 %3, a[%c16+3]\n\t"
               "v_accvgpr_read_b32 %4, a[%c16+4]\n\tv_accvgpr_read_b32 %5, a[%c16+5]\n\tv_accvgpr_read_b32 %6, a[%c16+6]\n\tv_accvgpr_read_b32 %7, a[%c16+7]\n\t"
               "v_accvgpr_read_b32 %8, a[%c16+8]\n\tv_accvgpr_read_b32 %9, a[%c16+9]\n\tv_accvgpr_read_b32 %10, a[%c16+10]\n\tv_accvgpr_read_b32 %11, a[%c16+11]\n\t"
               "v_accvgpr_read_b32 %12, a[%c16+12]\n\tv_accvgpr_read_b32 %13, a[%c16+13]\n\tv_accvgpr_read_b32 %14, a[%c16+14]\n\tv_accvgpr_read_b32 %15, a[%c16+15]"
               : "=v"(r[0]), "=v"(r[1]), "=v"(r[2]), "=v"(r[3]), "=v"(r[4]), "=v"(r[5]), "=v"(r[6]), "=v"(r[7]), "=v"(r[8]), "=v"(r[9]),
                 "=v"(r[10]), "=v"(r[11]), "=v"(r[12]), "=v"(r[13]), "=v"(r[14]), "=v"(r[15]) : "i"(B) : "memory");
  f32x16 x;
#pragma unroll
  for (int i = 0; i < 16; ++i) x[i] = r[i];
  return x;
}

__device__ __forceinline__ float xhalf_max(float x) {
  auto r = __builtin_amdgcn_permlane32_swap(__float_as_uint(x), __float_as_uint(x), false, false);
  return fmaxf(__uint_as_float(r[0]), __uint_as_float(r[1]));
}
__device__ __forceinline__ float xhalf_sum(float x) {
  auto r = __builtin_amdgcn_permlane32_swap(__float_as_uint(x), __float_as_uint(x), false, false);
  return __uint_as_float(r[0]) + __uint_as_float(r[1]);
}
__device__ __forceinline__ unsigned pack2(float a, float b) {
  return __builtin_bit_cast(unsigned, __builtin_convertvector((f32x2){a, b}, bf16x2));
}
__device__ __forceinline__ f32x4 unpack4(unsigned lo, unsigned hi) {
  return f32x4{__uint_as_float(lo << 16), __uint_as_float(lo & 0xffff0000u), __uint_as_float(hi << 16), __uint_as_float(hi & 0xffff0000u)};
}
__device__ __forceinline__ u32x4 pack8(const f32x16& x, int b) {
  return u32x4{pack2(x[b], x[b + 1]), pack2(x[b + 2], x[b + 3]), pack2(x[b + 4], x[b + 5]), pack2(x[b + 6], x[b + 7])};
}
__device__ __forceinline__ void fma8(f32x16& x, int b, float f, const u32x4& d) {
#pragma unroll
  for (int i = 0; i < 4; ++i) {
    x[b + 2 * i] = fmaf(f, __uint_as_float(d[i] << 16), x[b + 2 * i]);
    x[b + 2 * i + 1] = fmaf(f, __uint_as_float(d[i] & 0xffff0000u), x[b + 2 * i + 1]);
  }
}
__device__ __forceinline__ unsigned swz(unsigned row) { return ((row & 3) << 2) | ((row >> 2) & 3); }
__device__ __forceinline__ bf16x8 row_frag(LAS char* lds, unsigned rd, int st) {
  return *(const LAS bf16x8*)(lds + ((rd ^ ((st & 7) << 5)) + (st >> 3) * 256));
}
__device__ __forceinline__ bf16x8 col_frag(LAS char* lds, unsigned vrd, int c, int koff) {
  const int o = (c >> 2) * 256 + koff;
  const bf16x4 lo = __builtin_amdgcn_ds_read_tr16_b64_v4bf16((LAS bf16x4*)(lds + ((vrd ^ ((c & 3) << 6)) + o)));
  const bf16x4 hi = __builtin_amdgcn_ds_read_tr16_b64_v4bf16((LAS bf16x4*)(lds + ((vrd ^ ((c & 3) << 6) ^ 32) + o + 8 * ROWB)));
  return __builtin_shufflevector(lo, hi, 0, 1, 2, 3, 4, 5, 6, 7);
}
template <int NR> __device__ __forceinline__ void sm_ref(const f32x16& s, float& m, float& l) {
  float mx = s[0];
#pragma unroll
  for (int r = 1; r < NR; ++r) mx = fmaxf(mx, s[r]);
  mx = xhalf_max(mx);
  if (!__all(mx <= m + DEFER)) {
    const float mn = fmaxf(m, mx);
    l *= __builtin_amdgcn_exp2f((m - mn) * SCL2);
    m = mn;
  }
  asm volatile("" : "+v"(m), "+v"(l));
}
template <int B> __device__ __forceinline__ u32x4 sm_exp(const f32x16& s, float m, float& l) {
  float e[8];
#pragma unroll
  for (int r = 0; r < 8; ++r) e[r] = __builtin_amdgcn_exp2f((s[B + r] - m) * SCL2);
  l += ((e[0] + e[1]) + (e[2] + e[3])) + ((e[4] + e[5]) + (e[6] + e[7]));
  u32x4 p = {pack2(e[0], e[1]), pack2(e[2], e[3]), pack2(e[4], e[5]), pack2(e[6], e[7])};
  asm volatile("" : "+v"(p), "+v"(l));
  return p;
}

__global__ void __launch_bounds__(256, 1)
tree_attn(const char* __restrict__ Q, const char* __restrict__ K, const char* __restrict__ V, const char* __restrict__ KT,
          const char* __restrict__ VT, const char* __restrict__ MK, char* __restrict__ ws, unsigned short* __restrict__ out) {
  __shared__ __attribute__((aligned(1024))) char smem[LDS_BYTES];
  LAS char* lds = (LAS char*)smem;
  const unsigned lbase = (unsigned)(size_t)lds;
  const int tid = threadIdx.x, lane = tid & 63, wave = __builtin_amdgcn_readfirstlane(tid >> 6);
  const int wg = blockIdx.x;
  const int grp = (wg >> 5) * 8 + (wg & 7), mem = (wg >> 3) & 3;
  const int r32 = lane & 31, hh = lane >> 5;
  const bool leader = (mem == 1) && (wave == 0);
  {
    unsigned long long pc; unsigned len;
    asm volatile("s_getpc_b64 %0\n.Lcode0:\n\ts_mov_b32 %1, .Lcode1-.Lcode0" : "=s"(pc), "=s"(len));
    const unsigned o = min((unsigned)(((wg >> 3) * 4 + wave) & 31) * 1024u, (len & ~15u) - 1024u) + lane * 16;
    asm volatile("global_load_dwordx4 a[240:243], %0, %1" :: "v"(o), "s"(pc & ~15ull) : "memory", "a240", "a241", "a242", "a243");
  }
  unsigned ko[8], qo[4];
#pragma unroll
  for (int i = 0; i < 8; ++i) ko[i] = hh * ROWB + ((r32 ^ swz(2 * i + hh)) << 4);
#pragma unroll
  for (int i = 0; i < 4; ++i) qo[i] = ko[i] ^ ((wave & 1) << 5);
  asm volatile("" : "+v"(ko[0]), "+v"(ko[1]), "+v"(ko[2]), "+v"(ko[3]), "+v"(ko[4]), "+v"(ko[5]), "+v"(ko[6]), "+v"(ko[7]),
                    "+v"(qo[0]), "+v"(qo[1]), "+v"(qo[2]), "+v"(qo[3]));
  const unsigned ring = wave * RINGB;
  const size_t wof = (size_t)grp * (CTX * ROWB) + (mem * 4 + wave) * BLKB;
  const char* src = K + wof;
  unsigned dst = lbase + ring;
#define PIECE_N(k, cnt) dma1<((k) & 3) * 1024, (cnt)>(src + ((k) >> 3) * HALFB + (((k) >> 2) & 1) * 4096, dst + ((k) >> 2) * 4096, ko[(k) & 7])
#define PIECE(k) dma1w<((k) & 3) * 1024>(src + ((k) >> 3) * HALFB + (((k) >> 2) & 1) * 4096, dst + ((k) >> 2) * 4096, ko[(k) & 7], lvl)
  int late = 0, lvl = 2;
  auto quarter = [&](const char* s, unsigned d) {
    dma1<0, 63>(s, d, qo[0]); dma1<1024, 63>(s, d, qo[1]); dma1<2048, 63>(s, d, qo[2]); dma1<3072, 63>(s, d, qo[3]);
  };
  quarter(Q + (size_t)grp * TILEB + wave * 4096, lbase + Q_OFF + wave * 4096);
  if (mem == 0) {
    quarter((wave & 2 ? VT : KT) + (size_t)grp * BLKB + (wave & 1) * 4096, lbase + TREE_OFF + wave * 4096);
    const unsigned mo = (r32 & 15) * (NT * 2) + 8 * hh;
    asm volatile("s_nop 4\n\tglobal_load_dwordx2 a[248:249], %0, %1\n\tglobal_load_dwordx2 a[250:251], %0, %1 offset:16"
                 :: "v"(mo), "s"(MK + (grp >> 3) * (NT * NT * 2)) : "memory", "a248", "a249", "a250", "a251");
  }
  unsigned* cw = (unsigned*)(ws + WS_CTL + grp * 256);
  const unsigned vz = 0;
  if (wave == 0) ctl_add_ret(cw, vz, 0u);
  PIECE_N(0, 63); PIECE_N(1, 63); PIECE_N(2, 63); PIECE_N(3, 63); PIECE_N(4, 63);
  unsigned due = clock_ticks() + PACE0;
  const unsigned krd = r32 * ROWB + ((hh ^ swz(r32)) << 4);
  const int q4 = (lane & 15) >> 2;
  const unsigned vrd = (4 * hh + q4) * ROWB + (q4 << 6) + ((((lane >> 4) & 1) * 32 + (lane & 3) * 8) ^ (hh << 4));
  acc_zero32<0>(); acc_zero32<32>(); acc_zero32<64>(); acc_zero32<96>();
  WAIT_VM(5);
  const unsigned w0 = ctl_result();
  if (leader) ctl_and(cw, vz, W_EPOCH_MASK);
  BARRIER();
  PIECE(5);
  bf16x8 q[16];
#pragma unroll
  for (int st = 0; st < 16; ++st) q[st] = row_frag(lds, Q_OFF + krd, st);
  PIECE(6);
  BARRIER();
  float zm = -__builtin_inff(), zl = 0.f, om = -__builtin_inff();
  if (mem == 0 && wave == 1) {
    unsigned mk[4];
    asm volatile("v_accvgpr_read_b32 %0, a248\n\tv_accvgpr_read_b32 %1, a249\n\tv_accvgpr_read_b32 %2, a250\n\tv_accvgpr_read_b32 %3, a251"
                 : "=v"(mk[0]), "=v"(mk[1]), "=v"(mk[2]), "=v"(mk[3]) :: "memory");
    bf16x8 f[16]; f32x16 s;
#pragma unroll
    for (int st = 0; st < 16; ++st) f[st] = row_frag(lds, TREE_OFF + (krd & (BLKB - 1)), st);
    PIECE(7);
    qk_first(s, f, q); qk_next(s, f + 4, q + 4);
    PIECE(8);
    qk_next(s, f + 8, q + 8); qk_next(s, f + 12, q + 12);
    PIECE(9);
    qk_done(s);
#pragma unroll
    for (int c = 0; c < 8; ++c) f[c] = col_frag(lds, TREE_OFF + vrd, c, BLKB);
#pragma unroll
    for (int r = 0; r < 8; ++r) {
      const float mv = __uint_as_float((r & 1) ? (mk[r >> 1] & 0xffff0000u) : (mk[r >> 1] << 16));
      s[r] = fmaf(1.0f - mv, MASKRAW, s[r]);
    }
    sm_ref<8>(s, zm, zl);
    PIECE(10);
    const u32x4 p = sm_exp<0>(s, zm, zl);
    om = zm;
    PIECE(11);
    pv4<0>(f, p); pv4<4>(f + 4, p);
  } else {
    PIECE(7); PIECE(8); PIECE(9); PIECE(10); PIECE(11);
  }
  PIECE(12); PIECE(13); PIECE(14); PIECE(15);

  auto pace = [&]() {
    const int now = (int)(clock_ticks() - due), step = now - late;
    const int drop = step > JUMP ? step : 0;
    late = min(now - drop, SLIP);
    due += PACE + now - late;
    lvl = level_of(late);
  };
  bool ready = leader;
  const unsigned mt = MT_OFF + wave * (TPW * 128) + r32 * 4;
  unsigned slot = ring;
  u32x4 p0, p1;
  float ow = 0.f;
#pragma unroll 1
  for (int n = 0; n < TPW; ++n) {
    src = n + 1 < TPW ? src + TSTEP : V + wof;
    dst ^= TILEB;
    const bool look = wave == 0 && (leader ? n < 4 : !ready);
    if (look) ctl_add_ret(cw, vz, leader ? W_EPOCH : 0u);
    PIECE(0);
    pace();
    PIECE(1); PIECE(2); PIECE(3); PIECE(4);
    WAIT_VM(5);
    bf16x8 f[16]; f32x16 s;
#pragma unroll
    for (int st = 0; st < 16; ++st) f[st] = row_frag(lds, slot + krd, st);
    if (look) ready = ready || ctl_result() != w0;
    PIECE(5);
    qk_first(s, f, q);
    PIECE(6);
    qk_next(s, f + 4, q + 4);
    PIECE(7);
    qk_next(s, f + 8, q + 8);
    PIECE(8);
    qk_next(s, f + 12, q + 12);
    PIECE(9);
    qk_done(s);
    const float zprev = zm;
    sm_ref<16>(s, zm, zl);
    *(LAS float*)(lds + mt + n * 128) = zm;
    PIECE(10);
    if (n == TPW - 1) {
      BARRIER();
      float M = zm;
#pragma unroll
      for (int d = 1; d < 4; ++d) M = fmaxf(M, *(const LAS float*)(lds + MT_OFF + (((wave + d) & 3) * TPW + n) * 128 + r32 * 4));
      zl *= __builtin_amdgcn_exp2f((zm - M) * SCL2);
      ow = __builtin_amdgcn_exp2f((zprev - M) * SCL2);
      zm = M;
    }
    p0 = sm_exp<0>(s, zm, zl);
    PIECE(11);
    p1 = sm_exp<8>(s, zm, zl);
    PIECE(12);
    if (n < TPW - 1) park_put(n, p0, p1);
    PIECE(13); PIECE(14); PIECE(15);
    slot ^= TILEB;
  }
  zl = xhalf_sum(zl);
  *(LAS f32x2*)(lds + ML_OFF + (wave * 32 + r32) * 8) = f32x2{ow, zl};
  *(LAS u32x4*)(lds + TREE_OFF + wave * 2048 + lane * 16) = p0;
  *(LAS u32x4*)(lds + TREE_OFF + wave * 2048 + 1024 + lane * 16) = p1;
  auto rebase = [&](float tm) {
    if (!__all(tm == om)) {
      if (!__all(om == -__builtin_inff())) acc_scale(__builtin_amdgcn_exp2f((om - tm) * SCL2));
      om = tm;
    }
  };
  u32x4 r0, r1;
#pragma unroll 1
  for (int n = 0;; ++n) {
    src += TSTEP;
    dst ^= TILEB;
    PIECE(0);
    pace();
    const float tm = *(const LAS float*)(lds + mt + n * 128);
    park_get(n, r0, r1);
    PIECE(1);
    rebase(tm);
    PIECE(2); PIECE(3); PIECE(4);
    WAIT_VM(5);
    if (n == TPW - 2) break;
    bf16x8 f[16];
#pragma unroll
    for (int c = 0; c < 8; ++c) f[c] = col_frag(lds, slot + vrd, c, 0);
    PIECE(5);
#pragma unroll
    for (int c = 0; c < 8; ++c) f[8 + c] = col_frag(lds, slot + vrd, c, BLKB);
    PIECE(6);
    pv4<0>(f, r0);
    PIECE(7);
    pv4<4>(f + 4, r0);
    PIECE(8);
    pv4<0>(f + 8, r1);
    PIECE(9);
    pv4<4>(f + 12, r1);
    PIECE(10); PIECE(11); PIECE(12); PIECE(13); PIECE(14); PIECE(15);
    slot ^= TILEB;
  }
  f32x16 ox, oy;
  {
    bf16x8 f[16];
#pragma unroll
    for (int c = 0; c < 8; ++c) { f[c] = col_frag(lds, slot + vrd, c, 0); f[8 + c] = col_frag(lds, slot + vrd, c, BLKB); }
    PIECE_N(5, WEND - 1); PIECE_N(6, WEND - 1); PIECE_N(7, WEND - 1);
    pv4<0>(f, r0); pv4<0>(f + 8, r1);
    PIECE_N(8, WEND - 1); PIECE_N(9, WEND - 1);
    pv4<4>(f + 4, r0); pv4<4>(f + 12, r1);
    PIECE_N(10, WEND - 1); PIECE_N(11, WEND - 1);
  }
#define QUARTER(p) do { const f32x16 a_ = acc_get16<32 * p>(), b_ = acc_get16<32 * p + 16>(); \
    if (wave == p) { ox = a_ * ow; oy = b_ * ow; } \
    else { LAS char* d_ = lds + slot + p * 4096 + lane * 16; \
           *(LAS u32x4*)d_ = pack8(a_, 0); *(LAS u32x4*)(d_ + 1024) = pack8(a_, 8); *(LAS u32x4*)(d_ + 2048) = pack8(b_, 0); *(LAS u32x4*)(d_ + 3072) = pack8(b_, 8); } } while (0)
  QUARTER(0);
  PIECE_N(12, WEND - 1); PIECE_N(13, WEND - 1);
  QUARTER(1);
  PIECE_N(14, WEND - 1); PIECE_N(15, WEND - 1);
  QUARTER(2);
  QUARTER(3);
#undef QUARTER
  WAIT_VM(8);
  BARRIER();
  const unsigned rel = slot - ring, vq = (vrd ^ ((wave & 1) << 7)) + (wave >> 1) * 256;
  float L = zl;
  u32x4 pl[3][2];
  {
    f32x2 ml[3]; u32x4 im[3][4]; bf16x8 f[8];
#pragma unroll
    for (int d = 0; d < 3; ++d) {
      const int w = (wave + 1 + d) & 3;
      ml[d] = *(const LAS f32x2*)(lds + ML_OFF + (w * 32 + r32) * 8);
#pragma unroll
      for (int i = 0; i < 4; ++i) im[d][i] = *(const LAS u32x4*)(lds + w * RINGB + rel + wave * 4096 + lane * 16 + i * 1024);
#pragma unroll
      for (int i = 0; i < 2; ++i) pl[d][i] = *(const LAS u32x4*)(lds + TREE_OFF + w * 2048 + i * 1024 + lane * 16);
    }
#pragma unroll
    for (int d = 0; d < 4; ++d) {
      const unsigned a = ((wave + d) & 3) * RINGB + (rel ^ TILEB) + vq;
      f[2 * d] = col_frag(lds, a, 0, 0); f[2 * d + 1] = col_frag(lds, a, 1, 0);
    }
#define FOLD(d) do { L += ml[d][1]; fma8(ox, 0, ml[d][0], im[d][0]); fma8(ox, 8, ml[d][0], im[d][1]); fma8(oy, 0, ml[d][0], im[d][2]); fma8(oy, 8, ml[d][0], im[d][3]); \
    asm volatile("" : "+v"(ox), "+v"(oy)); } while (0)
    asm volatile("" ::: "memory");
    FOLD(0); FOLD(1); FOLD(2);
#undef FOLD
    pv2(ox, oy, f[0], f[1], p0);
#pragma unroll
    for (int d = 0; d < 3; ++d) pv2(ox, oy, f[2 * d + 2], f[2 * d + 3], pl[d][0]);
  }
  WAIT_VM(0);
  BARRIER();
  {
    bf16x8 f[8];
#pragma unroll
    for (int d = 0; d < 4; ++d) {
      const unsigned a = ((wave + d) & 3) * RINGB + (rel ^ TILEB) + vq;
      f[2 * d] = col_frag(lds, a, 0, BLKB); f[2 * d + 1] = col_frag(lds, a, 1, BLKB);
    }
    pv2(ox, oy, f[0], f[1], p1);
#pragma unroll
    for (int d = 0; d < 3; ++d) pv2(ox, oy, f[2 * d + 2], f[2 * d + 3], pl[d][1]);
    asm volatile("s_nop 15" : "+v"(ox), "+v"(oy));
  }
  const float M = zm;
  float f[4];

  const __amdgpu_buffer_rsrc_t rs = __builtin_amdgcn_make_buffer_rsrc(ws, 0, (int)WS_END, 0x00020000);
  const int ml_off = (int)WS_PML + (grp * NSPL * 32 + r32) * 8;
  const int po_off = (int)WS_PO + grp * NSPL * PARTB + wave * 4096 + lane * 16;
  if (wave == 0 && hh == 0) __builtin_amdgcn_raw_buffer_store_b64(__builtin_bit_cast(u32x2, f32x2{M, L}), rs, ml_off + mem * 256, 0, 16);
  f32x4 x = {}, y = {};
#pragma unroll
  for (int v = 0; v < 4; ++v) {
    const f32x16& o = v < 2 ? ox : oy;
    const int b = 8 * (v & 1);
    if (v == mem) { x = f32x4{o[b], o[b + 1], o[b + 2], o[b + 3]}; y = f32x4{o[b + 4], o[b + 5], o[b + 6], o[b + 7]}; }
    else __builtin_amdgcn_raw_buffer_store_b128(pack8(o, b), rs, po_off + mem * PARTB + v * 1024, 0, 16);
  }
  WAIT_VM(0);
  BARRIER();
  if (wave == 0) {
#pragma unroll 1
    for (int i = 0; i < SPIN_MAX && !ready; ++i) {
      ctl_add_ret(cw, vz, 0u); WAIT_VM(0);
      ready = ctl_result() != w0;
      if (!ready) __builtin_amdgcn_s_sleep(16);
    }
    ctl_add_ret(cw, vz, 1u << mem);
#pragma unroll 1
    for (int i = 0; i < SPIN_MAX; ++i) {
      WAIT_VM(0);
      const unsigned w = ctl_result() | (1u << mem);
      if ((w & 15u) == 15u) break;
      ctl_add_ret(cw, vz, leader ? W_EPOCH : 0u);
    }
  }
  BARRIER();
  f32x2 oml[3]; u32x4 oa[3];
#pragma unroll
  for (int d = 0; d < 3; ++d) {
    const int sm = (mem + 1 + d) & 3;
    oml[d] = __builtin_bit_cast(f32x2, __builtin_amdgcn_raw_buffer_load_b64(rs, ml_off, sm * 256, 16));
    oa[d] = __builtin_amdgcn_raw_buffer_load_b128(rs, po_off, sm * PARTB + mem * 1024, 16);
  }
  const float Mf = fmaxf(fmaxf(M, oml[0][0]), fmaxf(oml[1][0], oml[2][0]));
  f[3] = __builtin_amdgcn_exp2f((M - Mf) * SCL2);
  L *= f[3];
#pragma unroll
  for (int d = 0; d < 3; ++d) { f[d] = __builtin_amdgcn_exp2f((oml[d][0] - Mf) * SCL2); L = fmaf(f[d], oml[d][1], L); }
  const float inv = 1.0f / L;
  x *= f[3] * inv; y *= f[3] * inv;
#pragma unroll
  for (int d = 0; d < 3; ++d) { x += (f[d] * inv) * unpack4(oa[d][0], oa[d][1]); y += (f[d] * inv) * unpack4(oa[d][2], oa[d][3]); }
  const auto rx = __builtin_amdgcn_permlane32_swap(pack2(x[0], x[1]), pack2(y[0], y[1]), false, false);
  const auto ry = __builtin_amdgcn_permlane32_swap(pack2(x[2], x[3]), pack2(y[2], y[3]), false, false);
  *(u32x4*)((char*)out + (size_t)(grp * 32 + r32) * ROWB + wave * 128 + mem * 32 + hh * 16) = u32x4{rx[0], ry[0], rx[1], ry[1]};
  asm volatile(".Lcode1:");
}

extern "C" void kernel_launch(void* const* d_in, const int* in_sizes, int n_in, void* d_out, int out_size, void* d_ws, size_t ws_size,
                              hipStream_t stream) {
  tree_attn<<<NGRP * NSPL, 256, 0, stream>>>((const char*)d_in[0], (const char*)d_in[1], (const char*)d_in[2], (const char*)d_in[3],
                                             (const char*)d_in[4], (const char*)d_in[5], (char*)d_ws, (unsigned short*)d_out);
}
```

```cpp
#define DBG_STAMPS
#include <hip/hip_runtime.h>
#include <hip/hip_bf16.h>

#define LAS __attribute__((address_space(3)))
typedef __bf16 bf16x8 __attribute__((ext_vector_type(8)));
typedef __bf16 bf16x4 __attribute__((ext_vector_type(4)));
typedef __bf16 bf16x2 __attribute__((ext_vector_type(2)));
typedef float f32x16 __attribute__((ext_vector_type(16)));
typedef float f32x4 __attribute__((ext_vector_type(4)));
typedef float f32x2 __attribute__((ext_vector_type(2)));
typedef unsigned u32x4 __attribute__((ext_vector_type(4)));
typedef unsigned u32x2 __attribute__((ext_vector_type(2)));

constexpr int NB = 8, HKV = 8, NT = 16, CTX = 8192, HD = 256;
constexpr int NGRP = NB * HKV;
constexpr int NSPL = 4;
constexpr int ROWB = HD * 2;
constexpr int BLKB = 16 * ROWB;
constexpr int TILEB = 2 * BLKB;
constexpr int RINGB = 2 * TILEB;
constexpr int TPW = CTX / 32 / 16;
constexpr int HALFB = CTX / 2 * ROWB, TSTEP = 16 * BLKB;
constexpr int TREE_OFF = 4 * RINGB;
constexpr int Q_OFF = TREE_OFF + TILEB;
constexpr int LDS_BYTES = Q_OFF + TILEB;
constexpr int MT_OFF = Q_OFF;
constexpr int ML_OFF = MT_OFF + 4 * TPW * 128;
static_assert(LDS_BYTES <= 160 * 1024 && ML_OFF + 1024 <= LDS_BYTES && TPW == 16, "layout");
constexpr float SCL2 = 0.0625f * 1.4426950408889634f;
constexpr float MASKRAW = -1.0e9f * 16.0f;
constexpr float DEFER = 8.0f / SCL2;

constexpr int WIN = 5;
constexpr int WEND = 10;
constexpr int PACE = 215, PACE0 = 285;
constexpr int SLIP = 200, JUMP = 212;
constexpr int LV0 = -100, LV1 = -50, LV2 = 0, LV3 = 40, LV4 = 90;

constexpr unsigned W_EPOCH = 1u << 29, W_EPOCH_MASK = 0xE0000000u;
constexpr int SPIN_MAX = 1 << 16;
constexpr int PARTB = 32 * HD * 2;
constexpr size_t WS_CTL = 0, WS_PML = 65536, WS_PO = WS_PML + (size_t)NGRP * NSPL * 32 * 8;
constexpr size_t WS_END = WS_PO + (size_t)NGRP * NSPL * PARTB;

template <int OFF, int CNT> __device__ __forceinline__ void dma1(const char* src, unsigned lds, unsigned v) {
  unsigned keep;
  __builtin_amdgcn_sched_barrier(0);
  asm volatile("s_mov_b32 %0, m0\n\ts_mov_b32 m0, %2\n\ts_waitcnt vmcnt(%5)\n\ts_nop 2\n\t"
               "global_load_lds_dwordx4 %3, %1 offset:%4 nt\n\ts_mov_b32 m0, %0"
               : "=&s"(keep) : "s"(src), "s"(lds), "v"(v), "i"(OFF), "i"(CNT) : "memory");
  __builtin_amdgcn_sched_barrier(0);
}
template <int OFF> __device__ __forceinline__ void dma1w(const char* src, unsigned lds, unsigned v, int lvl) {
  unsigned keep;
  __builtin_amdgcn_sched_barrier(0);
  asm volatile("s_mov_b32 %0, m0\n\ts_mov_b32 m0, %2\n\t"
               "s_cmp_gt_i32 %5, 4\n\ts_waitcnt vmcnt(%6+3)\n\ts_cbranch_scc1 .Lb_%=\n\t"
               "s_cmp_gt_i32 %5, 3\n\ts_waitcnt vmcnt(%6+2)\n\ts_cbranch_scc1 .Lb_%=\n\t"
               "s_cmp_gt_i32 %5, 2\n\ts_waitcnt vmcnt(%6+1)\n\ts_cbranch_scc1 .Lb_%=\n\t"
               "s_cmp_gt_i32 %5, 1\n\ts_waitcnt vmcnt(%6)\n\ts_cbranch_scc1 .Lb_%=\n\t"
               "s_cmp_gt_i32 %5, 0\n\ts_nop 7\n\ts_cbranch_scc1 .Lb_%=\n\t"
               "s_waitcnt vmcnt(%6-1)\n\t.Lb_%=:\n\t"
               "global_load_lds_dwordx4 %3, %1 offset:%4 nt\n\ts_mov_b32 m0, %0"
               : "=&s"(keep) : "s"(src), "s"(lds), "v"(v), "i"(OFF), "s"(lvl), "i"(WIN - 1) : "memory", "scc");
  __builtin_amdgcn_sched_barrier(0);
}
__device__ __forceinline__ int level_of(int late) {
  int l;
  asm volatile("s_mov_b32 %0, 0\n\ts_cmp_gt_i32 %1, %2\n\ts_addc_u32 %0, %0, 0\n\ts_cmp_gt_i32 %1, %3\n\ts_addc_u32 %0, %0, 0\n\ts_cmp_gt_i32 %1, %4\n\ts_addc_u32 %0, %0, 0\n\t"
               "s_cmp_gt_i32 %1, %5\n\ts_addc_u32 %0, %0, 0\n\ts_cmp_gt_i32 %1, %6\n\ts_addc_u32 %0, %0, 0"
               : "=&s"(l) : "s"(late), "i"(LV0), "i"(LV1), "i"(LV2), "i"(LV3), "i"(LV4) : "scc");
  return l;
}
__device__ __forceinline__ unsigned clock_ticks() {
  unsigned long long t;
  asm volatile("s_memrealtime %0\n\ts_waitcnt lgkmcnt(0)" : "=s"(t) :: "memory");
  return (unsigned)t;
}
#define WAIT_VM(n) asm volatile("s_waitcnt vmcnt(%0)" :: "i"(n) : "memory")
#define WAIT_LGKM0() asm volatile("s_waitcnt lgkmcnt(0)" ::: "memory")
#define BARRIER() do { WAIT_LGKM0(); __builtin_amdgcn_s_barrier(); asm volatile("" ::: "memory"); } while (0)

__device__ __forceinline__ void ctl_add_ret(unsigned* w, unsigned zero, unsigned inc) {
  unsigned long long ex;
  asm volatile("v_accvgpr_write_b32 a254, %3\n\ts_mov_b64 %0, exec\n\ts_mov_b64 exec, 1\n\ts_nop 2\n\t"
               "global_atomic_add a255, %2, a254, %1 sc0\n\ts_mov_b64 exec, %0\n\ts_nop 1"
               : "=&s"(ex) : "s"(w), "v"(zero), "v"(inc) : "memory", "a254", "a255");
}
__device__ __forceinline__ unsigned ctl_result() {
  unsigned t, r;
  asm volatile("v_accvgpr_read_b32 %0, a255\n\ts_nop 1\n\tv_readfirstlane_b32 %1, %0\n\ts_nop 4" : "=&v"(t), "=s"(r) :: "memory");
  return r;
}
__device__ __forceinline__ void ctl_and(unsigned* w, unsigned zero, unsigned m) {
  unsigned long long ex;
  asm volatile("s_mov_b64 %0, exec\n\ts_mov_b64 exec, 1\n\ts_nop 2\n\tglobal_atomic_and %2, %3, %1\n\ts_mov_b64 exec, %0\n\ts_nop 1"
               : "=&s"(ex) : "s"(w), "v"(zero), "v"(m) : "memory");
}
#define PARK_HEAD "s_getpc_b64 vcc\n\t.Lp0_%=:\n\ts_add_u32 vcc_lo, vcc_lo, %8\n\ts_addc_u32 vcc_hi, vcc_hi, 0\n\t" \
                  "s_add_u32 vcc_lo, vcc_lo, .Lp1_%=-.Lp0_%=\n\ts_addc_u32 vcc_hi, vcc_hi, 0\n\ts_setpc_b64 vcc\n\t.Lp1_%=:\n\t.set .Lpi_%=, 128\n\t.rept 15\n\t"
#define PARK_TAIL "s_branch .Lp2_%=\n\t.set .Lpi_%=, .Lpi_%=+8\n\t.endr\n\t.Lp2_%=:\n\ts_nop 1"
__device__ __forceinline__ void park_put(int j, const u32x4& p0, const u32x4& p1) {
  asm volatile(PARK_HEAD
               "v_accvgpr_write_b32 a[.Lpi_%=], %0\n\tv_accvgpr_write_b32 a[.Lpi_%=+1], %1\n\tv_accvgpr_write_b32 a[.Lpi_%=+2], %2\n\t"
               "v_accvgpr_write_b32 a[.Lpi_%=+3], %3\n\tv_accvgpr_write_b32 a[.Lpi_%=+4], %4\n\tv_accvgpr_write_b32 a[.Lpi_%=+5], %5\n\t"
               "v_accvgpr_write_b32 a[.Lpi_%=+6], %6\n\tv_accvgpr_write_b32 a[.Lpi_%=+7], %7\n\t" PARK_TAIL
               :: "v"(p0[0]), "v"(p0[1]), "v"(p0[2]), "v"(p0[3]), "v"(p1[0]), "v"(p1[1]), "v"(p1[2]), "v"(p1[3]), "s"(j * 68)
               : "memory", "vcc", "scc");
}
__device__ __forceinline__ void park_get(int j, u32x4& p0, u32x4& p1) {
  asm volatile(PARK_HEAD
               "v_accvgpr_read_b32 %0, a[.Lpi_%=]\n\tv_accvgpr_read_b32 %1, a[.Lpi_%=+1]\n\tv_accvgpr_read_b32 %2, a[.Lpi_%=+2]\n\t"
               "v_accvgpr_read_b32 %3, a[.Lpi_%=+3]\n\tv_accvgpr_read_b32 %4, a[.Lpi_%=+4]\n\tv_accvgpr_read_b32 %5, a[.Lpi_%=+5]\n\t"
               "v_accvgpr_read_b32 %6, a[.Lpi_%=+6]\n\tv_accvgpr_read_b32 %7, a[.Lpi_%=+7]\n\t" PARK_TAIL
               : "=v"(p0[0]), "=v"(p0[1]), "=v"(p0[2]), "=v"(p0[3]), "=v"(p1[0]), "=v"(p1[1]), "=v"(p1[2]), "=v"(p1[3]) : "s"(j * 68)
               : "memory", "vcc", "scc");
}
template <int B> __device__ __forceinline__ void acc_zero32() {
  asm volatile(".set .Lz_%=, %c0\n\t.rept 32\n\tv_accvgpr_write_b32 a[.Lz_%=], 0\n\t.set .Lz_%=, .Lz_%=+1\n\t.endr" :: "i"(B) : "memory", "a0", "a127", "a247");
}
__device__ __forceinline__ void acc_scale(float alpha) {
  float t0, t1, t2, t3;
  asm volatile("s_nop 15\n\t.set .Ls_%=, 0\n\t.rept 32\n\t"
               "v_accvgpr_read_b32 %0, a[.Ls_%=]\n\tv_accvgpr_read_b32 %1, a[.Ls_%=+1]\n\tv_accvgpr_read_b32 %2, a[.Ls_%=+2]\n\tv_accvgpr_read_b32 %3, a[.Ls_%=+3]\n\t"
               "v_mul_f32 %0, %0, %4\n\tv_mul_f32 %1, %1, %4\n\tv_mul_f32 %2, %2, %4\n\tv_mul_f32 %3, %3, %4\n\t"
               "v_accvgpr_write_b32 a[.Ls_%=], %0\n\tv_accvgpr_write_b32 a[.Ls_%=+1], %1\n\tv_accvgpr_write_b32 a[.Ls_%=+2], %2\n\tv_accvgpr_write_b32 a[.Ls_%=+3], %3\n\t"
               ".set .Ls_%=, .Ls_%=+4\n\t.endr\n\ts_nop 1"
               : "=&v"(t0), "=&v"(t1), "=&v"(t2), "=&v"(t3) : "v"(alpha) : "memory");
}
#define M_(a, b) "v_mfma_f32_32x32x16_bf16 %0, %" #a ", %" #b ", %0\n\t"
__device__ __forceinline__ void qk_first(f32x16& s, const bf16x8* f, const bf16x8* q) {
  asm volatile("s_nop 1\n\tv_mfma_f32_32x32x16_bf16 %0, %1, %5, 0\n\t" M_(2, 6) M_(3, 7) M_(4, 8)
               : "=&v"(s) : "v"(f[0]), "v"(f[1]), "v"(f[2]), "v"(f[3]), "v"(q[0]), "v"(q[1]), "v"(q[2]), "v"(q[3]));
}
__device__ __forceinline__ void qk_next(f32x16& s, const bf16x8* f, const bf16x8* q) {
  asm volatile("s_nop 1\n\t" M_(1, 5) M_(2, 6) M_(3, 7) M_(4, 8)
               : "+v"(s) : "v"(f[0]), "v"(f[1]), "v"(f[2]), "v"(f[3]), "v"(q[0]), "v"(q[1]), "v"(q[2]), "v"(q[3]));
}
#undef M_
__device__ __forceinline__ void qk_done(f32x16& s) { asm volatile("s_nop 15" : "+v"(s)); }
template <int C> __device__ __forceinline__ void pv4(const bf16x8* f, const u32x4& p) {
  asm volatile("s_nop 1\n\t"
               "v_mfma_f32_32x32x16_bf16 a[%c5:%c5+15], %0, %4, a[%c5:%c5+15]\n\t"
               "v_mfma_f32_32x32x16_bf16 a[%c5+16:%c5+31], %1, %4, a[%c5+16:%c5+31]\n\t"
               "v_mfma_f32_32x32x16_bf16 a[%c5+32:%c5+47], %2, %4, a[%c5+32:%c5+47]\n\t"
               "v_mfma_f32_32x32x16_bf16 a[%c5+48:%c5+63], %3, %4, a[%c5+48:%c5+63]"
               :: "v"(f[0]), "v"(f[1]), "v"(f[2]), "v"(f[3]), "v"(p), "i"(16 * C) : "memory");
}
__device__ __forceinline__ void pv2(f32x16& x, f32x16& y, const bf16x8& v0, const bf16x8& v1, const u32x4& p) {
  asm volatile("s_nop 1\n\tv_mfma_f32_32x32x16_bf16 %0, %2, %4, %0\n\tv_mfma_f32_32x32x16_bf16 %1, %3, %4, %1"
               : "+v"(x), "+v"(y) : "v"(v0), "v"(v1), "v"(p));
}
template <int B> __device__ __forceinline__ f32x16 acc_get16() {
  float r[16];
  asm volatile("v_accvgpr_read_b32 %0, a[%c16]\n\tv_accvgpr_read_b32 %1, a[%c16+1]\n\tv_accvgpr_read_b32 %2, a[%c16+2]\n\tv_accvgpr_read_b32 %3, a[%c16+3]\n\t"
               "v_accvgpr_read_b32 %4, a[%c16+4]\n\tv_accvgpr_read_b32 %5, a[%c16+5]\n\tv_accvgpr_read_b32 %6, a[%c16+6]\n\tv_accvgpr_read_b32 %7, a[%c16+7]\n\t"
               "v_accvgpr_read_b32 %8, a[%c16+8]\n\tv_accvgpr_read_b32 %9, a[%c16+9]\n\tv_accvgpr_read_b32 %10, a[%c16+10]\n\tv_accvgpr_read_b32 %11, a[%c16+11]\n\t"
               "v_accvgpr_read_b32 %12, a[%c16+12]\n\tv_accvgpr_read_b32 %13, a[%c16+13]\n\tv_accvgpr_read_b32 %14, a[%c16+14]\n\tv_accvgpr_read_b32 %15, a[%c16+15]"
               : "=v"(r[0]), "=v"(r[1]), "=v"(r[2]), "=v"(r[3]), "=v"(r[4]), "=v"(r[5]), "=v"(r[6]), "=v"(r[7]), "=v"(r[8]), "=v"(r[9]),
                 "=v"(r[10]), "=v"(r[11]), "=v"(r[12]), "=v"(r[13]), "=v"(r[14]), "=v"(r[15]) : "i"(B) : "memory");
  f32x16 x;
#pragma unroll
  for (int i = 0; i < 16; ++i) x[i] = r[i];
  return x;
}

__device__ __forceinline__ float xhalf_max(float x) {
  auto r = __builtin_amdgcn_permlane32_swap(__float_as_uint(x), __float_as_uint(x), false, false);
  return fmaxf(__uint_as_float(r[0]), __uint_as_float(r[1]));
}
__device__ __forceinline__ float xhalf_sum(float x) {
  auto r = __builtin_amdgcn_permlane32_swap(__float_as_uint(x), __float_as_uint(x), false, false);
  return __uint_as_float(r[0]) + __uint_as_float(r[1]);
}
__device__ __forceinline__ unsigned pack2(float a, float b) {
  return __builtin_bit_cast(unsigned, __builtin_convertvector((f32x2){a, b}, bf16x2));
}
__device__ __forceinline__ f32x4 unpack4(unsigned lo, unsigned hi) {
  return f32x4{__uint_as_float(lo << 16), __uint_as_float(lo & 0xffff0000u), __uint_as_float(hi << 16), __uint_as_float(hi & 0xffff0000u)};
}
__device__ __forceinline__ u32x4 pack8(const f32x16& x, int b) {
  return u32x4{pack2(x[b], x[b + 1]), pack2(x[b + 2], x[b + 3]), pack2(x[b + 4], x[b + 5]), pack2(x[b + 6], x[b + 7])};
}
__device__ __forceinline__ void fma8(f32x16& x, int b, float f, const u32x4& d) {
#pragma unroll
  for (int i = 0; i < 4; ++i) {
    x[b + 2 * i] = fmaf(f, __uint_as_float(d[i] << 16), x[b + 2 * i]);
    x[b + 2 * i + 1] = fmaf(f, __uint_as_float(d[i] & 0xffff0000u), x[b + 2 * i + 1]);
  }
}
__device__ __forceinline__ unsigned swz(unsigned row) { return ((row & 3) << 2) | ((row >> 2) & 3); }
__device__ __forceinline__ bf16x8 row_frag(LAS char* lds, unsigned rd, int st) {
  return *(const LAS bf16x8*)(lds + ((rd ^ ((st & 7) << 5)) + (st >> 3) * 256));
}
__device__ __forceinline__ bf16x8 col_frag(LAS char* lds, unsigned vrd, int c, int koff) {
  const int o = (c >> 2) * 256 + koff;
  const bf16x4 lo = __builtin_amdgcn_ds_read_tr16_b64_v4bf16((LAS bf16x4*)(lds + ((vrd ^ ((c & 3) << 6)) + o)));
  const bf16x4 hi = __builtin_amdgcn_ds_read_tr16_b64_v4bf16((LAS bf16x4*)(lds + ((vrd ^ ((c & 3) << 6) ^ 32) + o + 8 * ROWB)));
  return __builtin_shufflevector(lo, hi, 0, 1, 2, 3, 4, 5, 6, 7);
}
template <int NR> __device__ __forceinline__ void sm_ref(const f32x16& s, float& m, float& l) {
  float mx = s[0];
#pragma unroll
  for (int r = 1; r < NR; ++r) mx = fmaxf(mx, s[r]);
  mx = xhalf_max(mx);
  if (!__all(mx <= m + DEFER)) {
    const float mn = fmaxf(m, mx);
    l *= __builtin_amdgcn_exp2f((m - mn) * SCL2);
    m = mn;
  }
  asm volatile("" : "+v"(m), "+v"(l));
}
template <int B> __device__ __forceinline__ u32x4 sm_exp(const f32x16& s, float m, float& l) {
  float e[8];
#pragma unroll
  for (int r = 0; r < 8; ++r) e[r] = __builtin_amdgcn_exp2f((s[B + r] - m) * SCL2);
  l += ((e[0] + e[1]) + (e[2] + e[3])) + ((e[4] + e[5]) + (e[6] + e[7]));
  u32x4 p = {pack2(e[0], e[1]), pack2(e[2], e[3]), pack2(e[4], e[5]), pack2(e[6], e[7])};
  asm volatile("" : "+v"(p), "+v"(l));
  return p;
}

__global__ void __launch_bounds__(256, 1)
tree_attn(const char* __restrict__ Q, const char* __restrict__ K, const char* __restrict__ V, const char* __restrict__ KT,
          const char* __restrict__ VT, const char* __restrict__ MK, char* __restrict__ ws, unsigned short* __restrict__ out) {
  __shared__ __attribute__((aligned(1024))) char smem[LDS_BYTES];
  LAS char* lds = (LAS char*)smem;
  const unsigned lbase = (unsigned)(size_t)lds;
  const int tid = threadIdx.x, lane = tid & 63, wave = __builtin_amdgcn_readfirstlane(tid >> 6);
  const int wg = blockIdx.x;
  const int grp = (wg >> 5) * 8 + (wg & 7), mem = (wg >> 3) & 3;
  const int r32 = lane & 31, hh = lane >> 5;
  const bool leader = (mem == 1) && (wave == 0);
  unsigned long long ts0 = __builtin_amdgcn_s_memrealtime(), ts1 = 0, ts2 = 0; int ntl = 0, nslow = 0, npoll = 0;
  {
    unsigned long long pc; unsigned len;
    asm volatile("s_getpc_b64 %0\n.Lcode0:\n\ts_mov_b32 %1, .Lcode1-.Lcode0" : "=s"(pc), "=s"(len));
    const unsigned o = min((unsigned)(((wg >> 3) * 4 + wave) & 31) * 1024u, (len & ~15u) - 1024u) + lane * 16;
    asm volatile("global_load_dwordx4 a[240:243], %0, %1" :: "v"(o), "s"(pc & ~15ull) : "memory", "a240", "a241", "a242", "a243");
  }
  unsigned ko[8], qo[4];
#pragma unroll
  for (int i = 0; i < 8; ++i) ko[i] = hh * ROWB + ((r32 ^ swz(2 * i + hh)) << 4);
#pragma unroll
  for (int i = 0; i < 4; ++i) qo[i] = ko[i] ^ ((wave & 1) << 5);
  asm volatile("" : "+v"(ko[0]), "+v"(ko[1]), "+v"(ko[2]), "+v"(ko[3]), "+v"(ko[4]), "+v"(ko[5]), "+v"(ko[6]), "+v"(ko[7]),
                    "+v"(qo[0]), "+v"(qo[1]), "+v"(qo[2]), "+v"(qo[3]));
  const unsigned ring = wave * RINGB;
  const size_t wof = (size_t)grp * (CTX * ROWB) + (mem * 4 + wave) * BLKB;
  const char* src = K + wof;
  unsigned dst = lbase + ring;
#define PIECE_N(k, cnt) dma1<((k) & 3) * 1024, (cnt)>(src + ((k) >> 3) * HALFB + (((k) >> 2) & 1) * 4096, dst + ((k) >> 2) * 4096, ko[(k) & 7])
#define PIECE(k) dma1w<((k) & 3) * 1024>(src + ((k) >> 3) * HALFB + (((k) >> 2) & 1) * 4096, dst + ((k) >> 2) * 4096, ko[(k) & 7], lvl)
  int late = 0, lvl = 2;
  auto quarter = [&](const char* s, unsigned d) {
    dma1<0, 63>(s, d, qo[0]); dma1<1024, 63>(s, d, qo[1]); dma1<2048, 63>(s, d, qo[2]); dma1<3072, 63>(s, d, qo[3]);
  };
  quarter(Q + (size_t)grp * TILEB + wave * 4096, lbase + Q_OFF + wave * 4096);
  if (mem == 0) {
    quarter((wave & 2 ? VT : KT) + (size_t)grp * BLKB + (wave & 1) * 4096, lbase + TREE_OFF + wave * 4096);
    const unsigned mo = (r32 & 15) * (NT * 2) + 8 * hh;
    asm volatile("s_nop 4\n\tglobal_load_dwordx2 a[248:249], %0, %1\n\tglobal_load_dwordx2 a[250:251], %0, %1 offset:16"
                 :: "v"(mo), "s"(MK + (grp >> 3) * (NT * NT * 2)) : "memory", "a248", "a249", "a250", "a251");
  }
  unsigned* cw = (unsigned*)(ws + WS_CTL + grp * 256);
  const unsigned vz = 0;
  if (wave == 0) ctl_add_ret(cw, vz, 0u);
  PIECE_N(0, 63); PIECE_N(1, 63); PIECE_N(2, 63); PIECE_N(3, 63); PIECE_N(4, 63);
  unsigned due = clock_ticks() + PACE0;
  const unsigned krd = r32 * ROWB + ((hh ^ swz(r32)) << 4);
  const int q4 = (lane & 15) >> 2;
  const unsigned vrd = (4 * hh + q4) * ROWB + (q4 << 6) + ((((lane >> 4) & 1) * 32 + (lane & 3) * 8) ^ (hh << 4));
  acc_zero32<0>(); acc_zero32<32>(); acc_zero32<64>(); acc_zero32<96>();
  WAIT_VM(5);
  const unsigned w0 = ctl_result();
  if (leader) ctl_and(cw, vz, W_EPOCH_MASK);
  BARRIER();
  PIECE(5);
  bf16x8 q[16];
#pragma unroll
  for (int st = 0; st < 16; ++st) q[st] = row_frag(lds, Q_OFF + krd, st);
  PIECE(6);
  BARRIER();
  float zm = -__builtin_inff(), zl = 0.f, om = -__builtin_inff();
  if (mem == 0 && wave == 1) {
    unsigned mk[4];
    asm volatile("v_accvgpr_read_b32 %0, a248\n\tv_accvgpr_read_b32 %1, a249\n\tv_accvgpr_read_b32 %2, a250\n\tv_accvgpr_read_b32 %3, a251"
                 : "=v"(mk[0]), "=v"(mk[1]), "=v"(mk[2]), "=v"(mk[3]) :: "memory");
    bf16x8 f[16]; f32x16 s;
#pragma unroll
    for (int st = 0; st < 16; ++st) f[st] = row_frag(lds, TREE_OFF + (krd & (BLKB - 1)), st);
    PIECE(7);
    qk_first(s, f, q); qk_next(s, f + 4, q + 4);
    PIECE(8);
    qk_next(s, f + 8, q + 8); qk_next(s, f + 12, q + 12);
    PIECE(9);
    qk_done(s);
#pragma unroll
    for (int c = 0; c < 8; ++c) f[c] = col_frag(lds, TREE_OFF + vrd, c, BLKB);
#pragma unroll
    for (int r = 0; r < 8; ++r) {
      const float mv = __uint_as_float((r & 1) ? (mk[r >> 1] & 0xffff0000u) : (mk[r >> 1] << 16));
      s[r] = fmaf(1.0f - mv, MASKRAW, s[r]);
    }
    sm_ref<8>(s, zm, zl);
    PIECE(10);
    const u32x4 p = sm_exp<0>(s, zm, zl);
    om = zm;
    PIECE(11);
    pv4<0>(f, p); pv4<4>(f + 4, p);
  } else {
    PIECE(7); PIECE(8); PIECE(9); PIECE(10); PIECE(11);
  }
  PIECE(12); PIECE(13); PIECE(14); PIECE(15);

  auto pace = [&]() {
    const int now = (int)(clock_ticks() - due), step = now - late;
    const int drop = step > JUMP ? step : 0;
    late = min(now - drop, SLIP);
    due += PACE + now - late;
    lvl = level_of(late);
  };
  bool ready = leader;
  const unsigned mt = MT_OFF + wave * (TPW * 128) + r32 * 4;
  unsigned slot = ring;
  u32x4 p0, p1;
  float ow = 0.f;
#pragma unroll 1
  for (int n = 0; n < TPW; ++n) {
    src = n + 1 < TPW ? src + TSTEP : V + wof;
    dst ^= TILEB;
    const bool look = wave == 0 && (leader ? n < 4 : !ready);
    if (look) ctl_add_ret(cw, vz, leader ? W_EPOCH : 0u);
    PIECE(0);
    pace();
    PIECE(1); PIECE(2); PIECE(3); PIECE(4);
    WAIT_VM(5);
    if (n == 0) { ts1 = __builtin_amdgcn_s_memrealtime(); __builtin_amdgcn_s_waitcnt(0xC07F); }
    bf16x8 f[16]; f32x16 s;
#pragma unroll
    for (int st = 0; st < 16; ++st) f[st] = row_frag(lds, slot + krd, st);
    if (look) ready = ready || ctl_result() != w0;
    PIECE(5);
    qk_first(s, f, q);
    PIECE(6);
    qk_next(s, f + 4, q + 4);
    PIECE(7);
    qk_next(s, f + 8, q + 8);
    PIECE(8);
    qk_next(s, f + 12, q + 12);
    PIECE(9);
    qk_done(s);
    const float zprev = zm;
    sm_ref<16>(s, zm, zl);
    *(LAS float*)(lds + mt + n * 128) = zm;
    PIECE(10);
    if (n == TPW - 1) {
      BARRIER();
      float M = zm;
#pragma unroll
      for (int d = 1; d < 4; ++d) M = fmaxf(M, *(const LAS float*)(lds + MT_OFF + (((wave + d) & 3) * TPW + n) * 128 + r32 * 4));
      zl *= __builtin_amdgcn_exp2f((zm - M) * SCL2);
      ow = __builtin_amdgcn_exp2f((zprev - M) * SCL2);
      zm = M;
    }
    p0 = sm_exp<0>(s, zm, zl);
    PIECE(11);
    p1 = sm_exp<8>(s, zm, zl);
    PIECE(12);
    if (n < TPW - 1) park_put(n, p0, p1);
    PIECE(13); PIECE(14); PIECE(15);
    slot ^= TILEB;
  }
  zl = xhalf_sum(zl);
  *(LAS f32x2*)(lds + ML_OFF + (wave * 32 + r32) * 8) = f32x2{ow, zl};
  *(LAS u32x4*)(lds + TREE_OFF + wave * 2048 + lane * 16) = p0;
  *(LAS u32x4*)(lds + TREE_OFF + wave * 2048 + 1024 + lane * 16) = p1;
  auto rebase = [&](float tm) {
    if (!__all(tm == om)) {
      if (!__all(om == -__builtin_inff())) acc_scale(__builtin_amdgcn_exp2f((om - tm) * SCL2));
      om = tm;
    }
  };
  u32x4 r0, r1;
#pragma unroll 1
  for (int n = 0;; ++n) {
    src += TSTEP;
    dst ^= TILEB;
    PIECE(0);
    pace();
    const float tm = *(const LAS float*)(lds + mt + n * 128);
    park_get(n, r0, r1);
    PIECE(1);
    rebase(tm);
    PIECE(2); PIECE(3); PIECE(4);
    WAIT_VM(5);
    if (n == TPW - 2) break;
    bf16x8 f[16];
#pragma unroll
    for (int c = 0; c < 8; ++c) f[c] = col_frag(lds, slot + vrd, c, 0);
    PIECE(5);
#pragma unroll
    for (int c = 0; c < 8; ++c) f[8 + c] = col_frag(lds, slot + vrd, c, BLKB);
    PIECE(6);
    pv4<0>(f, r0);
    PIECE(7);
    pv4<4>(f + 4, r0);
    PIECE(8);
    pv4<0>(f + 8, r1);
    PIECE(9);
    pv4<4>(f + 12, r1);
    PIECE(10); PIECE(11); PIECE(12); PIECE(13); PIECE(14); PIECE(15);
    slot ^= TILEB;
  }
  f32x16 ox, oy;
  {
    bf16x8 f[16];
#pragma unroll
    for (int c = 0; c < 8; ++c) { f[c] = col_frag(lds, slot + vrd, c, 0); f[8 + c] = col_frag(lds, slot + vrd, c, BLKB); }
    PIECE_N(5, WEND - 1); PIECE_N(6, WEND - 1); PIECE_N(7, WEND - 1);
    pv4<0>(f, r0); pv4<0>(f + 8, r1);
    PIECE_N(8, WEND - 1); PIECE_N(9, WEND - 1);
    pv4<4>(f + 4, r0); pv4<4>(f + 12, r1);
    PIECE_N(10, WEND - 1); PIECE_N(11, WEND - 1);
  }
#define QUARTER(p) do { const f32x16 a_ = acc_get16<32 * p>(), b_ = acc_get16<32 * p + 16>(); \
    if (wave == p) { ox = a_ * ow; oy = b_ * ow; } \
    else { LAS char* d_ = lds + slot + p * 4096 + lane * 16; \
           *(LAS u32x4*)d_ = pack8(a_, 0); *(LAS u32x4*)(d_ + 1024) = pack8(a_, 8); *(LAS u32x4*)(d_ + 2048) = pack8(b_, 0); *(LAS u32x4*)(d_ + 3072) = pack8(b_, 8); } } while (0)
  QUARTER(0);
  PIECE_N(12, WEND - 1); PIECE_N(13, WEND - 1);
  QUARTER(1);
  PIECE_N(14, WEND - 1); PIECE_N(15, WEND - 1);
  QUARTER(2);
  QUARTER(3);
#undef QUARTER
  WAIT_VM(8);
  BARRIER();
  const unsigned rel = slot - ring, vq = (vrd ^ ((wave & 1) << 7)) + (wave >> 1) * 256;
  float L = zl;
  u32x4 pl[3][2];
  {
    f32x2 ml[3]; u32x4 im[3][4]; bf16x8 f[8];
#pragma unroll
    for (int d = 0; d < 3; ++d) {
      const int w = (wave + 1 + d) & 3;
      ml[d] = *(const LAS f32x2*)(lds + ML_OFF + (w * 32 + r32) * 8);
#pragma unroll
      for (int i = 0; i < 4; ++i) im[d][i] = *(const LAS u32x4*)(lds + w * RINGB + rel + wave * 4096 + lane * 16 + i * 1024);
#pragma unroll
      for (int i = 0; i < 2; ++i) pl[d][i] = *(const LAS u32x4*)(lds + TREE_OFF + w * 2048 + i * 1024 + lane * 16);
    }
#pragma unroll
    for (int d = 0; d < 4; ++d) {
      const unsigned a = ((wave + d) & 3) * RINGB + (rel ^ TILEB) + vq;
      f[2 * d] = col_frag(lds, a, 0, 0); f[2 * d + 1] = col_frag(lds, a, 1, 0);
    }
#define FOLD(d) do { L += ml[d][1]; fma8(ox, 0, ml[d][0], im[d][0]); fma8(ox, 8, ml[d][0], im[d][1]); fma8(oy, 0, ml[d][0], im[d][2]); fma8(oy, 8, ml[d][0], im[d][3]); \
    asm volatile("" : "+v"(ox), "+v"(oy)); } while (0)
    asm volatile("" ::: "memory");
    FOLD(0); FOLD(1); FOLD(2);
#undef FOLD
    pv2(ox, oy, f[0], f[1], p0);
#pragma unroll
    for (int d = 0; d < 3; ++d) pv2(ox, oy, f[2 * d + 2], f[2 * d + 3], pl[d][0]);
  }
  WAIT_VM(0);
  BARRIER();
  {
    bf16x8 f[8];
#pragma unroll
    for (int d = 0; d < 4; ++d) {
      const unsigned a = ((wave + d) & 3) * RINGB + (rel ^ TILEB) + vq;
      f[2 * d] = col_frag(lds, a, 0, BLKB); f[2 * d + 1] = col_frag(lds, a, 1, BLKB);
    }
    pv2(ox, oy, f[0], f[1], p1);
#pragma unroll
    for (int d = 0; d < 3; ++d) pv2(ox, oy, f[2 * d + 2], f[2 * d + 3], pl[d][1]);
    asm volatile("s_nop 15" : "+v"(ox), "+v"(oy));
  }
  ts2 = __builtin_amdgcn_s_memrealtime(); __builtin_amdgcn_s_waitcnt(0xC07F);
  const float M = zm;
  float f[4];

  const __amdgpu_buffer_rsrc_t rs = __builtin_amdgcn_make_buffer_rsrc(ws, 0, (int)WS_END, 0x00020000);
  const int ml_off = (int)WS_PML + (grp * NSPL * 32 + r32) * 8;
  const int po_off = (int)WS_PO + grp * NSPL * PARTB + wave * 4096 + lane * 16;
  if (wave == 0 && hh == 0) __builtin_amdgcn_raw_buffer_store_b64(__builtin_bit_cast(u32x2, f32x2{M, L}), rs, ml_off + mem * 256, 0, 16);
  f32x4 x = {}, y = {};
#pragma unroll
  for (int v = 0; v < 4; ++v) {
    const f32x16& o = v < 2 ? ox : oy;
    const int b = 8 * (v & 1);
    if (v == mem) { x = f32x4{o[b], o[b + 1], o[b + 2], o[b + 3]}; y = f32x4{o[b + 4], o[b + 5], o[b + 6], o[b + 7]}; }
    else __builtin_amdgcn_raw_buffer_store_b128(pack8(o, b), rs, po_off + mem * PARTB + v * 1024, 0, 16);
  }
  WAIT_VM(0);
  BARRIER();
  if (wave == 0) {
#pragma unroll 1
    for (int i = 0; i < SPIN_MAX && !ready; ++i) {
      ctl_add_ret(cw, vz, 0u); WAIT_VM(0);
      ready = ctl_result() != w0;
      ++nslow;
      if (!ready) __builtin_amdgcn_s_sleep(16);
    }
    ctl_add_ret(cw, vz, 1u << mem);
#pragma unroll 1
    for (int i = 0; i < SPIN_MAX; ++i) {
      WAIT_VM(0);
      const unsigned w = ctl_result() | (1u << mem);
      ++npoll;
      if ((w & 15u) == 15u) break;
      ctl_add_ret(cw, vz, leader ? W_EPOCH : 0u);
    }
  }
  BARRIER();
  f32x2 oml[3]; u32x4 oa[3];
#pragma unroll
  for (int d = 0; d < 3; ++d) {
    const int sm = (mem + 1 + d) & 3;
    oml[d] = __builtin_bit_cast(f32x2, __builtin_amdgcn_raw_buffer_load_b64(rs, ml_off, sm * 256, 16));
    oa[d] = __builtin_amdgcn_raw_buffer_load_b128(rs, po_off, sm * PARTB + mem * 1024, 16);
  }
  const float Mf = fmaxf(fmaxf(M, oml[0][0]), fmaxf(oml[1][0], oml[2][0]));
  f[3] = __builtin_amdgcn_exp2f((M - Mf) * SCL2);
  L *= f[3];
#pragma unroll
  for (int d = 0; d < 3; ++d) { f[d] = __builtin_amdgcn_exp2f((oml[d][0] - Mf) * SCL2); L = fmaf(f[d], oml[d][1], L); }
  const float inv = 1.0f / L;
  x *= f[3] * inv; y *= f[3] * inv;
#pragma unroll
  for (int d = 0; d < 3; ++d) { x += (f[d] * inv) * unpack4(oa[d][0], oa[d][1]); y += (f[d] * inv) * unpack4(oa[d][2], oa[d][3]); }
  const auto rx = __builtin_amdgcn_permlane32_swap(pack2(x[0], x[1]), pack2(y[0], y[1]), false, false);
  const auto ry = __builtin_amdgcn_permlane32_swap(pack2(x[2], x[3]), pack2(y[2], y[3]), false, false);
  *(u32x4*)((char*)out + (size_t)(grp * 32 + r32) * ROWB + wave * 128 + mem * 32 + hh * 16) = u32x4{rx[0], ry[0], rx[1], ry[1]};
  asm volatile(".Lcode1:");
  WAIT_VM(0);
  if (lane == 0) { unsigned long long* sp = (unsigned long long*)(ws + (512ull << 20)) + (wg * 4 + wave) * 8;
    sp[0] = ts0; sp[1] = ts1; sp[2] = ts2; sp[3] = __builtin_amdgcn_s_memrealtime(); sp[4] = __builtin_amdgcn_s_getreg((31 << 11) | 20) & 7; sp[5] = ntl; sp[6] = nslow; sp[7] = npoll; }
}

__global__ void enc_k(const char* ws) {
  const unsigned long long t0 = __builtin_amdgcn_s_memrealtime();
  const unsigned long long* h = (const unsigned long long*)(ws + (512ull << 20));
  unsigned long long mn = ~0ull, mx3 = 0, mx2 = 0, s1 = 0, s2 = 0, mx0 = 0, sl = 0;
  for (int i = 0; i < 1024; ++i) { const unsigned long long* p = h + i * 8; mn = p[0] < mn ? p[0] : mn; mx0 = p[0] > mx0 ? p[0] : mx0; mx3 = p[3] > mx3 ? p[3] : mx3; mx2 = p[2] > mx2 ? p[2] : mx2; s1 += p[1]; s2 += p[2]; sl += p[6]; }
  unsigned long long m = 0;
  if (6 == 0) m = mx3 - mn;
  if (6 == 1) m = s1 / 1024 - mn;
  if (6 == 2) m = s2 / 1024 - mn;
  if (6 == 3) m = mx2 - mn;
  if (6 == 4) m = t0 - mx3;
  if (6 == 5) m = sl;
  if (6 == 6) m = 0;
  if (6 == 7) { m = mn - h[1024 * 8]; }
  if (6 == 8) { mn = h[1024 * 8]; m = 0; }
  if (6 == 9) m = mx0 - mn;
  if (m > 20000) m = 20000;
  while (__builtin_amdgcn_s_memrealtime() - mn < 100000ull + 20ull * m) __builtin_amdgcn_s_sleep(2);
}
extern "C" void kernel_launch(void* const* d_in, const int* in_sizes, int n_in, void* d_out, int out_size, void* d_ws, size_t ws_size,
                              hipStream_t stream) {
  tree_attn<<<NGRP * NSPL, 256, 0, stream>>>((const char*)d_in[0], (const char*)d_in[1], (const char*)d_in[2], (const char*)d_in[3],
                                             (const char*)d_in[4], (const char*)d_in[5], (char*)d_ws, (unsigned short*)d_out);
  enc_k<<<1, 1, 0, stream>>>((const char*)d_ws);
}
```

```cpp
#include <hip/hip_runtime.h>
#include <hip/hip_bf16.h>

#define LAS __attribute__((address_space(3)))
typedef __bf16 bf16x8 __attribute__((ext_vector_type(8)));
typedef __bf16 bf16x4 __attribute__((ext_vector_type(4)));
typedef __bf16 bf16x2 __attribute__((ext_vector_type(2)));
typedef float f32x16 __attribute__((ext_vector_type(16)));
typedef float f32x4 __attribute__((ext_vector_type(4)));
typedef float f32x2 __attribute__((ext_vector_type(2)));
typedef unsigned u32x4 __attribute__((ext_vector_type(4)));
typedef unsigned u32x2 __attribute__((ext_vector_type(2)));

constexpr int NB = 8, HKV = 8, NT = 16, CTX = 8192, HD = 256;
constexpr int NGRP = NB * HKV;
constexpr int NSPL = 4;
constexpr int ROWB = HD * 2;
constexpr int BLKB = 16 * ROWB;
constexpr int TILEB = 2 * BLKB;
constexpr int RINGB = 2 * TILEB;
constexpr int TPW = CTX / 32 / 16;
constexpr int HALFB = CTX / 2 * ROWB, TSTEP = 16 * BLKB;
constexpr int TREE_OFF = 4 * RINGB;
constexpr int Q_OFF = TREE_OFF + TILEB;
constexpr int LDS_BYTES = Q_OFF + TILEB;
constexpr int MT_OFF = Q_OFF;
constexpr int ML_OFF = MT_OFF + 4 * TPW * 128;
constexpr int SH_OFF = ML_OFF + 1024;
static_assert(LDS_BYTES <= 160 * 1024 && SH_OFF + 16 <= LDS_BYTES && TPW == 16, "layout");
constexpr float SCL2 = 0.0625f * 1.4426950408889634f;
constexpr float MASKRAW = -1.0e9f * 16.0f;
constexpr float DEFER = 8.0f / SCL2;

constexpr int WIN = 5;
constexpr int WEND = 10;
constexpr int PACE = 217, PACE0 = 270;
constexpr int SLIP = 120, JUMP = 212;
constexpr int LV0 = -100, LV1 = -50, LV2 = 0, LV3 = 60;
constexpr int NEAR = 300;

constexpr unsigned W_EPOCH = 1u << 29, W_EPOCH_MASK = 0xE0000000u;
constexpr int SPIN_MAX = 1 << 16;
constexpr int PARTB = 32 * HD * 2;
constexpr size_t WS_CTL = 0, WS_T0 = 32768, WS_PML = 65536, WS_PO = WS_PML + (size_t)NGRP * NSPL * 32 * 8;
constexpr size_t WS_END = WS_PO + (size_t)NGRP * NSPL * PARTB;

template <int OFF, int CNT> __device__ __forceinline__ void dma1(const char* src, unsigned lds, unsigned v) {
  unsigned keep;
  __builtin_amdgcn_sched_barrier(0);
  asm volatile("s_mov_b32 %0, m0\n\ts_mov_b32 m0, %2\n\ts_waitcnt vmcnt(%5)\n\ts_nop 2\n\t"
               "global_load_lds_dwordx4 %3, %1 offset:%4 nt\n\ts_mov_b32 m0, %0"
               : "=&s"(keep) : "s"(src), "s"(lds), "v"(v), "i"(OFF), "i"(CNT) : "memory");
  __builtin_amdgcn_sched_barrier(0);
}
template <int OFF> __device__ __forceinline__ void dma1w(const char* src, unsigned lds, unsigned v, int lvl) {
  unsigned keep;
  __builtin_amdgcn_sched_barrier(0);
  asm volatile("s_mov_b32 %0, m0\n\ts_mov_b32 m0, %2\n\t"
               "s_cmp_gt_i32 %5, 3\n\ts_waitcnt vmcnt(%6+2)\n\ts_cbranch_scc1 .Lb_%=\n\t"
               "s_cmp_gt_i32 %5, 2\n\ts_waitcnt vmcnt(%6+1)\n\ts_cbranch_scc1 .Lb_%=\n\t"
               "s_cmp_gt_i32 %5, 1\n\ts_waitcnt vmcnt(%6)\n\ts_cbranch_scc1 .Lb_%=\n\t"
               "s_cmp_gt_i32 %5, 0\n\ts_nop 7\n\ts_cbranch_scc1 .Lb_%=\n\t"
               "s_waitcnt vmcnt(%6-1)\n\t.Lb_%=:\n\t"
               "global_load_lds_dwordx4 %3, %1 offset:%4 nt\n\ts_mov_b32 m0, %0"
               : "=&s"(keep) : "s"(src), "s"(lds), "v"(v), "i"(OFF), "s"(lvl), "i"(WIN - 1) : "memory", "scc");
  __builtin_amdgcn_sched_barrier(0);
}
__device__ __forceinline__ int level_of(int late) {
  int l;
  asm volatile("s_mov_b32 %0, 0\n\ts_cmp_gt_i32 %1, %2\n\ts_addc_u32 %0, %0, 0\n\ts_cmp_gt_i32 %1, %3\n\ts_addc_u32 %0, %0, 0\n\t"
               "s_cmp_gt_i32 %1, %4\n\ts_addc_u32 %0, %0, 0\n\ts_cmp_gt_i32 %1, %5\n\ts_addc_u32 %0, %0, 0"
               : "=&s"(l) : "s"(late), "i"(LV0), "i"(LV1), "i"(LV2), "i"(LV3) : "scc");
  return l;
}
__device__ __forceinline__ unsigned clock_ticks() {
  unsigned long long t;
  asm volatile("s_memrealtime %0\n\ts_waitcnt lgkmcnt(0)" : "=s"(t) :: "memory");
  return (unsigned)t;
}
#define WAIT_VM(n) asm volatile("s_waitcnt vmcnt(%0)" :: "i"(n) : "memory")
#define WAIT_LGKM0() asm volatile("s_waitcnt lgkmcnt(0)" ::: "memory")
#define BARRIER() do { WAIT_LGKM0(); __builtin_amdgcn_s_barrier(); asm volatile("" ::: "memory"); } while (0)

__device__ __forceinline__ void ctl_add_ret(unsigned* w, unsigned zero, unsigned inc) {
  unsigned long long ex;
  asm volatile("v_accvgpr_write_b32 a254, %3\n\ts_mov_b64 %0, exec\n\ts_mov_b64 exec, 1\n\ts_nop 2\n\t"
               "global_atomic_add a255, %2, a254, %1 sc0\n\ts_mov_b64 exec, %0\n\ts_nop 1"
               : "=&s"(ex) : "s"(w), "v"(zero), "v"(inc) : "memory", "a254", "a255");
}
__device__ __forceinline__ unsigned ctl_result() {
  unsigned t, r;
  asm volatile("v_accvgpr_read_b32 %0, a255\n\ts_nop 1\n\tv_readfirstlane_b32 %1, %0\n\ts_nop 4" : "=&v"(t), "=s"(r) :: "memory");
  return r;
}
__device__ __forceinline__ void ctl_and(unsigned* w, unsigned zero, unsigned m) {
  unsigned long long ex;
  asm volatile("s_mov_b64 %0, exec\n\ts_mov_b64 exec, 1\n\ts_nop 2\n\tglobal_atomic_and %2, %3, %1\n\ts_mov_b64 exec, %0\n\ts_nop 1"
               : "=&s"(ex) : "s"(w), "v"(zero), "v"(m) : "memory");
}
#define PARK_HEAD "s_getpc_b64 vcc\n\t.Lp0_%=:\n\ts_add_u32 vcc_lo, vcc_lo, %8\n\ts_addc_u32 vcc_hi, vcc_hi, 0\n\t" \
                  "s_add_u32 vcc_lo, vcc_lo, .Lp1_%=-.Lp0_%=\n\ts_addc_u32 vcc_hi, vcc_hi, 0\n\ts_setpc_b64 vcc\n\t.Lp1_%=:\n\t.set .Lpi_%=, 128\n\t.rept 15\n\t"
#define PARK_TAIL "s_branch .Lp2_%=\n\t.set .Lpi_%=, .Lpi_%=+8\n\t.endr\n\t.Lp2_%=:\n\ts_nop 1"
__device__ __forceinline__ void park_put(int j, const u32x4& p0, const u32x4& p1) {
  asm volatile(PARK_HEAD
               "v_accvgpr_write_b32 a[.Lpi_%=], %0\n\tv_accvgpr_write_b32 a[.Lpi_%=+1], %1\n\tv_accvgpr_write_b32 a[.Lpi_%=+2], %2\n\t"
               "v_accvgpr_write_b32 a[.Lpi_%=+3], %3\n\tv_accvgpr_write_b32 a[.Lpi_%=+4], %4\n\tv_accvgpr_write_b32 a[.Lpi_%=+5], %5\n\t"
               "v_accvgpr_write_b32 a[.Lpi_%=+6], %6\n\tv_accvgpr_write_b32 a[.Lpi_%=+7], %7\n\t" PARK_TAIL
               :: "v"(p0[0]), "v"(p0[1]), "v"(p0[2]), "v"(p0[3]), "v"(p1[0]), "v"(p1[1]), "v"(p1[2]), "v"(p1[3]), "s"(j * 68)
               : "memory", "vcc", "scc");
}
__device__ __forceinline__ void park_get(int j, u32x4& p0, u32x4& p1) {
  asm volatile(PARK_HEAD
               "v_accvgpr_read_b32 %0, a[.Lpi_%=]\n\tv_accvgpr_read_b32 %1, a[.Lpi_%=+1]\n\tv_accvgpr_read_b32 %2, a[.Lpi_%=+2]\n\t"
               "v_accvgpr_read_b32 %3, a[.Lpi_%=+3]\n\tv_accvgpr_read_b32 %4, a[.Lpi_%=+4]\n\tv_accvgpr_read_b32 %5, a[.Lpi_%=+5]\n\t"
               "v_accvgpr_read_b32 %6, a[.Lpi_%=+6]\n\tv_accvgpr_read_b32 %7, a[.Lpi_%=+7]\n\t" PARK_TAIL
               : "=v"(p0[0]), "=v"(p0[1]), "=v"(p0[2]), "=v"(p0[3]), "=v"(p1[0]), "=v"(p1[1]), "=v"(p1[2]), "=v"(p1[3]) : "s"(j * 68)
               : "memory", "vcc", "scc");
}
template <int B> __device__ __forceinline__ void acc_zero32() {
  asm volatile(".set .Lz_%=, %c0\n\t.rept 32\n\tv_accvgpr_write_b32 a[.Lz_%=], 0\n\t.set .Lz_%=, .Lz_%=+1\n\t.endr" :: "i"(B) : "memory", "a0", "a127", "a247");
}
__device__ __forceinline__ void acc_scale(float alpha) {
  float t0, t1, t2, t3;
  asm volatile("s_nop 15\n\t.set .Ls_%=, 0\n\t.rept 32\n\t"
               "v_accvgpr_read_b32 %0, a[.Ls_%=]\n\tv_accvgpr_read_b32 %1, a[.Ls_%=+1]\n\tv_accvgpr_read_b32 %2, a[.Ls_%=+2]\n\tv_accvgpr_read_b32 %3, a[.Ls_%=+3]\n\t"
               "v_mul_f32 %0, %0, %4\n\tv_mul_f32 %1, %1, %4\n\tv_mul_f32 %2, %2, %4\n\tv_mul_f32 %3, %3, %4\n\t"
               "v_accvgpr_write_b32 a[.Ls_%=], %0\n\tv_accvgpr_write_b32 a[.Ls_%=+1], %1\n\tv_accvgpr_write_b32 a[.Ls_%=+2], %2\n\tv_accvgpr_write_b32 a[.Ls_%=+3], %3\n\t"
               ".set .Ls_%=, .Ls_%=+4\n\t.endr\n\ts_nop 1"
               : "=&v"(t0), "=&v"(t1), "=&v"(t2), "=&v"(t3) : "v"(alpha) : "memory");
}
#define M_(a, b) "v_mfma_f32_32x32x16_bf16 %0, %" #a ", %" #b ", %0\n\t"
__device__ __forceinline__ void qk_first(f32x16& s, const bf16x8* f, const bf16x8* q) {
  asm volatile("s_nop 1\n\tv_mfma_f32_32x32x16_bf16 %0, %1, %5, 0\n\t" M_(2, 6) M_(3, 7) M_(4, 8)
               : "=&v"(s) : "v"(f[0]), "v"(f[1]), "v"(f[2]), "v"(f[3]), "v"(q[0]), "v"(q[1]), "v"(q[2]), "v"(q[3]));
}
__device__ __forceinline__ void qk_next(f32x16& s, const bf16x8* f, const bf16x8* q) {
  asm volatile("s_nop 1\n\t" M_(1, 5) M_(2, 6) M_(3, 7) M_(4, 8)
               : "+v"(s) : "v"(f[0]), "v"(f[1]), "v"(f[2]), "v"(f[3]), "v"(q[0]), "v"(q[1]), "v"(q[2]), "v"(q[3]));
}
#undef M_
__device__ __forceinline__ void qk_done(f32x16& s) { asm volatile("s_nop 15" : "+v"(s)); }
template <int C> __device__ __forceinline__ void pv4(const bf16x8* f, const u32x4& p) {
  asm volatile("s_nop 1\n\t"
               "v_mfma_f32_32x32x16_bf16 a[%c5:%c5+15], %0, %4, a[%c5:%c5+15]\n\t"
               "v_mfma_f32_32x32x16_bf16 a[%c5+16:%c5+31], %1, %4, a[%c5+16:%c5+31]\n\t"
               "v_mfma_f32_32x32x16_bf16 a[%c5+32:%c5+47], %2, %4, a[%c5+32:%c5+47]\n\t"
               "v_mfma_f32_32x32x16_bf16 a[%c5+48:%c5+63], %3, %4, a[%c5+48:%c5+63]"
               :: "v"(f[0]), "v"(f[1]), "v"(f[2]), "v"(f[3]), "v"(p), "i"(16 * C) : "memory");
}
__device__ __forceinline__ void pv2(f32x16& x, f32x16& y, const bf16x8& v0, const bf16x8& v1, const u32x4& p) {
  asm volatile("s_nop 1\n\tv_mfma_f32_32x32x16_bf16 %0, %2, %4, %0\n\tv_mfma_f32_32x32x16_bf16 %1, %3, %4, %1"
               : "+v"(x), "+v"(y) : "v"(v0), "v"(v1), "v"(p));
}
template <int B> __device__ __forceinline__ f32x16 acc_get16() {
  float r[16];
  asm volatile("v_accvgpr_read_b32 %0, a[%c16]\n\tv_accvgpr_read_b32 %1, a[%c16+1]\n\tv_accvgpr_read_b32 %2, a[%c16+2]\n\tv_accvgpr_read_b32 %3, a[%c16+3]\n\t"
               "v_accvgpr_read_b32 %4, a[%c16+4]\n\tv_accvgpr_read_b32 %5, a[%c16+5]\n\tv_accvgpr_read_b32 %6, a[%c16+6]\n\tv_accvgpr_read_b32 %7, a[%c16+7]\n\t"
               "v_accvgpr_read_b32 %8, a[%c16+8]\n\tv_accvgpr_read_b32 %9, a[%c16+9]\n\tv_accvgpr_read_b32 %10, a[%c16+10]\n\tv_accvgpr_read_b32 %11, a[%c16+11]\n\t"
               "v_accvgpr_read_b32 %12, a[%c16+12]\n\tv_accvgpr_read_b32 %13, a[%c16+13]\n\tv_accvgpr_read_b32 %14, a[%c16+14]\n\tv_accvgpr_read_b32 %15, a[%c16+15]"
               : "=v"(r[0]), "=v"(r[1]), "=v"(r[2]), "=v"(r[3]), "=v"(r[4]), "=v"(r[5]), "=v"(r[6]), "=v"(r[7]), "=v"(r[8]), "=v"(r[9]),
                 "=v"(r[10]), "=v"(r[11]), "=v"(r[12]), "=v"(r[13]), "=v"(r[14]), "=v"(r[15]) : "i"(B) : "memory");
  f32x16 x;
#pragma unroll
  for (int i = 0; i < 16; ++i) x[i] = r[i];
  return x;
}

__device__ __forceinline__ float xhalf_max(float x) {
  auto r = __builtin_amdgcn_permlane32_swap(__float_as_uint(x), __float_as_uint(x), false, false);
  return fmaxf(__uint_as_float(r[0]), __uint_as_float(r[1]));
}
__device__ __forceinline__ float xhalf_sum(float x) {
  auto r = __builtin_amdgcn_permlane32_swap(__float_as_uint(x), __float_as_uint(x), false, false);
  return __uint_as_float(r[0]) + __uint_as_float(r[1]);
}
__device__ __forceinline__ unsigned pack2(float a, float b) {
  return __builtin_bit_cast(unsigned, __builtin_convertvector((f32x2){a, b}, bf16x2));
}
__device__ __forceinline__ f32x4 unpack4(unsigned lo, unsigned hi) {
  return f32x4{__uint_as_float(lo << 16), __uint_as_float(lo & 0xffff0000u), __uint_as_float(hi << 16), __uint_as_float(hi & 0xffff0000u)};
}
__device__ __forceinline__ u32x4 pack8(const f32x16& x, int b) {
  return u32x4{pack2(x[b], x[b + 1]), pack2(x[b + 2], x[b + 3]), pack2(x[b + 4], x[b + 5]), pack2(x[b + 6], x[b + 7])};
}
__device__ __forceinline__ void fma8(f32x16& x, int b, float f, const u32x4& d) {
#pragma unroll
  for (int i = 0; i < 4; ++i) {
    x[b + 2 * i] = fmaf(f, __uint_as_float(d[i] << 16), x[b + 2 * i]);
    x[b + 2 * i + 1] = fmaf(f, __uint_as_float(d[i] & 0xffff0000u), x[b + 2 * i + 1]);
  }
}
__device__ __forceinline__ unsigned swz(unsigned row) { return ((row & 3) << 2) | ((row >> 2) & 3); }
__device__ __forceinline__ bf16x8 row_frag(LAS char* lds, unsigned rd, int st) {
  return *(const LAS bf16x8*)(lds + ((rd ^ ((st & 7) << 5)) + (st >> 3) * 256));
}
__device__ __forceinline__ bf16x8 col_frag(LAS char* lds, unsigned vrd, int c, int koff) {
  const int o = (c >> 2) * 256 + koff;
  const bf16x4 lo = __builtin_amdgcn_ds_read_tr16_b64_v4bf16((LAS bf16x4*)(lds + ((vrd ^ ((c & 3) << 6)) + o)));
  const bf16x4 hi = __builtin_amdgcn_ds_read_tr16_b64_v4bf16((LAS bf16x4*)(lds + ((vrd ^ ((c & 3) << 6) ^ 32) + o + 8 * ROWB)));
  return __builtin_shufflevector(lo, hi, 0, 1, 2, 3, 4, 5, 6, 7);
}
template <int NR> __device__ __forceinline__ void sm_ref(const f32x16& s, float& m, float& l) {
  float mx = s[0];
#pragma unroll
  for (int r = 1; r < NR; ++r) mx = fmaxf(mx, s[r]);
  mx = xhalf_max(mx);
  if (!__all(mx <= m + DEFER)) {
    const float mn = fmaxf(m, mx);
    l *= __builtin_amdgcn_exp2f((m - mn) * SCL2);
    m = mn;
  }
  asm volatile("" : "+v"(m), "+v"(l));
}
template <int B> __device__ __forceinline__ u32x4 sm_exp(const f32x16& s, float m, float& l) {
  float e[8];
#pragma unroll
  for (int r = 0; r < 8; ++r) e[r] = __builtin_amdgcn_exp2f((s[B + r] - m) * SCL2);
  l += ((e[0] + e[1]) + (e[2] + e[3])) + ((e[4] + e[5]) + (e[6] + e[7]));
  u32x4 p = {pack2(e[0], e[1]), pack2(e[2], e[3]), pack2(e[4], e[5]), pack2(e[6], e[7])};
  asm volatile("" : "+v"(p), "+v"(l));
  return p;
}

__global__ void __launch_bounds__(256, 1)
tree_attn(const char* __restrict__ Q, const char* __restrict__ K, const char* __restrict__ V, const char* __restrict__ KT,
          const char* __restrict__ VT, const char* __restrict__ MK, char* __restrict__ ws, unsigned short* __restrict__ out) {
  __shared__ __attribute__((aligned(1024))) char smem[LDS_BYTES];
  LAS char* lds = (LAS char*)smem;
  const unsigned lbase = (unsigned)(size_t)lds;
  const int tid = threadIdx.x, lane = tid & 63, wave = __builtin_amdgcn_readfirstlane(tid >> 6);
  const int wg = blockIdx.x;
  const int grp = (wg >> 5) * 8 + (wg & 7), mem = (wg >> 3) & 3;
  const int r32 = lane & 31, hh = lane >> 5;
  const bool leader = (mem == 1) && (wave == 0);
  {
    unsigned long long pc; unsigned len;
    asm volatile("s_getpc_b64 %0\n.Lcode0:\n\ts_mov_b32 %1, .Lcode1-.Lcode0" : "=s"(pc), "=s"(len));
    const unsigned o = min((unsigned)(((wg >> 3) * 4 + wave) & 31) * 1024u, (len & ~15u) - 1024u) + lane * 16;
    asm volatile("global_load_dwordx4 a[240:243], %0, %1" :: "v"(o), "s"(pc & ~15ull) : "memory", "a240", "a241", "a242", "a243");
  }
  unsigned ko[8], qo[4];
#pragma unroll
  for (int i = 0; i < 8; ++i) ko[i] = hh * ROWB + ((r32 ^ swz(2 * i + hh)) << 4);
#pragma unroll
  for (int i = 0; i < 4; ++i) qo[i] = ko[i] ^ ((wave & 1) << 5);
  asm volatile("" : "+v"(ko[0]), "+v"(ko[1]), "+v"(ko[2]), "+v"(ko[3]), "+v"(ko[4]), "+v"(ko[5]), "+v"(ko[6]), "+v"(ko[7]),
                    "+v"(qo[0]), "+v"(qo[1]), "+v"(qo[2]), "+v"(qo[3]));
  const unsigned ring = wave * RINGB;
  const size_t wof = (size_t)grp * (CTX * ROWB) + (mem * 4 + wave) * BLKB;
  const char* src = K + wof;
  unsigned dst = lbase + ring;
#define PIECE_N(k, cnt) dma1<((k) & 3) * 1024, (cnt)>(src + ((k) >> 3) * HALFB + (((k) >> 2) & 1) * 4096, dst + ((k) >> 2) * 4096, ko[(k) & 7])
#define PIECE(k) dma1w<((k) & 3) * 1024>(src + ((k) >> 3) * HALFB + (((k) >> 2) & 1) * 4096, dst + ((k) >> 2) * 4096, ko[(k) & 7], lvl)
  int late = 0, lvl = 2;
  auto quarter = [&](const char* s, unsigned d) {
    dma1<0, 63>(s, d, qo[0]); dma1<1024, 63>(s, d, qo[1]); dma1<2048, 63>(s, d, qo[2]); dma1<3072, 63>(s, d, qo[3]);
  };
  quarter(Q + (size_t)grp * TILEB + wave * 4096, lbase + Q_OFF + wave * 4096);
  if (mem == 0) {
    quarter((wave & 2 ? VT : KT) + (size_t)grp * BLKB + (wave & 1) * 4096, lbase + TREE_OFF + wave * 4096);
    const unsigned mo = (r32 & 15) * (NT * 2) + 8 * hh;
    asm volatile("s_nop 4\n\tglobal_load_dwordx2 a[248:249], %0, %1\n\tglobal_load_dwordx2 a[250:251], %0, %1 offset:16"
                 :: "v"(mo), "s"(MK + (grp >> 3) * (NT * NT * 2)) : "memory", "a248", "a249", "a250", "a251");
  }
  unsigned* cw = (unsigned*)(ws + WS_CTL + grp * 256);
  const unsigned vz = 0;
  if (wave == 0) ctl_add_ret(cw, vz, 0u);
  PIECE_N(0, 63); PIECE_N(1, 63); PIECE_N(2, 63); PIECE_N(3, 63); PIECE_N(4, 63);
  unsigned due = clock_ticks() + PACE0;
  const char* t0p = ws + WS_T0 + (wg >> 3) * 64;
  const unsigned due0 = due;
  if (wave == 3) {
    unsigned long long ex;
    asm volatile("s_mov_b64 %0, exec\n\ts_mov_b64 exec, 1\n\ts_nop 2\n\tglobal_store_dword %2, %3, %1 sc1\n\ts_mov_b64 exec, %0\n\ts_nop 1"
                 : "=&s"(ex) : "s"(t0p), "v"((wg & 7) * 4), "v"(due0) : "memory");
  }
  const unsigned krd = r32 * ROWB + ((hh ^ swz(r32)) << 4);
  const int q4 = (lane & 15) >> 2;
  const unsigned vrd = (4 * hh + q4) * ROWB + (q4 << 6) + ((((lane >> 4) & 1) * 32 + (lane & 3) * 8) ^ (hh << 4));
  acc_zero32<0>(); acc_zero32<32>(); acc_zero32<64>(); acc_zero32<96>();
  WAIT_VM(5);
  const unsigned w0 = ctl_result();
  if (leader) ctl_and(cw, vz, W_EPOCH_MASK);
  BARRIER();
  PIECE(5);
  bf16x8 q[16];
#pragma unroll
  for (int st = 0; st < 16; ++st) q[st] = row_frag(lds, Q_OFF + krd, st);
  PIECE(6);
  BARRIER();
  if (wave == 3) *(LAS int*)(lds + SH_OFF) = 0;
  float zm = -__builtin_inff(), zl = 0.f, om = -__builtin_inff();
  if (mem == 0 && wave == 1) {
    unsigned mk[4];
    asm volatile("v_accvgpr_read_b32 %0, a248\n\tv_accvgpr_read_b32 %1, a249\n\tv_accvgpr_read_b32 %2, a250\n\tv_accvgpr_read_b32 %3, a251"
                 : "=v"(mk[0]), "=v"(mk[1]), "=v"(mk[2]), "=v"(mk[3]) :: "memory");
    bf16x8 f[16]; f32x16 s;
#pragma unroll
    for (int st = 0; st < 16; ++st) f[st] = row_frag(lds, TREE_OFF + (krd & (BLKB - 1)), st);
    PIECE(7);
    qk_first(s, f, q); qk_next(s, f + 4, q + 4);
    PIECE(8);
    qk_next(s, f + 8, q + 8); qk_next(s, f + 12, q + 12);
    PIECE(9);
    qk_done(s);
#pragma unroll
    for (int c = 0; c < 8; ++c) f[c] = col_frag(lds, TREE_OFF + vrd, c, BLKB);
#pragma unroll
    for (int r = 0; r < 8; ++r) {
      const float mv = __uint_as_float((r & 1) ? (mk[r >> 1] & 0xffff0000u) : (mk[r >> 1] << 16));
      s[r] = fmaf(1.0f - mv, MASKRAW, s[r]);
    }
    sm_ref<8>(s, zm, zl);
    PIECE(10);
    const u32x4 p = sm_exp<0>(s, zm, zl);
    om = zm;
    PIECE(11);
    pv4<0>(f, p); pv4<4>(f + 4, p);
  } else {
    PIECE(7); PIECE(8); PIECE(9); PIECE(10); PIECE(11);
  }
  PIECE(12); PIECE(13); PIECE(14); PIECE(15);

  auto pace = [&]() {
    const int now = (int)(clock_ticks() - due), step = now - late;
    const int drop = step > JUMP ? step : 0;
    late = min(now - drop, SLIP);
    lvl = level_of(late);
    due += PACE + now - late;
  };
  bool ready = leader;
  const unsigned mt = MT_OFF + wave * (TPW * 128) + r32 * 4;
  unsigned slot = ring;
  u32x4 p0, p1;
  float ow = 0.f;
#pragma unroll 1
  for (int n = 0; n < TPW; ++n) {
    src = n + 1 < TPW ? src + TSTEP : V + wof;
    dst ^= TILEB;
    const bool look = wave == 0 && (leader ? n < 4 : !ready);
    if (look) ctl_add_ret(cw, vz, leader ? W_EPOCH : 0u);
    if (n == 0 && wave == 3)
      asm volatile("s_nop 4\n\tglobal_load_dword a252, %0, %1 sc1" :: "v"((lane & 7) * 4), "s"(t0p) : "memory", "a252");
    if (n == 2) {
      const int sh = __builtin_amdgcn_readfirstlane(*(volatile LAS int*)(lds + SH_OFF));
      if (sh > -NEAR && sh < NEAR) { due += sh; late -= sh; }
    }
    PIECE(0);
    pace();
    PIECE(1); PIECE(2); PIECE(3); PIECE(4);
    WAIT_VM(5);
    bf16x8 f[16]; f32x16 s;
#pragma unroll
    for (int st = 0; st < 16; ++st) f[st] = row_frag(lds, slot + krd, st);
    if (look) ready = ready || ctl_result() != w0;
    if (n == 0 && wave == 3) {
      int t;
      asm volatile("v_accvgpr_read_b32 %0, a252\n\ts_nop 1" : "=v"(t) :: "memory");
      const int d = t - (int)due0;
      const bool near = d > -NEAR && d < NEAR;
      float sum = near ? (float)d : 0.f, cnt = near ? 1.f : 0.f;
#pragma unroll
      for (int m = 1; m < 8; m <<= 1) { sum += __shfl_xor(sum, m); cnt += __shfl_xor(cnt, m); }
      *(volatile LAS int*)(lds + SH_OFF) = (int)(sum * __builtin_amdgcn_rcpf(fmaxf(cnt, 1.f)));
    }
    PIECE(5);
    qk_first(s, f, q);
    PIECE(6);
    qk_next(s, f + 4, q + 4);
    PIECE(7);
    qk_next(s, f + 8, q + 8);
    PIECE(8);
    qk_next(s, f + 12, q + 12);
    PIECE(9);
    qk_done(s);
    const float zprev = zm;
    sm_ref<16>(s, zm, zl);
    *(LAS float*)(lds + mt + n * 128) = zm;
    PIECE(10);
    if (n == TPW - 1) {
      BARRIER();
      float M = zm;
#pragma unroll
      for (int d = 1; d < 4; ++d) M = fmaxf(M, *(const LAS float*)(lds + MT_OFF + (((wave + d) & 3) * TPW + n) * 128 + r32 * 4));
      zl *= __builtin_amdgcn_exp2f((zm - M) * SCL2);
      ow = __builtin_amdgcn_exp2f((zprev - M) * SCL2);
      zm = M;
    }
    p0 = sm_exp<0>(s, zm, zl);
    PIECE(11);
    p1 = sm_exp<8>(s, zm, zl);
    PIECE(12);
    if (n < TPW - 1) park_put(n, p0, p1);
    PIECE(13); PIECE(14); PIECE(15);
    slot ^= TILEB;
  }
  zl = xhalf_sum(zl);
  *(LAS f32x2*)(lds + ML_OFF + (wave * 32 + r32) * 8) = f32x2{ow, zl};
  *(LAS u32x4*)(lds + TREE_OFF + wave * 2048 + lane * 16) = p0;
  *(LAS u32x4*)(lds + TREE_OFF + wave * 2048 + 1024 + lane * 16) = p1;
  auto rebase = [&](float tm) {
    if (!__all(tm == om)) {
      if (!__all(om == -__builtin_inff())) acc_scale(__builtin_amdgcn_exp2f((om - tm) * SCL2));
      om = tm;
    }
  };
  u32x4 r0, r1;
#pragma unroll 1
  for (int n = 0;; ++n) {
    src += TSTEP;
    dst ^= TILEB;
    PIECE(0);
    pace();
    const float tm = *(const LAS float*)(lds + mt + n * 128);
    park_get(n, r0, r1);
    PIECE(1);
    rebase(tm);
    PIECE(2); PIECE(3); PIECE(4);
    WAIT_VM(5);
    if (n == TPW - 2) break;
    bf16x8 f[16];
#pragma unroll
    for (int c = 0; c < 8; ++c) f[c] = col_frag(lds, slot + vrd, c, 0);
    PIECE(5);
#pragma unroll
    for (int c = 0; c < 8; ++c) f[8 + c] = col_frag(lds, slot + vrd, c, BLKB);
    PIECE(6);
    pv4<0>(f, r0);
    PIECE(7);
    pv4<4>(f + 4, r0);
    PIECE(8);
    pv4<0>(f + 8, r1);
    PIECE(9);
    pv4<4>(f + 12, r1);
    PIECE(10); PIECE(11); PIECE(12); PIECE(13); PIECE(14); PIECE(15);
    slot ^= TILEB;
  }
  f32x16 ox, oy;
  {
    bf16x8 f[16];
#pragma unroll
    for (int c = 0; c < 8; ++c) { f[c] = col_frag(lds, slot + vrd, c, 0); f[8 + c] = col_frag(lds, slot + vrd, c, BLKB); }
    PIECE_N(5, WEND - 1); PIECE_N(6, WEND - 1); PIECE_N(7, WEND - 1);
    pv4<0>(f, r0); pv4<0>(f + 8, r1);
    PIECE_N(8, WEND - 1); PIECE_N(9, WEND - 1);
    pv4<4>(f + 4, r0); pv4<4>(f + 12, r1);
    PIECE_N(10, WEND - 1); PIECE_N(11, WEND - 1);
  }
#define QUARTER(p) do { const f32x16 a_ = acc_get16<32 * p>(), b_ = acc_get16<32 * p + 16>(); \
    if (wave == p) { ox = a_ * ow; oy = b_ * ow; } \
    else { LAS char* d_ = lds + slot + p * 4096 + lane * 16; \
           *(LAS u32x4*)d_ = pack8(a_, 0); *(LAS u32x4*)(d_ + 1024) = pack8(a_, 8); *(LAS u32x4*)(d_ + 2048) = pack8(b_, 0); *(LAS u32x4*)(d_ + 3072) = pack8(b_, 8); } } while (0)
  QUARTER(0);
  PIECE_N(12, WEND - 1); PIECE_N(13, WEND - 1);
  QUARTER(1);
  PIECE_N(14, WEND - 1); PIECE_N(15, WEND - 1);
  QUARTER(2);
  QUARTER(3);
#undef QUARTER
  WAIT_VM(8);
  BARRIER();
  const unsigned rel = slot - ring, vq = (vrd ^ ((wave & 1) << 7)) + (wave >> 1) * 256;
  float L = zl;
  u32x4 pl[3][2];
  {
    f32x2 ml[3]; u32x4 im[3][4]; bf16x8 f[8];
#pragma unroll
    for (int d = 0; d < 3; ++d) {
      const int w = (wave + 1 + d) & 3;
      ml[d] = *(const LAS f32x2*)(lds + ML_OFF + (w * 32 + r32) * 8);
#pragma unroll
      for (int i = 0; i < 4; ++i) im[d][i] = *(const LAS u32x4*)(lds + w * RINGB + rel + wave * 4096 + lane * 16 + i * 1024);
#pragma unroll
      for (int i = 0; i < 2; ++i) pl[d][i] = *(const LAS u32x4*)(lds + TREE_OFF + w * 2048 + i * 1024 + lane * 16);
    }
#pragma unroll
    for (int d = 0; d < 4; ++d) {
      const unsigned a = ((wave + d) & 3) * RINGB + (rel ^ TILEB) + vq;
      f[2 * d] = col_frag(lds, a, 0, 0); f[2 * d + 1] = col_frag(lds, a, 1, 0);
    }
#define FOLD(d) do { L += ml[d][1]; fma8(ox, 0, ml[d][0], im[d][0]); fma8(ox, 8, ml[d][0], im[d][1]); fma8(oy, 0, ml[d][0], im[d][2]); fma8(oy, 8, ml[d][0], im[d][3]); \
    asm volatile("" : "+v"(ox), "+v"(oy)); } while (0)
    asm volatile("" ::: "memory");
    FOLD(0); FOLD(1); FOLD(2);
#undef FOLD
    pv2(ox, oy, f[0], f[1], p0);
#pragma unroll
    for (int d = 0; d < 3; ++d) pv2(ox, oy, f[2 * d + 2], f[2 * d + 3], pl[d][0]);
  }
  WAIT_VM(0);
  BARRIER();
  {
    bf16x8 f[8];
#pragma unroll
    for (int d = 0; d < 4; ++d) {
      const unsigned a = ((wave + d) & 3) * RINGB + (rel ^ TILEB) + vq;
      f[2 * d] = col_frag(lds, a, 0, BLKB); f[2 * d + 1] = col_frag(lds, a, 1, BLKB);
    }
    pv2(ox, oy, f[0], f[1], p1);
#pragma unroll
    for (int d = 0; d < 3; ++d) pv2(ox, oy, f[2 * d + 2], f[2 * d + 3], pl[d][1]);
    asm volatile("s_nop 15" : "+v"(ox), "+v"(oy));
  }
  const float M = zm;
  float f[4];

  const __amdgpu_buffer_rsrc_t rs = __builtin_amdgcn_make_buffer_rsrc(ws, 0, (int)WS_END, 0x00020000);
  const int ml_off = (int)WS_PML + (grp * NSPL * 32 + r32) * 8;
  const int po_off = (int)WS_PO + grp * NSPL * PARTB + wave * 4096 + lane * 16;
  if (wave == 0 && hh == 0) __builtin_amdgcn_raw_buffer_store_b64(__builtin_bit_cast(u32x2, f32x2{M, L}), rs, ml_off + mem * 256, 0, 16);
  f32x4 x = {}, y = {};
#pragma unroll
  for (int v = 0; v < 4; ++v) {
    const f32x16& o = v < 2 ? ox : oy;
    const int b = 8 * (v & 1);
    if (v == mem) { x = f32x4{o[b], o[b + 1], o[b + 2], o[b + 3]}; y = f32x4{o[b + 4], o[b + 5], o[b + 6], o[b + 7]}; }
    else __builtin_amdgcn_raw_buffer_store_b128(pack8(o, b), rs, po_off + mem * PARTB + v * 1024, 0, 16);
  }
  WAIT_VM(0);
  BARRIER();
  if (wave == 0) {
#pragma unroll 1
    for (int i = 0; i < SPIN_MAX && !ready; ++i) {
      ctl_add_ret(cw, vz, 0u); WAIT_VM(0);
      ready = ctl_result() != w0;
      if (!ready) __builtin_amdgcn_s_sleep(16);
    }
    ctl_add_ret(cw, vz, 1u << mem);
#pragma unroll 1
    for (int i = 0; i < SPIN_MAX; ++i) {
      WAIT_VM(0);
      const unsigned w = ctl_result() | (1u << mem);
      if ((w & 15u) == 15u) break;
      ctl_add_ret(cw, vz, leader ? W_EPOCH : 0u);
    }
  }
  BARRIER();
  f32x2 oml[3]; u32x4 oa[3];
#pragma unroll
  for (int d = 0; d < 3; ++d) {
    const int sm = (mem + 1 + d) & 3;
    oml[d] = __builtin_bit_cast(f32x2, __builtin_amdgcn_raw_buffer_load_b64(rs, ml_off, sm * 256, 16));
    oa[d] = __builtin_amdgcn_raw_buffer_load_b128(rs, po_off, sm * PARTB + mem * 1024, 16);
  }
  const float Mf = fmaxf(fmaxf(M, oml[0][0]), fmaxf(oml[1][0], oml[2][0]));
  f[3] = __builtin_amdgcn_exp2f((M - Mf) * SCL2);
  L *= f[3];
#pragma unroll
  for (int d = 0; d < 3; ++d) { f[d] = __builtin_amdgcn_exp2f((oml[d][0] - Mf) * SCL2); L = fmaf(f[d], oml[d][1], L); }
  const float inv = 1.0f / L;
  x *= f[3] * inv; y *= f[3] * inv;
#pragma unroll
  for (int d = 0; d < 3; ++d) { x += (f[d] * inv) * unpack4(oa[d][0], oa[d][1]); y += (f[d] * inv) * unpack4(oa[d][2], oa[d][3]); }
  const auto rx = __builtin_amdgcn_permlane32_swap(pack2(x[0], x[1]), pack2(y[0], y[1]), false, false);
  const auto ry = __builtin_amdgcn_permlane32_swap(pack2(x[2], x[3]), pack2(y[2], y[3]), false, false);
  *(u32x4*)((char*)out + (size_t)(grp * 32 + r32) * ROWB + wave * 128 + mem * 32 + hh * 16) = u32x4{rx[0], ry[0], rx[1], ry[1]};
  asm volatile(".Lcode1:");
}

extern "C" void kernel_launch(void* const* d_in, const int* in_sizes, int n_in, void* d_out, int out_size, void* d_ws, size_t ws_size,
                              hipStream_t stream) {
  tree_attn<<<NGRP * NSPL, 256, 0, stream>>>((const char*)d_in[0], (const char*)d_in[1], (const char*)d_in[2], (const char*)d_in[3],
                                             (const char*)d_in[4], (const char*)d_in[5], (char*)d_ws, (unsigned short*)d_out);
}
```

```cpp
#define DBG_STAMPS
#include <hip/hip_runtime.h>
#include <hip/hip_bf16.h>

#define LAS __attribute__((address_space(3)))
typedef __bf16 bf16x8 __attribute__((ext_vector_type(8)));
typedef __bf16 bf16x4 __attribute__((ext_vector_type(4)));
typedef __bf16 bf16x2 __attribute__((ext_vector_type(2)));
typedef float f32x16 __attribute__((ext_vector_type(16)));
typedef float f32x4 __attribute__((ext_vector_type(4)));
typedef float f32x2 __attribute__((ext_vector_type(2)));
typedef unsigned u32x4 __attribute__((ext_vector_type(4)));
typedef unsigned u32x2 __attribute__((ext_vector_type(2)));

constexpr int NB = 8, HKV = 8, NT = 16, CTX = 8192, HD = 256;
constexpr int NGRP = NB * HKV;
constexpr int NSPL = 4;
constexpr int ROWB = HD * 2;
constexpr int BLKB = 16 * ROWB;
constexpr int TILEB = 2 * BLKB;
constexpr int RINGB = 2 * TILEB;
constexpr int TPW = CTX / 32 / 16;
constexpr int HALFB = CTX / 2 * ROWB, TSTEP = 16 * BLKB;
constexpr int TREE_OFF = 4 * RINGB;
constexpr int Q_OFF = TREE_OFF + TILEB;
constexpr int LDS_BYTES = Q_OFF + TILEB;
constexpr int MT_OFF = Q_OFF;
constexpr int ML_OFF = MT_OFF + 4 * TPW * 128;
constexpr int SH_OFF = ML_OFF + 1024;
static_assert(LDS_BYTES <= 160 * 1024 && SH_OFF + 16 <= LDS_BYTES && TPW == 16, "layout");
constexpr float SCL2 = 0.0625f * 1.4426950408889634f;
constexpr float MASKRAW = -1.0e9f * 16.0f;
constexpr float DEFER = 8.0f / SCL2;

constexpr int WIN = 5;
constexpr int WEND = 10;
constexpr int PACE = 217, PACE0 = 270;
constexpr int SLIP = 120, JUMP = 212;
constexpr int LV0 = -100, LV1 = -50, LV2 = 0, LV3 = 60;
constexpr int NEAR = 300;

constexpr unsigned W_EPOCH = 1u << 29, W_EPOCH_MASK = 0xE0000000u;
constexpr int SPIN_MAX = 1 << 16;
constexpr int PARTB = 32 * HD * 2;
constexpr size_t WS_CTL = 0, WS_T0 = 32768, WS_PML = 65536, WS_PO = WS_PML + (size_t)NGRP * NSPL * 32 * 8;
constexpr size_t WS_END = WS_PO + (size_t)NGRP * NSPL * PARTB;

template <int OFF, int CNT> __device__ __forceinline__ void dma1(const char* src, unsigned lds, unsigned v) {
  unsigned keep;
  __builtin_amdgcn_sched_barrier(0);
  asm volatile("s_mov_b32 %0, m0\n\ts_mov_b32 m0, %2\n\ts_waitcnt vmcnt(%5)\n\ts_nop 2\n\t"
               "global_load_lds_dwordx4 %3, %1 offset:%4 nt\n\ts_mov_b32 m0, %0"
               : "=&s"(keep) : "s"(src), "s"(lds), "v"(v), "i"(OFF), "i"(CNT) : "memory");
  __builtin_amdgcn_sched_barrier(0);
}
template <int OFF> __device__ __forceinline__ void dma1w(const char* src, unsigned lds, unsigned v, int lvl) {
  unsigned keep;
  __builtin_amdgcn_sched_barrier(0);
  asm volatile("s_mov_b32 %0, m0\n\ts_mov_b32 m0, %2\n\t"
               "s_cmp_gt_i32 %5, 3\n\ts_waitcnt vmcnt(%6+2)\n\ts_cbranch_scc1 .Lb_%=\n\t"
               "s_cmp_gt_i32 %5, 2\n\ts_waitcnt vmcnt(%6+1)\n\ts_cbranch_scc1 .Lb_%=\n\t"
               "s_cmp_gt_i32 %5, 1\n\ts_waitcnt vmcnt(%6)\n\ts_cbranch_scc1 .Lb_%=\n\t"
               "s_cmp_gt_i32 %5, 0\n\ts_nop 7\n\ts_cbranch_scc1 .Lb_%=\n\t"
               "s_waitcnt vmcnt(%6-1)\n\t.Lb_%=:\n\t"
               "global_load_lds_dwordx4 %3, %1 offset:%4 nt\n\ts_mov_b32 m0, %0"
               : "=&s"(keep) : "s"(src), "s"(lds), "v"(v), "i"(OFF), "s"(lvl), "i"(WIN - 1) : "memory", "scc");
  __builtin_amdgcn_sched_barrier(0);
}
__device__ __forceinline__ int level_of(int late) {
  int l;
  asm volatile("s_mov_b32 %0, 0\n\ts_cmp_gt_i32 %1, %2\n\ts_addc_u32 %0, %0, 0\n\ts_cmp_gt_i32 %1, %3\n\ts_addc_u32 %0, %0, 0\n\t"
               "s_cmp_gt_i32 %1, %4\n\ts_addc_u32 %0, %0, 0\n\ts_cmp_gt_i32 %1, %5\n\ts_addc_u32 %0, %0, 0"
               : "=&s"(l) : "s"(late), "i"(LV0), "i"(LV1), "i"(LV2), "i"(LV3) : "scc");
  return l;
}
__device__ __forceinline__ unsigned clock_ticks() {
  unsigned long long t;
  asm volatile("s_memrealtime %0\n\ts_waitcnt lgkmcnt(0)" : "=s"(t) :: "memory");
  return (unsigned)t;
}
#define WAIT_VM(n) asm volatile("s_waitcnt vmcnt(%0)" :: "i"(n) : "memory")
#define WAIT_LGKM0() asm volatile("s_waitcnt lgkmcnt(0)" ::: "memory")
#define BARRIER() do { WAIT_LGKM0(); __builtin_amdgcn_s_barrier(); asm volatile("" ::: "memory"); } while (0)

__device__ __forceinline__ void ctl_add_ret(unsigned* w, unsigned zero, unsigned inc) {
  unsigned long long ex;
  asm volatile("v_accvgpr_write_b32 a254, %3\n\ts_mov_b64 %0, exec\n\ts_mov_b64 exec, 1\n\ts_nop 2\n\t"
               "global_atomic_add a255, %2, a254, %1 sc0\n\ts_mov_b64 exec, %0\n\ts_nop 1"
               : "=&s"(ex) : "s"(w), "v"(zero), "v"(inc) : "memory", "a254", "a255");
}
__device__ __forceinline__ unsigned ctl_result() {
  unsigned t, r;
  asm volatile("v_accvgpr_read_b32 %0, a255\n\ts_nop 1\n\tv_readfirstlane_b32 %1, %0\n\ts_nop 4" : "=&v"(t), "=s"(r) :: "memory");
  return r;
}
__device__ __forceinline__ void ctl_and(unsigned* w, unsigned zero, unsigned m) {
  unsigned long long ex;
  asm volatile("s_mov_b64 %0, exec\n\ts_mov_b64 exec, 1\n\ts_nop 2\n\tglobal_atomic_and %2, %3, %1\n\ts_mov_b64 exec, %0\n\ts_nop 1"
               : "=&s"(ex) : "s"(w), "v"(zero), "v"(m) : "memory");
}
#define PARK_HEAD "s_getpc_b64 vcc\n\t.Lp0_%=:\n\ts_add_u32 vcc_lo, vcc_lo, %8\n\ts_addc_u32 vcc_hi, vcc_hi, 0\n\t" \
                  "s_add_u32 vcc_lo, vcc_lo, .Lp1_%=-.Lp0_%=\n\ts_addc_u32 vcc_hi, vcc_hi, 0\n\ts_setpc_b64 vcc\n\t.Lp1_%=:\n\t.set .Lpi_%=, 128\n\t.rept 15\n\t"
#define PARK_TAIL "s_branch .Lp2_%=\n\t.set .Lpi_%=, .Lpi_%=+8\n\t.endr\n\t.Lp2_%=:\n\ts_nop 1"
__device__ __forceinline__ void park_put(int j, const u32x4& p0, const u32x4& p1) {
  asm volatile(PARK_HEAD
               "v_accvgpr_write_b32 a[.Lpi_%=], %0\n\tv_accvgpr_write_b32 a[.Lpi_%=+1], %1\n\tv_accvgpr_write_b32 a[.Lpi_%=+2], %2\n\t"
               "v_accvgpr_write_b32 a[.Lpi_%=+3], %3\n\tv_accvgpr_write_b32 a[.Lpi_%=+4], %4\n\tv_accvgpr_write_b32 a[.Lpi_%=+5], %5\n\t"
               "v_accvgpr_write_b32 a[.Lpi_%=+6], %6\n\tv_accvgpr_write_b32 a[.Lpi_%=+7], %7\n\t" PARK_TAIL
               :: "v"(p0[0]), "v"(p0[1]), "v"(p0[2]), "v"(p0[3]), "v"(p1[0]), "v"(p1[1]), "v"(p1[2]), "v"(p1[3]), "s"(j * 68)
               : "memory", "vcc", "scc");
}
__device__ __forceinline__ void park_get(int j, u32x4& p0, u32x4& p1) {
  asm volatile(PARK_HEAD
               "v_accvgpr_read_b32 %0, a[.Lpi_%=]\n\tv_accvgpr_read_b32 %1, a[.Lpi_%=+1]\n\tv_accvgpr_read_b32 %2, a[.Lpi_%=+2]\n\t"
               "v_accvgpr_read_b32 %3, a[.Lpi_%=+3]\n\tv_accvgpr_read_b32 %4, a[.Lpi_%=+4]\n\tv_accvgpr_read_b32 %5, a[.Lpi_%=+5]\n\t"
               "v_accvgpr_read_b32 %6, a[.Lpi_%=+6]\n\tv_accvgpr_read_b32 %7, a[.Lpi_%=+7]\n\t" PARK_TAIL
               : "=v"(p0[0]), "=v"(p0[1]), "=v"(p0[2]), "=v"(p0[3]), "=v"(p1[0]), "=v"(p1[1]), "=v"(p1[2]), "=v"(p1[3]) : "s"(j * 68)
               : "memory", "vcc", "scc");
}
template <int B> __device__ __forceinline__ void acc_zero32() {
  asm volatile(".set .Lz_%=, %c0\n\t.rept 32\n\tv_accvgpr_write_b32 a[.Lz_%=], 0\n\t.set .Lz_%=, .Lz_%=+1\n\t.endr" :: "i"(B) : "memory", "a0", "a127", "a247");
}
__device__ __forceinline__ void acc_scale(float alpha) {
  float t0, t1, t2, t3;
  asm volatile("s_nop 15\n\t.set .Ls_%=, 0\n\t.rept 32\n\t"
               "v_accvgpr_read_b32 %0, a[.Ls_%=]\n\tv_accvgpr_read_b32 %1, a[.Ls_%=+1]\n\tv_accvgpr_read_b32 %2, a[.Ls_%=+2]\n\tv_accvgpr_read_b32 %3, a[.Ls_%=+3]\n\t"
               "v_mul_f32 %0, %0, %4\n\tv_mul_f32 %1, %1, %4\n\tv_mul_f32 %2, %2, %4\n\tv_mul_f32 %3, %3, %4\n\t"
               "v_accvgpr_write_b32 a[.Ls_%=], %0\n\tv_accvgpr_write_b32 a[.Ls_%=+1], %1\n\tv_accvgpr_write_b32 a[.Ls_%=+2], %2\n\tv_accvgpr_write_b32 a[.Ls_%=+3], %3\n\t"
               ".set .Ls_%=, .Ls_%=+4\n\t.endr\n\ts_nop 1"
               : "=&v"(t0), "=&v"(t1), "=&v"(t2), "=&v"(t3) : "v"(alpha) : "memory");
}
#define M_(a, b) "v_mfma_f32_32x32x16_bf16 %0, %" #a ", %" #b ", %0\n\t"
__device__ __forceinline__ void qk_first(f32x16& s, const bf16x8* f, const bf16x8* q) {
  asm volatile("s_nop 1\n\tv_mfma_f32_32x32x16_bf16 %0, %1, %5, 0\n\t" M_(2, 6) M_(3, 7) M_(4, 8)
               : "=&v"(s) : "v"(f[0]), "v"(f[1]), "v"(f[2]), "v"(f[3]), "v"(q[0]), "v"(q[1]), "v"(q[2]), "v"(q[3]));
}
__device__ __forceinline__ void qk_next(f32x16& s, const bf16x8* f, const bf16x8* q) {
  asm volatile("s_nop 1\n\t" M_(1, 5) M_(2, 6) M_(3, 7) M_(4, 8)
               : "+v"(s) : "v"(f[0]), "v"(f[1]), "v"(f[2]), "v"(f[3]), "v"(q[0]), "v"(q[1]), "v"(q[2]), "v"(q[3]));
}
#undef M_
__device__ __forceinline__ void qk_done(f32x16& s) { asm volatile("s_nop 15" : "+v"(s)); }
template <int C> __device__ __forceinline__ void pv4(const bf16x8* f, const u32x4& p) {
  asm volatile("s_nop 1\n\t"
               "v_mfma_f32_32x32x16_bf16 a[%c5:%c5+15], %0, %4, a[%c5:%c5+15]\n\t"
               "v_mfma_f32_32x32x16_bf16 a[%c5+16:%c5+31], %1, %4, a[%c5+16:%c5+31]\n\t"
               "v_mfma_f32_32x32x16_bf16 a[%c5+32:%c5+47], %2, %4, a[%c5+32:%c5+47]\n\t"
               "v_mfma_f32_32x32x16_bf16 a[%c5+48:%c5+63], %3, %4, a[%c5+48:%c5+63]"
               :: "v"(f[0]), "v"(f[1]), "v"(f[2]), "v"(f[3]), "v"(p), "i"(16 * C) : "memory");
}
__device__ __forceinline__ void pv2(f32x16& x, f32x16& y, const bf16x8& v0, const bf16x8& v1, const u32x4& p) {
  asm volatile("s_nop 1\n\tv_mfma_f32_32x32x16_bf16 %0, %2, %4, %0\n\tv_mfma_f32_32x32x16_bf16 %1, %3, %4, %1"
               : "+v"(x), "+v"(y) : "v"(v0), "v"(v1), "v"(p));
}
template <int B> __device__ __forceinline__ f32x16 acc_get16() {
  float r[16];
  asm volatile("v_accvgpr_read_b32 %0, a[%c16]\n\tv_accvgpr_read_b32 %1, a[%c16+1]\n\tv_accvgpr_read_b32 %2, a[%c16+2]\n\tv_accvgpr_read_b32 %3, a[%c16+3]\n\t"
               "v_accvgpr_read_b32 %4, a[%c16+4]\n\tv_accvgpr_read_b32 %5, a[%c16+5]\n\tv_accvgpr_read_b32 %6, a[%c16+6]\n\tv_accvgpr_read_b32 %7, a[%c16+7]\n\t"
               "v_accvgpr_read_b32 %8, a[%c16+8]\n\tv_accvgpr_read_b32 %9, a[%c16+9]\n\tv_accvgpr_read_b32 %10, a[%c16+10]\n\tv_accvgpr_read_b32 %11, a[%c16+11]\n\t"
               "v_accvgpr_read_b32 %12, a[%c16+12]\n\tv_accvgpr_read_b32 %13, a[%c16+13]\n\tv_accvgpr_read_b32 %14, a[%c16+14]\n\tv_accvgpr_read_b32 %15, a[%c16+15]"
               : "=v"(r[0]), "=v"(r[1]), "=v"(r[2]), "=v"(r[3]), "=v"(r[4]), "=v"(r[5]), "=v"(r[6]), "=v"(r[7]), "=v"(r[8]), "=v"(r[9]),
                 "=v"(r[10]), "=v"(r[11]), "=v"(r[12]), "=v"(r[13]), "=v"(r[14]), "=v"(r[15]) : "i"(B) : "memory");
  f32x16 x;
#pragma unroll
  for (int i = 0; i < 16; ++i) x[i] = r[i];
  return x;
}

__device__ __forceinline__ float xhalf_max(float x) {
  auto r = __builtin_amdgcn_permlane32_swap(__float_as_uint(x), __float_as_uint(x), false, false);
  return fmaxf(__uint_as_float(r[0]), __uint_as_float(r[1]));
}
__device__ __forceinline__ float xhalf_sum(float x) {
  auto r = __builtin_amdgcn_permlane32_swap(__float_as_uint(x), __float_as_uint(x), false, false);
  return __uint_as_float(r[0]) + __uint_as_float(r[1]);
}
__device__ __forceinline__ unsigned pack2(float a, float b) {
  return __builtin_bit_cast(unsigned, __builtin_convertvector((f32x2){a, b}, bf16x2));
}
__device__ __forceinline__ f32x4 unpack4(unsigned lo, unsigned hi) {
  return f32x4{__uint_as_float(lo << 16), __uint_as_float(lo & 0xffff0000u), __uint_as_float(hi << 16), __uint_as_float(hi & 0xffff0000u)};
}
__device__ __forceinline__ u32x4 pack8(const f32x16& x, int b) {
  return u32x4{pack2(x[b], x[b + 1]), pack2(x[b + 2], x[b + 3]), pack2(x[b + 4], x[b + 5]), pack2(x[b + 6], x[b + 7])};
}
__device__ __forceinline__ void fma8(f32x16& x, int b, float f, const u32x4& d) {
#pragma unroll
  for (int i = 0; i < 4; ++i) {
    x[b + 2 * i] = fmaf(f, __uint_as_float(d[i] << 16), x[b + 2 * i]);
    x[b + 2 * i + 1] = fmaf(f, __uint_as_float(d[i] & 0xffff0000u), x[b + 2 * i + 1]);
  }
}
__device__ __forceinline__ unsigned swz(unsigned row) { return ((row & 3) << 2) | ((row >> 2) & 3); }
__device__ __forceinline__ bf16x8 row_frag(LAS char* lds, unsigned rd, int st) {
  return *(const LAS bf16x8*)(lds + ((rd ^ ((st & 7) << 5)) + (st >> 3) * 256));
}
__device__ __forceinline__ bf16x8 col_frag(LAS char* lds, unsigned vrd, int c, int koff) {
  const int o = (c >> 2) * 256 + koff;
  const bf16x4 lo = __builtin_amdgcn_ds_read_tr16_b64_v4bf16((LAS bf16x4*)(lds + ((vrd ^ ((c & 3) << 6)) + o)));
  const bf16x4 hi = __builtin_amdgcn_ds_read_tr16_b64_v4bf16((LAS bf16x4*)(lds + ((vrd ^ ((c & 3) << 6) ^ 32) + o + 8 * ROWB)));
  return __builtin_shufflevector(lo, hi, 0, 1, 2, 3, 4, 5, 6, 7);
}
template <int NR> __device__ __forceinline__ void sm_ref(const f32x16& s, float& m, float& l) {
  float mx = s[0];
#pragma unroll
  for (int r = 1; r < NR; ++r) mx = fmaxf(mx, s[r]);
  mx = xhalf_max(mx);
  if (!__all(mx <= m + DEFER)) {
    const float mn = fmaxf(m, mx);
    l *= __builtin_amdgcn_exp2f((m - mn) * SCL2);
    m = mn;
  }
  asm volatile("" : "+v"(m), "+v"(l));
}
template <int B> __device__ __forceinline__ u32x4 sm_exp(const f32x16& s, float m, float& l) {
  float e[8];
#pragma unroll
  for (int r = 0; r < 8; ++r) e[r] = __builtin_amdgcn_exp2f((s[B + r] - m) * SCL2);
  l += ((e[0] + e[1]) + (e[2] + e[3])) + ((e[4] + e[5]) + (e[6] + e[7]));
  u32x4 p = {pack2(e[0], e[1]), pack2(e[2], e[3]), pack2(e[4], e[5]), pack2(e[6], e[7])};
  asm volatile("" : "+v"(p), "+v"(l));
  return p;
}

__global__ void __launch_bounds__(256, 1)
tree_attn(const char* __restrict__ Q, const char* __restrict__ K, const char* __restrict__ V, const char* __restrict__ KT,
          const char* __restrict__ VT, const char* __restrict__ MK, char* __restrict__ ws, unsigned short* __restrict__ out) {
  __shared__ __attribute__((aligned(1024))) char smem[LDS_BYTES];
  LAS char* lds = (LAS char*)smem;
  const unsigned lbase = (unsigned)(size_t)lds;
  const int tid = threadIdx.x, lane = tid & 63, wave = __builtin_amdgcn_readfirstlane(tid >> 6);
  const int wg = blockIdx.x;
  const int grp = (wg >> 5) * 8 + (wg & 7), mem = (wg >> 3) & 3;
  const int r32 = lane & 31, hh = lane >> 5;
  const bool leader = (mem == 1) && (wave == 0);
  unsigned long long ts0 = __builtin_amdgcn_s_memrealtime(), ts1 = 0, ts2 = 0; int ntl = 0, nslow = 0, npoll = 0;
  {
    unsigned long long pc; unsigned len;
    asm volatile("s_getpc_b64 %0\n.Lcode0:\n\ts_mov_b32 %1, .Lcode1-.Lcode0" : "=s"(pc), "=s"(len));
    const unsigned o = min((unsigned)(((wg >> 3) * 4 + wave) & 31) * 1024u, (len & ~15u) - 1024u) + lane * 16;
    asm volatile("global_load_dwordx4 a[240:243], %0, %1" :: "v"(o), "s"(pc & ~15ull) : "memory", "a240", "a241", "a242", "a243");
  }
  unsigned ko[8], qo[4];
#pragma unroll
  for (int i = 0; i < 8; ++i) ko[i] = hh * ROWB + ((r32 ^ swz(2 * i + hh)) << 4);
#pragma unroll
  for (int i = 0; i < 4; ++i) qo[i] = ko[i] ^ ((wave & 1) << 5);
  asm volatile("" : "+v"(ko[0]), "+v"(ko[1]), "+v"(ko[2]), "+v"(ko[3]), "+v"(ko[4]), "+v"(ko[5]), "+v"(ko[6]), "+v"(ko[7]),
                    "+v"(qo[0]), "+v"(qo[1]), "+v"(qo[2]), "+v"(qo[3]));
  const unsigned ring = wave * RINGB;
  const size_t wof = (size_t)grp * (CTX * ROWB) + (mem * 4 + wave) * BLKB;
  const char* src = K + wof;
  unsigned dst = lbase + ring;
#define PIECE_N(k, cnt) dma1<((k) & 3) * 1024, (cnt)>(src + ((k) >> 3) * HALFB + (((k) >> 2) & 1) * 4096, dst + ((k) >> 2) * 4096, ko[(k) & 7])
#define PIECE(k) dma1w<((k) & 3) * 1024>(src + ((k) >> 3) * HALFB + (((k) >> 2) & 1) * 4096, dst + ((k) >> 2) * 4096, ko[(k) & 7], lvl)
  int late = 0, lvl = 2;
  auto quarter = [&](const char* s, unsigned d) {
    dma1<0, 63>(s, d, qo[0]); dma1<1024, 63>(s, d, qo[1]); dma1<2048, 63>(s, d, qo[2]); dma1<3072, 63>(s, d, qo[3]);
  };
  asm volatile("" :: "s"(Q), "s"(K), "s"(ws));
  ts1 = __builtin_amdgcn_s_memrealtime(); __builtin_amdgcn_s_waitcnt(0xC07F);
  quarter(Q + (size_t)grp * TILEB + wave * 4096, lbase + Q_OFF + wave * 4096);
  if (mem == 0) {
    quarter((wave & 2 ? VT : KT) + (size_t)grp * BLKB + (wave & 1) * 4096, lbase + TREE_OFF + wave * 4096);
    const unsigned mo = (r32 & 15) * (NT * 2) + 8 * hh;
    asm volatile("s_nop 4\n\tglobal_load_dwordx2 a[248:249], %0, %1\n\tglobal_load_dwordx2 a[250:251], %0, %1 offset:16"
                 :: "v"(mo), "s"(MK + (grp >> 3) * (NT * NT * 2)) : "memory", "a248", "a249", "a250", "a251");
  }
  unsigned* cw = (unsigned*)(ws + WS_CTL + grp * 256);
  const unsigned vz = 0;
  if (wave == 0) ctl_add_ret(cw, vz, 0u);
  PIECE_N(0, 63); PIECE_N(1, 63); PIECE_N(2, 63); PIECE_N(3, 63); PIECE_N(4, 63);
  unsigned due = clock_ticks() + PACE0;
  const char* t0p = ws + WS_T0 + (wg >> 3) * 64;
  const unsigned due0 = due;
  if (wave == 3) {
    unsigned long long ex;
    asm volatile("s_mov_b64 %0, exec\n\ts_mov_b64 exec, 1\n\ts_nop 2\n\tglobal_store_dword %2, %3, %1 sc1\n\ts_mov_b64 exec, %0\n\ts_nop 1"
                 : "=&s"(ex) : "s"(t0p), "v"((wg & 7) * 4), "v"(due0) : "memory");
  }
  const unsigned krd = r32 * ROWB + ((hh ^ swz(r32)) << 4);
  const int q4 = (lane & 15) >> 2;
  const unsigned vrd = (4 * hh + q4) * ROWB + (q4 << 6) + ((((lane >> 4) & 1) * 32 + (lane & 3) * 8) ^ (hh << 4));
  acc_zero32<0>(); acc_zero32<32>(); acc_zero32<64>(); acc_zero32<96>();
  WAIT_VM(5);
  const unsigned w0 = ctl_result();
  if (leader) ctl_and(cw, vz, W_EPOCH_MASK);
  BARRIER();
  PIECE(5);
  bf16x8 q[16];
#pragma unroll
  for (int st = 0; st < 16; ++st) q[st] = row_frag(lds, Q_OFF + krd, st);
  PIECE(6);
  BARRIER();
  if (wave == 3) *(LAS int*)(lds + SH_OFF) = 0;
  float zm = -__builtin_inff(), zl = 0.f, om = -__builtin_inff();
  if (mem == 0 && wave == 1) {
    unsigned mk[4];
    asm volatile("v_accvgpr_read_b32 %0, a248\n\tv_accvgpr_read_b32 %1, a249\n\tv_accvgpr_read_b32 %2, a250\n\tv_accvgpr_read_b32 %3, a251"
                 : "=v"(mk[0]), "=v"(mk[1]), "=v"(mk[2]), "=v"(mk[3]) :: "memory");
    bf16x8 f[16]; f32x16 s;
#pragma unroll
    for (int st = 0; st < 16; ++st) f[st] = row_frag(lds, TREE_OFF + (krd & (BLKB - 1)), st);
    PIECE(7);
    qk_first(s, f, q); qk_next(s, f + 4, q + 4);
    PIECE(8);
    qk_next(s, f + 8, q + 8); qk_next(s, f + 12, q + 12);
    PIECE(9);
    qk_done(s);
#pragma unroll
    for (int c = 0; c < 8; ++c) f[c] = col_frag(lds, TREE_OFF + vrd, c, BLKB);
#pragma unroll
    for (int r = 0; r < 8; ++r) {
      const float mv = __uint_as_float((r & 1) ? (mk[r >> 1] & 0xffff0000u) : (mk[r >> 1] << 16));
      s[r] = fmaf(1.0f - mv, MASKRAW, s[r]);
    }
    sm_ref<8>(s, zm, zl);
    PIECE(10);
    const u32x4 p = sm_exp<0>(s, zm, zl);
    om = zm;
    PIECE(11);
    pv4<0>(f, p); pv4<4>(f + 4, p);
  } else {
    PIECE(7); PIECE(8); PIECE(9); PIECE(10); PIECE(11);
  }
  PIECE(12); PIECE(13); PIECE(14); PIECE(15);

  auto pace = [&]() {
    const int now = (int)(clock_ticks() - due), step = now - late;
    const int drop = step > JUMP ? step : 0;
    late = min(now - drop, SLIP);
    lvl = level_of(late);
    due += PACE + now - late;
  };
  bool ready = leader;
  const unsigned mt = MT_OFF + wave * (TPW * 128) + r32 * 4;
  unsigned slot = ring;
  u32x4 p0, p1;
  float ow = 0.f;
#pragma unroll 1
  for (int n = 0; n < TPW; ++n) {
    src = n + 1 < TPW ? src + TSTEP : V + wof;
    dst ^= TILEB;
    const bool look = wave == 0 && (leader ? n < 4 : !ready);
    if (look) ctl_add_ret(cw, vz, leader ? W_EPOCH : 0u);
    if (n == 0 && wave == 3)
      asm volatile("s_nop 4\n\tglobal_load_dword a252, %0, %1 sc1" :: "v"((lane & 7) * 4), "s"(t0p) : "memory", "a252");
    if (n == 2) {
      const int sh = __builtin_amdgcn_readfirstlane(*(volatile LAS int*)(lds + SH_OFF));
      if (sh > -NEAR && sh < NEAR) { due += sh; late -= sh; }
    }
    PIECE(0);
    pace();
    PIECE(1); PIECE(2); PIECE(3); PIECE(4);
    WAIT_VM(5);
    bf16x8 f[16]; f32x16 s;
#pragma unroll
    for (int st = 0; st < 16; ++st) f[st] = row_frag(lds, slot + krd, st);
    if (look) ready = ready || ctl_result() != w0;
    if (n == 0 && wave == 3) {
      int t;
      asm volatile("v_accvgpr_read_b32 %0, a252\n\ts_nop 1" : "=v"(t) :: "memory");
      const int d = t - (int)due0;
      const bool near = d > -NEAR && d < NEAR;
      float sum = near ? (float)d : 0.f, cnt = near ? 1.f : 0.f;
#pragma unroll
      for (int m = 1; m < 8; m <<= 1) { sum += __shfl_xor(sum, m); cnt += __shfl_xor(cnt, m); }
      *(volatile LAS int*)(lds + SH_OFF) = (int)(sum * __builtin_amdgcn_rcpf(fmaxf(cnt, 1.f)));
    }
    PIECE(5);
    qk_first(s, f, q);
    PIECE(6);
    qk_next(s, f + 4, q + 4);
    PIECE(7);
    qk_next(s, f + 8, q + 8);
    PIECE(8);
    qk_next(s, f + 12, q + 12);
    PIECE(9);
    qk_done(s);
    const float zprev = zm;
    sm_ref<16>(s, zm, zl);
    *(LAS float*)(lds + mt + n * 128) = zm;
    PIECE(10);
    if (n == TPW - 1) {
      BARRIER();
      float M = zm;
#pragma unroll
      for (int d = 1; d < 4; ++d) M = fmaxf(M, *(const LAS float*)(lds + MT_OFF + (((wave + d) & 3) * TPW + n) * 128 + r32 * 4));
      zl *= __builtin_amdgcn_exp2f((zm - M) * SCL2);
      ow = __builtin_amdgcn_exp2f((zprev - M) * SCL2);
      zm = M;
    }
    p0 = sm_exp<0>(s, zm, zl);
    PIECE(11);
    p1 = sm_exp<8>(s, zm, zl);
    PIECE(12);
    if (n < TPW - 1) park_put(n, p0, p1);
    PIECE(13); PIECE(14); PIECE(15);
    slot ^= TILEB;
  }
  zl = xhalf_sum(zl);
  *(LAS f32x2*)(lds + ML_OFF + (wave * 32 + r32) * 8) = f32x2{ow, zl};
  *(LAS u32x4*)(lds + TREE_OFF + wave * 2048 + lane * 16) = p0;
  *(LAS u32x4*)(lds + TREE_OFF + wave * 2048 + 1024 + lane * 16) = p1;
  auto rebase = [&](float tm) {
    if (!__all(tm == om)) {
      if (!__all(om == -__builtin_inff())) acc_scale(__builtin_amdgcn_exp2f((om - tm) * SCL2));
      om = tm;
    }
  };
  u32x4 r0, r1;
#pragma unroll 1
  for (int n = 0;; ++n) {
    src += TSTEP;
    dst ^= TILEB;
    PIECE(0);
    pace();
    const float tm = *(const LAS float*)(lds + mt + n * 128);
    park_get(n, r0, r1);
    PIECE(1);
    rebase(tm);
    PIECE(2); PIECE(3); PIECE(4);
    WAIT_VM(5);
    if (n == TPW - 2) break;
    bf16x8 f[16];
#pragma unroll
    for (int c = 0; c < 8; ++c) f[c] = col_frag(lds, slot + vrd, c, 0);
    PIECE(5);
#pragma unroll
    for (int c = 0; c < 8; ++c) f[8 + c] = col_frag(lds, slot + vrd, c, BLKB);
    PIECE(6);
    pv4<0>(f, r0);
    PIECE(7);
    pv4<4>(f + 4, r0);
    PIECE(8);
    pv4<0>(f + 8, r1);
    PIECE(9);
    pv4<4>(f + 12, r1);
    PIECE(10); PIECE(11); PIECE(12); PIECE(13); PIECE(14); PIECE(15);
    slot ^= TILEB;
  }
  f32x16 ox, oy;
  {
    bf16x8 f[16];
#pragma unroll
    for (int c = 0; c < 8; ++c) { f[c] = col_frag(lds, slot + vrd, c, 0); f[8 + c] = col_frag(lds, slot + vrd, c, BLKB); }
    PIECE_N(5, WEND - 1); PIECE_N(6, WEND - 1); PIECE_N(7, WEND - 1);
    pv4<0>(f, r0); pv4<0>(f + 8, r1);
    PIECE_N(8, WEND - 1); PIECE_N(9, WEND - 1);
    pv4<4>(f + 4, r0); pv4<4>(f + 12, r1);
    PIECE_N(10, WEND - 1); PIECE_N(11, WEND - 1);
  }
#define QUARTER(p) do { const f32x16 a_ = acc_get16<32 * p>(), b_ = acc_get16<32 * p + 16>(); \
    if (wave == p) { ox = a_ * ow; oy = b_ * ow; } \
    else { LAS char* d_ = lds + slot + p * 4096 + lane * 16; \
           *(LAS u32x4*)d_ = pack8(a_, 0); *(LAS u32x4*)(d_ + 1024) = pack8(a_, 8); *(LAS u32x4*)(d_ + 2048) = pack8(b_, 0); *(LAS u32x4*)(d_ + 3072) = pack8(b_, 8); } } while (0)
  QUARTER(0);
  PIECE_N(12, WEND - 1); PIECE_N(13, WEND - 1);
  QUARTER(1);
  PIECE_N(14, WEND - 1); PIECE_N(15, WEND - 1);
  QUARTER(2);
  QUARTER(3);
#undef QUARTER
  WAIT_VM(8);
  BARRIER();
  const unsigned rel = slot - ring, vq = (vrd ^ ((wave & 1) << 7)) + (wave >> 1) * 256;
  float L = zl;
  u32x4 pl[3][2];
  {
    f32x2 ml[3]; u32x4 im[3][4]; bf16x8 f[8];
#pragma unroll
    for (int d = 0; d < 3; ++d) {
      const int w = (wave + 1 + d) & 3;
      ml[d] = *(const LAS f32x2*)(lds + ML_OFF + (w * 32 + r32) * 8);
#pragma unroll
      for (int i = 0; i < 4; ++i) im[d][i] = *(const LAS u32x4*)(lds + w * RINGB + rel + wave * 4096 + lane * 16 + i * 1024);
#pragma unroll
      for (int i = 0; i < 2; ++i) pl[d][i] = *(const LAS u32x4*)(lds + TREE_OFF + w * 2048 + i * 1024 + lane * 16);
    }
#pragma unroll
    for (int d = 0; d < 4; ++d) {
      const unsigned a = ((wave + d) & 3) * RINGB + (rel ^ TILEB) + vq;
      f[2 * d] = col_frag(lds, a, 0, 0); f[2 * d + 1] = col_frag(lds, a, 1, 0);
    }
#define FOLD(d) do { L += ml[d][1]; fma8(ox, 0, ml[d][0], im[d][0]); fma8(ox, 8, ml[d][0], im[d][1]); fma8(oy, 0, ml[d][0], im[d][2]); fma8(oy, 8, ml[d][0], im[d][3]); \
    asm volatile("" : "+v"(ox), "+v"(oy)); } while (0)
    asm volatile("" ::: "memory");
    FOLD(0); FOLD(1); FOLD(2);
#undef FOLD
    pv2(ox, oy, f[0], f[1], p0);
#pragma unroll
    for (int d = 0; d < 3; ++d) pv2(ox, oy, f[2 * d + 2], f[2 * d + 3], pl[d][0]);
  }
  WAIT_VM(0);
  BARRIER();
  {
    bf16x8 f[8];
#pragma unroll
    for (int d = 0; d < 4; ++d) {
      const unsigned a = ((wave + d) & 3) * RINGB + (rel ^ TILEB) + vq;
      f[2 * d] = col_frag(lds, a, 0, BLKB); f[2 * d + 1] = col_frag(lds, a, 1, BLKB);
    }
    pv2(ox, oy, f[0], f[1], p1);
#pragma unroll
    for (int d = 0; d < 3; ++d) pv2(ox, oy, f[2 * d + 2], f[2 * d + 3], pl[d][1]);
    asm volatile("s_nop 15" : "+v"(ox), "+v"(oy));
  }
  ts2 = __builtin_amdgcn_s_memrealtime(); __builtin_amdgcn_s_waitcnt(0xC07F);
  const float M = zm;
  float f[4];

  const __amdgpu_buffer_rsrc_t rs = __builtin_amdgcn_make_buffer_rsrc(ws, 0, (int)WS_END, 0x00020000);
  const int ml_off = (int)WS_PML + (grp * NSPL * 32 + r32) * 8;
  const int po_off = (int)WS_PO + grp * NSPL * PARTB + wave * 4096 + lane * 16;
  if (wave == 0 && hh == 0) __builtin_amdgcn_raw_buffer_store_b64(__builtin_bit_cast(u32x2, f32x2{M, L}), rs, ml_off + mem * 256, 0, 16);
  f32x4 x = {}, y = {};
#pragma unroll
  for (int v = 0; v < 4; ++v) {
    const f32x16& o = v < 2 ? ox : oy;
    const int b = 8 * (v & 1);
    if (v == mem) { x = f32x4{o[b], o[b + 1], o[b + 2], o[b + 3]}; y = f32x4{o[b + 4], o[b + 5], o[b + 6], o[b + 7]}; }
    else __builtin_amdgcn_raw_buffer_store_b128(pack8(o, b), rs, po_off + mem * PARTB + v * 1024, 0, 16);
  }
  WAIT_VM(0);
  BARRIER();
  if (wave == 0) {
#pragma unroll 1
    for (int i = 0; i < SPIN_MAX && !ready; ++i) {
      ctl_add_ret(cw, vz, 0u); WAIT_VM(0);
      ready = ctl_result() != w0;
      ++nslow;
      if (!ready) __builtin_amdgcn_s_sleep(16);
    }
    ctl_add_ret(cw, vz, 1u << mem);
#pragma unroll 1
    for (int i = 0; i < SPIN_MAX; ++i) {
      WAIT_VM(0);
      const unsigned w = ctl_result() | (1u << mem);
      ++npoll;
      if ((w & 15u) == 15u) break;
      ctl_add_ret(cw, vz, leader ? W_EPOCH : 0u);
    }
  }
  BARRIER();
  f32x2 oml[3]; u32x4 oa[3];
#pragma unroll
  for (int d = 0; d < 3; ++d) {
    const int sm = (mem + 1 + d) & 3;
    oml[d] = __builtin_bit_cast(f32x2, __builtin_amdgcn_raw_buffer_load_b64(rs, ml_off, sm * 256, 16));
    oa[d] = __builtin_amdgcn_raw_buffer_load_b128(rs, po_off, sm * PARTB + mem * 1024, 16);
  }
  const float Mf = fmaxf(fmaxf(M, oml[0][0]), fmaxf(oml[1][0], oml[2][0]));
  f[3] = __builtin_amdgcn_exp2f((M - Mf) * SCL2);
  L *= f[3];
#pragma unroll
  for (int d = 0; d < 3; ++d) { f[d] = __builtin_amdgcn_exp2f((oml[d][0] - Mf) * SCL2); L = fmaf(f[d], oml[d][1], L); }
  const float inv = 1.0f / L;
  x *= f[3] * inv; y *= f[3] * inv;
#pragma unroll
  for (int d = 0; d < 3; ++d) { x += (f[d] * inv) * unpack4(oa[d][0], oa[d][1]); y += (f[d] * inv) * unpack4(oa[d][2], oa[d][3]); }
  const auto rx = __builtin_amdgcn_permlane32_swap(pack2(x[0], x[1]), pack2(y[0], y[1]), false, false);
  const auto ry = __builtin_amdgcn_permlane32_swap(pack2(x[2], x[3]), pack2(y[2], y[3]), false, false);
  *(u32x4*)((char*)out + (size_t)(grp * 32 + r32) * ROWB + wave * 128 + mem * 32 + hh * 16) = u32x4{rx[0], ry[0], rx[1], ry[1]};
  asm volatile(".Lcode1:");
  WAIT_VM(0);
  if (lane == 0) { unsigned long long* sp = (unsigned long long*)(ws + (512ull << 20)) + (wg * 4 + wave) * 8;
    sp[0] = ts0; sp[1] = ts1; sp[2] = ts2; sp[3] = __builtin_amdgcn_s_memrealtime(); sp[4] = __builtin_amdgcn_s_getreg((31 << 11) | 20) & 7; sp[5] = ntl; sp[6] = nslow; sp[7] = npoll; }
}

__global__ void enc_k(const char* ws) {
  const unsigned long long t0 = __builtin_amdgcn_s_memrealtime();
  const unsigned long long* h = (const unsigned long long*)(ws + (512ull << 20));
  unsigned long long mn = ~0ull, mx3 = 0, mx2 = 0, s1 = 0, s2 = 0, mx0 = 0, sl = 0;
  for (int i = 0; i < 1024; ++i) { const unsigned long long* p = h + i * 8; mn = p[0] < mn ? p[0] : mn; mx0 = p[0] > mx0 ? p[0] : mx0; mx3 = p[3] > mx3 ? p[3] : mx3; mx2 = p[2] > mx2 ? p[2] : mx2; s1 += p[1]; s2 += p[2]; sl += p[6]; }
  unsigned long long m = 0;
  if (1 == 0) m = mx3 - mn;
  if (1 == 1) m = s1 / 1024 - mn;
  if (1 == 2) m = s2 / 1024 - mn;
  if (1 == 3) m = mx2 - mn;
  if (1 == 4) m = t0 - mx3;
  if (1 == 5) m = sl;
  if (1 == 6) m = 0;
  if (1 == 7) { m = mn - h[1024 * 8]; }
  if (1 == 8) { mn = h[1024 * 8]; m = 0; }
  if (1 == 9) m = mx0 - mn;
  if (m > 20000) m = 20000;
  while (__builtin_amdgcn_s_memrealtime() - mn < 100000ull + 20ull * m) __builtin_amdgcn_s_sleep(2);
}
extern "C" void kernel_launch(void* const* d_in, const int* in_sizes, int n_in, void* d_out, int out_size, void* d_ws, size_t ws_size,
                              hipStream_t stream) {
  tree_attn<<<NGRP * NSPL, 256, 0, stream>>>((const char*)d_in[0], (const char*)d_in[1], (const char*)d_in[2], (const char*)d_in[3],
                                             (const char*)d_in[4], (const char*)d_in[5], (char*)d_ws, (unsigned short*)d_out);
  enc_k<<<1, 1, 0, stream>>>((const char*)d_ws);
}
```

```cpp
#define DBG_STAMPS
#include <hip/hip_runtime.h>
#include <hip/hip_bf16.h>

#define LAS __attribute__((address_space(3)))
typedef __bf16 bf16x8 __attribute__((ext_vector_type(8)));
typedef __bf16 bf16x4 __attribute__((ext_vector_type(4)));
typedef __bf16 bf16x2 __attribute__((ext_vector_type(2)));
typedef float f32x16 __attribute__((ext_vector_type(16)));
typedef float f32x4 __attribute__((ext_vector_type(4)));
typedef float f32x2 __attribute__((ext_vector_type(2)));
typedef unsigned u32x4 __attribute__((ext_vector_type(4)));
typedef unsigned u32x2 __attribute__((ext_vector_type(2)));

constexpr int NB = 8, HKV = 8, NT = 16, CTX = 8192, HD = 256;
constexpr int NGRP = NB * HKV;
constexpr int NSPL = 4;
constexpr int ROWB = HD * 2;
constexpr int BLKB = 16 * ROWB;
constexpr int TILEB = 2 * BLKB;
constexpr int RINGB = 2 * TILEB;
constexpr int TPW = CTX / 32 / 16;
constexpr int HALFB = CTX / 2 * ROWB, TSTEP = 16 * BLKB;
constexpr int TREE_OFF = 4 * RINGB;
constexpr int Q_OFF = TREE_OFF + TILEB;
constexpr int LDS_BYTES = Q_OFF + TILEB;
constexpr int MT_OFF = Q_OFF;
constexpr int ML_OFF = MT_OFF + 4 * TPW * 128;
constexpr int SH_OFF = ML_OFF + 1024;
static_assert(LDS_BYTES <= 160 * 1024 && SH_OFF + 16 <= LDS_BYTES && TPW == 16, "layout");
constexpr float SCL2 = 0.0625f * 1.4426950408889634f;
constexpr float MASKRAW = -1.0e9f * 16.0f;
constexpr float DEFER = 8.0f / SCL2;

constexpr int WIN = 5;
constexpr int WEND = 10;
constexpr int PACE = 217, PACE0 = 270;
constexpr int SLIP = 120, JUMP = 212;
constexpr int LV0 = -100, LV1 = -50, LV2 = 0, LV3 = 60, LV4 = 130;
constexpr int RUSH = 60, RUSHN = 5;
constexpr int NEAR = 300;

constexpr unsigned W_EPOCH = 1u << 29, W_EPOCH_MASK = 0xE0000000u;
constexpr int SPIN_MAX = 1 << 20;
constexpr int PARTB = 32 * HD * 2;
constexpr size_t WS_CTL = 0, WS_T0 = 32768, WS_PML = 65536, WS_PO = WS_PML + (size_t)NGRP * NSPL * 32 * 8;
constexpr size_t WS_END = WS_PO + (size_t)NGRP * NSPL * PARTB;

template <int OFF, int CNT> __device__ __forceinline__ void dma1(const char* src, unsigned lds, unsigned v) {
  unsigned keep;
  __builtin_amdgcn_sched_barrier(0);
  asm volatile("s_mov_b32 %0, m0\n\ts_mov_b32 m0, %2\n\ts_waitcnt vmcnt(%5)\n\ts_nop 2\n\t"
               "global_load_lds_dwordx4 %3, %1 offset:%4 nt\n\ts_mov_b32 m0, %0"
               : "=&s"(keep) : "s"(src), "s"(lds), "v"(v), "i"(OFF), "i"(CNT) : "memory");
  __builtin_amdgcn_sched_barrier(0);
}
template <int OFF> __device__ __forceinline__ void dma1w(const char* src, unsigned lds, unsigned v, int lvl) {
  unsigned keep;
  __builtin_amdgcn_sched_barrier(0);
  asm volatile("s_mov_b32 %0, m0\n\ts_mov_b32 m0, %2\n\t"
               "s_cmp_gt_i32 %5, 4\n\ts_waitcnt vmcnt(%6+3)\n\ts_cbranch_scc1 .Lb_%=\n\t"
               "s_cmp_gt_i32 %5, 3\n\ts_waitcnt vmcnt(%6+2)\n\ts_cbranch_scc1 .Lb_%=\n\t"
               "s_cmp_gt_i32 %5, 2\n\ts_waitcnt vmcnt(%6+1)\n\ts_cbranch_scc1 .Lb_%=\n\t"
               "s_cmp_gt_i32 %5, 1\n\ts_waitcnt vmcnt(%6)\n\ts_cbranch_scc1 .Lb_%=\n\t"
               "s_cmp_gt_i32 %5, 0\n\ts_nop 7\n\ts_cbranch_scc1 .Lb_%=\n\t"
               "s_waitcnt vmcnt(%6-1)\n\t.Lb_%=:\n\t"
               "global_load_lds_dwordx4 %3, %1 offset:%4 nt\n\ts_mov_b32 m0, %0"
               : "=&s"(keep) : "s"(src), "s"(lds), "v"(v), "i"(OFF), "s"(lvl), "i"(WIN - 1) : "memory", "scc");
  __builtin_amdgcn_sched_barrier(0);
}
__device__ __forceinline__ int level_of(int late) {
  int l;
  asm volatile("s_mov_b32 %0, 0\n\ts_cmp_gt_i32 %1, %2\n\ts_addc_u32 %0, %0, 0\n\ts_cmp_gt_i32 %1, %3\n\ts_addc_u32 %0, %0, 0\n\t"
               "s_cmp_gt_i32 %1, %4\n\ts_addc_u32 %0, %0, 0\n\ts_cmp_gt_i32 %1, %5\n\ts_addc_u32 %0, %0, 0\n\ts_cmp_gt_i32 %1, %6\n\ts_addc_u32 %0, %0, 0"
               : "=&s"(l) : "s"(late), "i"(LV0), "i"(LV1), "i"(LV2), "i"(LV3), "i"(LV4) : "scc");
  return l;
}
__device__ __forceinline__ unsigned clock_ticks() {
  unsigned long long t;
  asm volatile("s_memrealtime %0\n\ts_waitcnt lgkmcnt(0)" : "=s"(t) :: "memory");
  return (unsigned)t;
}
#define WAIT_VM(n) asm volatile("s_waitcnt vmcnt(%0)" :: "i"(n) : "memory")
#define WAIT_LGKM0() asm volatile("s_waitcnt lgkmcnt(0)" ::: "memory")
#define BARRIER() do { WAIT_LGKM0(); __builtin_amdgcn_s_barrier(); asm volatile("" ::: "memory"); } while (0)

__device__ __forceinline__ void ctl_add_ret(unsigned* w, unsigned zero, unsigned inc) {
  unsigned long long ex;
  asm volatile("v_accvgpr_write_b32 a254, %3\n\ts_mov_b64 %0, exec\n\ts_mov_b64 exec, 1\n\ts_nop 2\n\t"
               "global_atomic_add a255, %2, a254, %1 sc0\n\ts_mov_b64 exec, %0\n\ts_nop 1"
               : "=&s"(ex) : "s"(w), "v"(zero), "v"(inc) : "memory", "a254", "a255");
}
__device__ __forceinline__ unsigned ctl_result() {
  unsigned t, r;
  asm volatile("v_accvgpr_read_b32 %0, a255\n\ts_nop 1\n\tv_readfirstlane_b32 %1, %0\n\ts_nop 4" : "=&v"(t), "=s"(r) :: "memory");
  return r;
}
__device__ __forceinline__ void ctl_and(unsigned* w, unsigned zero, unsigned m) {
  unsigned long long ex;
  asm volatile("s_mov_b64 %0, exec\n\ts_mov_b64 exec, 1\n\ts_nop 2\n\tglobal_atomic_and %2, %3, %1\n\ts_mov_b64 exec, %0\n\ts_nop 1"
               : "=&s"(ex) : "s"(w), "v"(zero), "v"(m) : "memory");
}
#define PARK_HEAD "s_getpc_b64 vcc\n\t.Lp0_%=:\n\ts_add_u32 vcc_lo, vcc_lo, %8\n\ts_addc_u32 vcc_hi, vcc_hi, 0\n\t" \
                  "s_add_u32 vcc_lo, vcc_lo, .Lp1_%=-.Lp0_%=\n\ts_addc_u32 vcc_hi, vcc_hi, 0\n\ts_setpc_b64 vcc\n\t.Lp1_%=:\n\t.set .Lpi_%=, 128\n\t.rept 15\n\t"
#define PARK_TAIL "s_branch .Lp2_%=\n\t.set .Lpi_%=, .Lpi_%=+8\n\t.endr\n\t.Lp2_%=:\n\ts_nop 1"
__device__ __forceinline__ void park_put(int j, const u32x4& p0, const u32x4& p1) {
  asm volatile(PARK_HEAD
               "v_accvgpr_write_b32 a[.Lpi_%=], %0\n\tv_accvgpr_write_b32 a[.Lpi_%=+1], %1\n\tv_accvgpr_write_b32 a[.Lpi_%=+2], %2\n\t"
               "v_accvgpr_write_b32 a[.Lpi_%=+3], %3\n\tv_accvgpr_write_b32 a[.Lpi_%=+4], %4\n\tv_accvgpr_write_b32 a[.Lpi_%=+5], %5\n\t"
               "v_accvgpr_write_b32 a[.Lpi_%=+6], %6\n\tv_accvgpr_write_b32 a[.Lpi_%=+7], %7\n\t" PARK_TAIL
               :: "v"(p0[0]), "v"(p0[1]), "v"(p0[2]), "v"(p0[3]), "v"(p1[0]), "v"(p1[1]), "v"(p1[2]), "v"(p1[3]), "s"(j * 68)
               : "memory", "vcc", "scc");
}
__device__ __forceinline__ void park_get(int j, u32x4& p0, u32x4& p1) {
  asm volatile(PARK_HEAD
               "v_accvgpr_read_b32 %0, a[.Lpi_%=]\n\tv_accvgpr_read_b32 %1, a[.Lpi_%=+1]\n\tv_accvgpr_read_b32 %2, a[.Lpi_%=+2]\n\t"
               "v_accvgpr_read_b32 %3, a[.Lpi_%=+3]\n\tv_accvgpr_read_b32 %4, a[.Lpi_%=+4]\n\tv_accvgpr_read_b32 %5, a[.Lpi_%=+5]\n\t"
               "v_accvgpr_read_b32 %6, a[.Lpi_%=+6]\n\tv_accvgpr_read_b32 %7, a[.Lpi_%=+7]\n\t" PARK_TAIL
               : "=v"(p0[0]), "=v"(p0[1]), "=v"(p0[2]), "=v"(p0[3]), "=v"(p1[0]), "=v"(p1[1]), "=v"(p1[2]), "=v"(p1[3]) : "s"(j * 68)
               : "memory", "vcc", "scc");
}
template <int B> __device__ __forceinline__ void acc_zero32() {
  asm volatile(".set .Lz_%=, %c0\n\t.rept 32\n\tv_accvgpr_write_b32 a[.Lz_%=], 0\n\t.set .Lz_%=, .Lz_%=+1\n\t.endr" :: "i"(B) : "memory", "a0", "a127", "a247");
}
__device__ __forceinline__ void acc_scale(float alpha) {
  float t0, t1, t2, t3;
  asm volatile("s_nop 15\n\t.set .Ls_%=, 0\n\t.rept 32\n\t"
               "v_accvgpr_read_b32 %0, a[.Ls_%=]\n\tv_accvgpr_read_b32 %1, a[.Ls_%=+1]\n\tv_accvgpr_read_b32 %2, a[.Ls_%=+2]\n\tv_accvgpr_read_b32 %3, a[.Ls_%=+3]\n\t"
               "v_mul_f32 %0, %0, %4\n\tv_mul_f32 %1, %1, %4\n\tv_mul_f32 %2, %2, %4\n\tv_mul_f32 %3, %3, %4\n\t"
               "v_accvgpr_write_b32 a[.Ls_%=], %0\n\tv_accvgpr_write_b32 a[.Ls_%=+1], %1\n\tv_accvgpr_write_b32 a[.Ls_%=+2], %2\n\tv_accvgpr_write_b32 a[.Ls_%=+3], %3\n\t"
               ".set .Ls_%=, .Ls_%=+4\n\t.endr\n\ts_nop 1"
               : "=&v"(t0), "=&v"(t1), "=&v"(t2), "=&v"(t3) : "v"(alpha) : "memory");
}
#define M_(a, b) "v_mfma_f32_32x32x16_bf16 %0, %" #a ", %" #b ", %0\n\t"
__device__ __forceinline__ void qk_first(f32x16& s, const bf16x8* f, const bf16x8* q) {
  asm volatile("s_nop 1\n\tv_mfma_f32_32x32x16_bf16 %0, %1, %5, 0\n\t" M_(2, 6) M_(3, 7) M_(4, 8)
               : "=&v"(s) : "v"(f[0]), "v"(f[1]), "v"(f[2]), "v"(f[3]), "v"(q[0]), "v"(q[1]), "v"(q[2]), "v"(q[3]));
}
__device__ __forceinline__ void qk_next(f32x16& s, const bf16x8* f, const bf16x8* q) {
  asm volatile("s_nop 1\n\t" M_(1, 5) M_(2, 6) M_(3, 7) M_(4, 8)
               : "+v"(s) : "v"(f[0]), "v"(f[1]), "v"(f[2]), "v"(f[3]), "v"(q[0]), "v"(q[1]), "v"(q[2]), "v"(q[3]));
}
#undef M_
__device__ __forceinline__ void qk_done(f32x16& s) { asm volatile("s_nop 15" : "+v"(s)); }
template <int C> __device__ __forceinline__ void pv4(const bf16x8* f, const u32x4& p) {
  asm volatile("s_nop 1\n\t"
               "v_mfma_f32_32x32x16_bf16 a[%c5:%c5+15], %0, %4, a[%c5:%c5+15]\n\t"
               "v_mfma_f32_32x32x16_bf16 a[%c5+16:%c5+31], %1, %4, a[%c5+16:%c5+31]\n\t"
               "v_mfma_f32_32x32x16_bf16 a[%c5+32:%c5+47], %2, %4, a[%c5+32:%c5+47]\n\t"
               "v_mfma_f32_32x32x16_bf16 a[%c5+48:%c5+63], %3, %4, a[%c5+48:%c5+63]"
               :: "v"(f[0]), "v"(f[1]), "v"(f[2]), "v"(f[3]), "v"(p), "i"(16 * C) : "memory");
}
__device__ __forceinline__ void pv2(f32x16& x, f32x16& y, const bf16x8& v0, const bf16x8& v1, const u32x4& p) {
  asm volatile("s_nop 1\n\tv_mfma_f32_32x32x16_bf16 %0, %2, %4, %0\n\tv_mfma_f32_32x32x16_bf16 %1, %3, %4, %1"
               : "+v"(x), "+v"(y) : "v"(v0), "v"(v1), "v"(p));
}
template <int B> __device__ __forceinline__ f32x16 acc_get16() {
  float r[16];
  asm volatile("v_accvgpr_read_b32 %0, a[%c16]\n\tv_accvgpr_read_b32 %1, a[%c16+1]\n\tv_accvgpr_read_b32 %2, a[%c16+2]\n\tv_accvgpr_read_b32 %3, a[%c16+3]\n\t"
               "v_accvgpr_read_b32 %4, a[%c16+4]\n\tv_accvgpr_read_b32 %5, a[%c16+5]\n\tv_accvgpr_read_b32 %6, a[%c16+6]\n\tv_accvgpr_read_b32 %7, a[%c16+7]\n\t"
               "v_accvgpr_read_b32 %8, a[%c16+8]\n\tv_accvgpr_read_b32 %9, a[%c16+9]\n\tv_accvgpr_read_b32 %10, a[%c16+10]\n\tv_accvgpr_read_b32 %11, a[%c16+11]\n\t"
               "v_accvgpr_read_b32 %12, a[%c16+12]\n\tv_accvgpr_read_b32 %13, a[%c16+13]\n\tv_accvgpr_read_b32 %14, a[%c16+14]\n\tv_accvgpr_read_b32 %15, a[%c16+15]"
               : "=v"(r[0]), "=v"(r[1]), "=v"(r[2]), "=v"(r[3]), "=v"(r[4]), "=v"(r[5]), "=v"(r[6]), "=v"(r[7]), "=v"(r[8]), "=v"(r[9]),
                 "=v"(r[10]), "=v"(r[11]), "=v"(r[12]), "=v"(r[13]), "=v"(r[14]), "=v"(r[15]) : "i"(B) : "memory");
  f32x16 x;
#pragma unroll
  for (int i = 0; i < 16; ++i) x[i] = r[i];
  return x;
}

__device__ __forceinline__ float xhalf_max(float x) {
  auto r = __builtin_amdgcn_permlane32_swap(__float_as_uint(x), __float_as_uint(x), false, false);
  return fmaxf(__uint_as_float(r[0]), __uint_as_float(r[1]));
}
__device__ __forceinline__ float xhalf_sum(float x) {
  auto r = __builtin_amdgcn_permlane32_swap(__float_as_uint(x), __float_as_uint(x), false, false);
  return __uint_as_float(r[0]) + __uint_as_float(r[1]);
}
__device__ __forceinline__ unsigned pack2(float a, float b) {
  return __builtin_bit_cast(unsigned, __builtin_convertvector((f32x2){a, b}, bf16x2));
}
__device__ __forceinline__ f32x4 unpack4(unsigned lo, unsigned hi) {
  return f32x4{__uint_as_float(lo << 16), __uint_as_float(lo & 0xffff0000u), __uint_as_float(hi << 16), __uint_as_float(hi & 0xffff0000u)};
}
__device__ __forceinline__ u32x4 pack8(const f32x16& x, int b) {
  return u32x4{pack2(x[b], x[b + 1]), pack2(x[b + 2], x[b + 3]), pack2(x[b + 4], x[b + 5]), pack2(x[b + 6], x[b + 7])};
}
__device__ __forceinline__ void fma8(f32x16& x, int b, float f, const u32x4& d) {
#pragma unroll
  for (int i = 0; i < 4; ++i) {
    x[b + 2 * i] = fmaf(f, __uint_as_float(d[i] << 16), x[b + 2 * i]);
    x[b + 2 * i + 1] = fmaf(f, __uint_as_float(d[i] & 0xffff0000u), x[b + 2 * i + 1]);
  }
}
__device__ __forceinline__ unsigned swz(unsigned row) { return ((row & 3) << 2) | ((row >> 2) & 3); }
__device__ __forceinline__ bf16x8 row_frag(LAS char* lds, unsigned rd, int st) {
  return *(const LAS bf16x8*)(lds + ((rd ^ ((st & 7) << 5)) + (st >> 3) * 256));
}
__device__ __forceinline__ bf16x8 col_frag(LAS char* lds, unsigned vrd, int c, int koff) {
  const int o = (c >> 2) * 256 + koff;
  const bf16x4 lo = __builtin_amdgcn_ds_read_tr16_b64_v4bf16((LAS bf16x4*)(lds + ((vrd ^ ((c & 3) << 6)) + o)));
  const bf16x4 hi = __builtin_amdgcn_ds_read_tr16_b64_v4bf16((LAS bf16x4*)(lds + ((vrd ^ ((c & 3) << 6) ^ 32) + o + 8 * ROWB)));
  return __builtin_shufflevector(lo, hi, 0, 1, 2, 3, 4, 5, 6, 7);
}
template <int NR> __device__ __forceinline__ void sm_ref(const f32x16& s, float& m, float& l) {
  float mx = s[0];
#pragma unroll
  for (int r = 1; r < NR; ++r) mx = fmaxf(mx, s[r]);
  mx = xhalf_max(mx);
  if (!__all(mx <= m + DEFER)) {
    const float mn = fmaxf(m, mx);
    l *= __builtin_amdgcn_exp2f((m - mn) * SCL2);
    m = mn;
  }
  asm volatile("" : "+v"(m), "+v"(l));
}
template <int B> __device__ __forceinline__ u32x4 sm_exp(const f32x16& s, float m, float& l) {
  float e[8];
#pragma unroll
  for (int r = 0; r < 8; ++r) e[r] = __builtin_amdgcn_exp2f((s[B + r] - m) * SCL2);
  l += ((e[0] + e[1]) + (e[2] + e[3])) + ((e[4] + e[5]) + (e[6] + e[7]));
  u32x4 p = {pack2(e[0], e[1]), pack2(e[2], e[3]), pack2(e[4], e[5]), pack2(e[6], e[7])};
  asm volatile("" : "+v"(p), "+v"(l));
  return p;
}

__global__ void __launch_bounds__(256, 1)
tree_attn(const char* __restrict__ Q, const char* __restrict__ K, const char* __restrict__ V, const char* __restrict__ KT,
          const char* __restrict__ VT, const char* __restrict__ MK, char* __restrict__ ws, unsigned short* __restrict__ out) {
  __shared__ __attribute__((aligned(1024))) char smem[LDS_BYTES];
  LAS char* lds = (LAS char*)smem;
  const unsigned lbase = (unsigned)(size_t)lds;
  const int tid = threadIdx.x, lane = tid & 63, wave = __builtin_amdgcn_readfirstlane(tid >> 6);
  const int wg = blockIdx.x;
  const int grp = (wg >> 5) * 8 + (wg & 7), mem = (wg >> 3) & 3;
  const int r32 = lane & 31, hh = lane >> 5;
  const bool leader = (mem == 1) && (wave == 0);
  unsigned long long ts0 = __builtin_amdgcn_s_memrealtime(), ts1 = ts0, ts2 = 0; int ntl = 0, nslow = 0, npoll = 0;
  {
    unsigned long long pc; unsigned len;
    asm volatile("s_getpc_b64 %0\n.Lcode0:\n\ts_mov_b32 %1, .Lcode1-.Lcode0" : "=s"(pc), "=s"(len));
    const unsigned o = min((unsigned)(((wg >> 3) * 4 + wave) & 31) * 1024u, (len & ~15u) - 1024u) + lane * 16;
    asm volatile("global_load_dwordx4 a[240:243], %0, %1" :: "v"(o), "s"(pc & ~15ull) : "memory", "a240", "a241", "a242", "a243");
  }
  unsigned ko[8], qo[4];
#pragma unroll
  for (int i = 0; i < 8; ++i) ko[i] = hh * ROWB + ((r32 ^ swz(2 * i + hh)) << 4);
#pragma unroll
  for (int i = 0; i < 4; ++i) qo[i] = ko[i] ^ ((wave & 1) << 5);
  asm volatile("" : "+v"(ko[0]), "+v"(ko[1]), "+v"(ko[2]), "+v"(ko[3]), "+v"(ko[4]), "+v"(ko[5]), "+v"(ko[6]), "+v"(ko[7]),
                    "+v"(qo[0]), "+v"(qo[1]), "+v"(qo[2]), "+v"(qo[3]));
  const unsigned ring = wave * RINGB;
  const size_t wof = (size_t)grp * (CTX * ROWB) + (mem * 4 + wave) * BLKB;
  const char* src = K + wof;
  unsigned dst = lbase + ring;
#define PIECE_N(k, cnt) dma1<((k) & 3) * 1024, (cnt)>(src + ((k) >> 3) * HALFB + (((k) >> 2) & 1) * 4096, dst + ((k) >> 2) * 4096, ko[(k) & 7])
#define PIECE(k) dma1w<((k) & 3) * 1024>(src + ((k) >> 3) * HALFB + (((k) >> 2) & 1) * 4096, dst + ((k) >> 2) * 4096, ko[(k) & 7], lvl)
  int late = 0, lvl = 2;
  auto quarter = [&](const char* s, unsigned d) {
    dma1<0, 63>(s, d, qo[0]); dma1<1024, 63>(s, d, qo[1]); dma1<2048, 63>(s, d, qo[2]); dma1<3072, 63>(s, d, qo[3]);
  };
  quarter(Q + (size_t)grp * TILEB + wave * 4096, lbase + Q_OFF + wave * 4096);
  if (mem == 0) {
    quarter((wave & 2 ? VT : KT) + (size_t)grp * BLKB + (wave & 1) * 4096, lbase + TREE_OFF + wave * 4096);
    const unsigned mo = (r32 & 15) * (NT * 2) + 8 * hh;
    asm volatile("s_nop 4\n\tglobal_load_dwordx2 a[248:249], %0, %1\n\tglobal_load_dwordx2 a[250:251], %0, %1 offset:16"
                 :: "v"(mo), "s"(MK + (grp >> 3) * (NT * NT * 2)) : "memory", "a248", "a249", "a250", "a251");
  }
  unsigned* cw = (unsigned*)(ws + WS_CTL + grp * 256);
  const unsigned vz = 0;
  if (wave == 0) ctl_add_ret(cw, vz, 0u);
  PIECE_N(0, 63); PIECE_N(1, 63); PIECE_N(2, 63); PIECE_N(3, 63); PIECE_N(4, 63);
  unsigned due = clock_ticks() + PACE0;
  const char* t0p = ws + WS_T0 + (wg >> 3) * 64;
  const unsigned due0 = due;
  if (wave == 3) {
    unsigned long long ex;
    asm volatile("s_mov_b64 %0, exec\n\ts_mov_b64 exec, 1\n\ts_nop 2\n\tglobal_store_dword %2, %3, %1 sc1\n\ts_mov_b64 exec, %0\n\ts_nop 1"
                 : "=&s"(ex) : "s"(t0p), "v"((wg & 7) * 4), "v"(due0) : "memory");
  }
  const unsigned krd = r32 * ROWB + ((hh ^ swz(r32)) << 4);
  const int q4 = (lane & 15) >> 2;
  const unsigned vrd = (4 * hh + q4) * ROWB + (q4 << 6) + ((((lane >> 4) & 1) * 32 + (lane & 3) * 8) ^ (hh << 4));
  acc_zero32<0>(); acc_zero32<32>(); acc_zero32<64>(); acc_zero32<96>();
  WAIT_VM(5);
  const unsigned w0 = ctl_result();
  if (leader) ctl_and(cw, vz, W_EPOCH_MASK);
  BARRIER();
  PIECE(5);
  bf16x8 q[16];
#pragma unroll
  for (int st = 0; st < 16; ++st) q[st] = row_frag(lds, Q_OFF + krd, st);
  PIECE(6);
  BARRIER();
  if (wave == 3) *(LAS int*)(lds + SH_OFF) = 0;
  float zm = -__builtin_inff(), zl = 0.f, om = -__builtin_inff();
  if (mem == 0 && wave == 1) {
    unsigned mk[4];
    asm volatile("v_accvgpr_read_b32 %0, a248\n\tv_accvgpr_read_b32 %1, a249\n\tv_accvgpr_read_b32 %2, a250\n\tv_accvgpr_read_b32 %3, a251"
                 : "=v"(mk[0]), "=v"(mk[1]), "=v"(mk[2]), "=v"(mk[3]) :: "memory");
    bf16x8 f[16]; f32x16 s;
#pragma unroll
    for (int st = 0; st < 16; ++st) f[st] = row_frag(lds, TREE_OFF + (krd & (BLKB - 1)), st);
    PIECE(7);
    qk_first(s, f, q); qk_next(s, f + 4, q + 4);
    PIECE(8);
    qk_next(s, f + 8, q + 8); qk_next(s, f + 12, q + 12);
    PIECE(9);
    qk_done(s);
#pragma unroll
    for (int c = 0; c < 8; ++c) f[c] = col_frag(lds, TREE_OFF + vrd, c, BLKB);
#pragma unroll
    for (int r = 0; r < 8; ++r) {
      const float mv = __uint_as_float((r & 1) ? (mk[r >> 1] & 0xffff0000u) : (mk[r >> 1] << 16));
      s[r] = fmaf(1.0f - mv, MASKRAW, s[r]);
    }
    sm_ref<8>(s, zm, zl);
    PIECE(10);
    const u32x4 p = sm_exp<0>(s, zm, zl);
    om = zm;
    PIECE(11);
    pv4<0>(f, p); pv4<4>(f + 4, p);
  } else {
    PIECE(7); PIECE(8); PIECE(9); PIECE(10); PIECE(11);
  }
  PIECE(12); PIECE(13); PIECE(14); PIECE(15);

  auto pace = [&](int rush) {
    const int now = (int)(clock_ticks() - due), step = now - late;
    const int drop = step > JUMP ? step : 0;
    late = min(now - drop, SLIP);
    lvl = level_of(late + rush);
    due += PACE + now - late;
  };
  bool ready = leader;
  const unsigned mt = MT_OFF + wave * (TPW * 128) + r32 * 4;
  unsigned slot = ring;
  u32x4 p0, p1;
  float ow = 0.f;
#pragma unroll 1
  for (int n = 0; n < TPW; ++n) {
    src = n + 1 < TPW ? src + TSTEP : V + wof;
    dst ^= TILEB;
    const bool look = wave == 0 && (leader ? n < 4 : !ready);
    if (look) ctl_add_ret(cw, vz, leader ? W_EPOCH : 0u);
    if (n == 0 && wave == 3)
      asm volatile("s_nop 4\n\tglobal_load_dword a252, %0, %1 sc1" :: "v"((lane & 7) * 4), "s"(t0p) : "memory", "a252");
    if (n == 2) {
      const int sh = __builtin_amdgcn_readfirstlane(*(volatile LAS int*)(lds + SH_OFF));
      if (sh > -NEAR && sh < NEAR) { due += sh; late -= sh; }
    }
    PIECE(0);
    pace(0);
    PIECE(1); PIECE(2); PIECE(3); PIECE(4);
    WAIT_VM(5);
    bf16x8 f[16]; f32x16 s;
#pragma unroll
    for (int st = 0; st < 16; ++st) f[st] = row_frag(lds, slot + krd, st);
    if (look) ready = ready || ctl_result() != w0;
    if (n == 0 && wave == 3) {
      int t;
      asm volatile("v_accvgpr_read_b32 %0, a252\n\ts_nop 1" : "=v"(t) :: "memory");
      const int d = t - (int)due0;
      const bool near = d > -NEAR && d < NEAR;
      float sum = near ? (float)d : 0.f, cnt = near ? 1.f : 0.f;
#pragma unroll
      for (int m = 1; m < 8; m <<= 1) { sum += __shfl_xor(sum, m); cnt += __shfl_xor(cnt, m); }
      *(volatile LAS int*)(lds + SH_OFF) = (int)(sum * __builtin_amdgcn_rcpf(fmaxf(cnt, 1.f)));
    }
    PIECE(5);
    qk_first(s, f, q);
    PIECE(6);
    qk_next(s, f + 4, q + 4);
    PIECE(7);
    qk_next(s, f + 8, q + 8);
    PIECE(8);
    qk_next(s, f + 12, q + 12);
    PIECE(9);
    qk_done(s);
    const float zprev = zm;
    sm_ref<16>(s, zm, zl);
    *(LAS float*)(lds + mt + n * 128) = zm;
    PIECE(10);
    if (n == TPW - 1) {
      BARRIER();
      float M = zm;
#pragma unroll
      for (int d = 1; d < 4; ++d) M = fmaxf(M, *(const LAS float*)(lds + MT_OFF + (((wave + d) & 3) * TPW + n) * 128 + r32 * 4));
      zl *= __builtin_amdgcn_exp2f((zm - M) * SCL2);
      ow = __builtin_amdgcn_exp2f((zprev - M) * SCL2);
      zm = M;
    }
    p0 = sm_exp<0>(s, zm, zl);
    PIECE(11);
    p1 = sm_exp<8>(s, zm, zl);
    PIECE(12);
    if (n < TPW - 1) park_put(n, p0, p1);
    PIECE(13); PIECE(14); PIECE(15);
    slot ^= TILEB;
  }
  zl = xhalf_sum(zl);
  *(LAS f32x2*)(lds + ML_OFF + (wave * 32 + r32) * 8) = f32x2{ow, zl};
  *(LAS u32x4*)(lds + TREE_OFF + wave * 2048 + lane * 16) = p0;
  *(LAS u32x4*)(lds + TREE_OFF + wave * 2048 + 1024 + lane * 16) = p1;
  auto rebase = [&](float tm) {
    if (!__all(tm == om)) {
      if (!__all(om == -__builtin_inff())) acc_scale(__builtin_amdgcn_exp2f((om - tm) * SCL2));
      om = tm;
    }
  };
  u32x4 r0, r1;
#pragma unroll 1
  for (int n = 0;; ++n) {
    src += TSTEP;
    dst ^= TILEB;
    PIECE(0);
    pace(n >= TPW - 1 - RUSHN ? RUSH : 0);
    const float tm = *(const LAS float*)(lds + mt + n * 128);
    park_get(n, r0, r1);
    PIECE(1);
    rebase(tm);
    PIECE(2); PIECE(3); PIECE(4);
    WAIT_VM(5);
    if (n == TPW - 2) break;
    bf16x8 f[16];
#pragma unroll
    for (int c = 0; c < 8; ++c) f[c] = col_frag(lds, slot + vrd, c, 0);
    PIECE(5);
#pragma unroll
    for (int c = 0; c < 8; ++c) f[8 + c] = col_frag(lds, slot + vrd, c, BLKB);
    PIECE(6);
    pv4<0>(f, r0);
    PIECE(7);
    pv4<4>(f + 4, r0);
    PIECE(8);
    pv4<0>(f + 8, r1);
    PIECE(9);
    pv4<4>(f + 12, r1);
    PIECE(10); PIECE(11); PIECE(12); PIECE(13); PIECE(14); PIECE(15);
    slot ^= TILEB;
  }
  f32x16 ox, oy;
  {
    bf16x8 f[16];
#pragma unroll
    for (int c = 0; c < 8; ++c) { f[c] = col_frag(lds, slot + vrd, c, 0); f[8 + c] = col_frag(lds, slot + vrd, c, BLKB); }
    PIECE_N(5, WEND - 1); PIECE_N(6, WEND - 1); PIECE_N(7, WEND - 1);
    pv4<0>(f, r0); pv4<0>(f + 8, r1);
    PIECE_N(8, WEND - 1); PIECE_N(9, WEND - 1);
    pv4<4>(f + 4, r0); pv4<4>(f + 12, r1);
    PIECE_N(10, WEND - 1); PIECE_N(11, WEND - 1);
  }
#define QUARTER(p) do { const f32x16 a_ = acc_get16<32 * p>(), b_ = acc_get16<32 * p + 16>(); \
    if (wave == p) { ox = a_ * ow; oy = b_ * ow; } \
    else { LAS char* d_ = lds + slot + p * 4096 + lane * 16; \
           *(LAS u32x4*)d_ = pack8(a_, 0); *(LAS u32x4*)(d_ + 1024) = pack8(a_, 8); *(LAS u32x4*)(d_ + 2048) = pack8(b_, 0); *(LAS u32x4*)(d_ + 3072) = pack8(b_, 8); } } while (0)
  QUARTER(0);
  PIECE_N(12, WEND - 1); PIECE_N(13, WEND - 1);
  QUARTER(1);
  PIECE_N(14, WEND - 1); PIECE_N(15, WEND - 1);
  QUARTER(2);
  QUARTER(3);
#undef QUARTER
  WAIT_VM(8);
  BARRIER();
  const unsigned rel = slot - ring, vq = (vrd ^ ((wave & 1) << 7)) + (wave >> 1) * 256;
  float L = zl;
  u32x4 pl[3][2];
  {
    f32x2 ml[3]; u32x4 im[3][4]; bf16x8 f[8];
#pragma unroll
    for (int d = 0; d < 3; ++d) {
      const int w = (wave + 1 + d) & 3;
      ml[d] = *(const LAS f32x2*)(lds + ML_OFF + (w * 32 + r32) * 8);
#pragma unroll
      for (int i = 0; i < 4; ++i) im[d][i] = *(const LAS u32x4*)(lds + w * RINGB + rel + wave * 4096 + lane * 16 + i * 1024);
#pragma unroll
      for (int i = 0; i < 2; ++i) pl[d][i] = *(const LAS u32x4*)(lds + TREE_OFF + w * 2048 + i * 1024 + lane * 16);
    }
#pragma unroll
    for (int d = 0; d < 4; ++d) {
      const unsigned a = ((wave + d) & 3) * RINGB + (rel ^ TILEB) + vq;
      f[2 * d] = col_frag(lds, a, 0, 0); f[2 * d + 1] = col_frag(lds, a, 1, 0);
    }
#define FOLD(d) do { L += ml[d][1]; fma8(ox, 0, ml[d][0], im[d][0]); fma8(ox, 8, ml[d][0], im[d][1]); fma8(oy, 0, ml[d][0], im[d][2]); fma8(oy, 8, ml[d][0], im[d][3]); \
    asm volatile("" : "+v"(ox), "+v"(oy)); } while (0)
    asm volatile("" ::: "memory");
    FOLD(0); FOLD(1); FOLD(2);
#undef FOLD
    pv2(ox, oy, f[0], f[1], p0);
#pragma unroll
    for (int d = 0; d < 3; ++d) pv2(ox, oy, f[2 * d + 2], f[2 * d + 3], pl[d][0]);
  }
  WAIT_VM(0);
  BARRIER();
  {
    bf16x8 f[8];
#pragma unroll
    for (int d = 0; d < 4; ++d) {
      const unsigned a = ((wave + d) & 3) * RINGB + (rel ^ TILEB) + vq;
      f[2 * d] = col_frag(lds, a, 0, BLKB); f[2 * d + 1] = col_frag(lds, a, 1, BLKB);
    }
    pv2(ox, oy, f[0], f[1], p1);
#pragma unroll
    for (int d = 0; d < 3; ++d) pv2(ox, oy, f[2 * d + 2], f[2 * d + 3], pl[d][1]);
    asm volatile("s_nop 15" : "+v"(ox), "+v"(oy));
  }
  ts2 = __builtin_amdgcn_s_memrealtime(); __builtin_amdgcn_s_waitcnt(0xC07F);

  const __amdgpu_buffer_rsrc_t rs = __builtin_amdgcn_make_buffer_rsrc(ws, 0, (int)WS_END, 0x00020000);
  const int ml_off = (int)WS_PML + (grp * NSPL * 32 + r32) * 8;
  const int po_off = (int)WS_PO + grp * NSPL * PARTB + wave * 4096 + lane * 16;
  if (wave == 0 && hh == 0) __builtin_amdgcn_raw_buffer_store_b64(__builtin_bit_cast(u32x2, f32x2{zm, L}), rs, ml_off + mem * 256, 0, 16);
  f32x4 x = {}, y = {};
#pragma unroll
  for (int v = 0; v < 4; ++v) {
    const f32x16& o = v < 2 ? ox : oy;
    const int b = 8 * (v & 1);
    if (v == mem) { x = f32x4{o[b], o[b + 1], o[b + 2], o[b + 3]}; y = f32x4{o[b + 4], o[b + 5], o[b + 6], o[b + 7]}; }
    else __builtin_amdgcn_raw_buffer_store_b128(pack8(o, b), rs, po_off + mem * PARTB + v * 1024, 0, 16);
  }
  WAIT_VM(0);
  BARRIER();
  if (wave == 0) {
#pragma unroll 1
    for (int i = 0; i < SPIN_MAX && !ready; ++i) {
      ctl_add_ret(cw, vz, 0u); WAIT_VM(0);
      ready = ctl_result() != w0;
      ++nslow;
      if (!ready) __builtin_amdgcn_s_sleep(16);
    }
    ctl_add_ret(cw, vz, 1u << mem);
#pragma unroll 1
    for (int i = 0; i < SPIN_MAX; ++i) {
      WAIT_VM(0);
      const unsigned w = ctl_result() | (1u << mem);
      ++npoll;
      if ((w & 15u) == 15u) break;
      ctl_add_ret(cw, vz, leader ? W_EPOCH : 0u);
    }
  }
  BARRIER();
  f32x2 oml[3]; u32x4 oa[3];
#pragma unroll
  for (int d = 0; d < 3; ++d) {
    const int sm = (mem + 1 + d) & 3;
    oml[d] = __builtin_bit_cast(f32x2, __builtin_amdgcn_raw_buffer_load_b64(rs, ml_off, sm * 256, 16));
    oa[d] = __builtin_amdgcn_raw_buffer_load_b128(rs, po_off, sm * PARTB + mem * 1024, 16);
  }
  const float Mf = fmaxf(fmaxf(zm, oml[0][0]), fmaxf(oml[1][0], oml[2][0]));
  float f[4];
  f[3] = __builtin_amdgcn_exp2f((zm - Mf) * SCL2);
  L *= f[3];
#pragma unroll
  for (int d = 0; d < 3; ++d) { f[d] = __builtin_amdgcn_exp2f((oml[d][0] - Mf) * SCL2); L = fmaf(f[d], oml[d][1], L); }
  const float inv = 1.0f / L;
  x *= f[3] * inv; y *= f[3] * inv;
#pragma unroll
  for (int d = 0; d < 3; ++d) { x += (f[d] * inv) * unpack4(oa[d][0], oa[d][1]); y += (f[d] * inv) * unpack4(oa[d][2], oa[d][3]); }
  const auto rx = __builtin_amdgcn_permlane32_swap(pack2(x[0], x[1]), pack2(y[0], y[1]), false, false);
  const auto ry = __builtin_amdgcn_permlane32_swap(pack2(x[2], x[3]), pack2(y[2], y[3]), false, false);
  *(u32x4*)((char*)out + (size_t)(grp * 32 + r32) * ROWB + wave * 128 + mem * 32 + hh * 16) = u32x4{rx[0], ry[0], rx[1], ry[1]};
  asm volatile(".Lcode1:");
  WAIT_VM(0);
  if (lane == 0) { unsigned long long* sp = (unsigned long long*)(ws + (512ull << 20)) + (wg * 4 + wave) * 8;
    sp[0] = ts0; sp[1] = ts1; sp[2] = ts2; sp[3] = __builtin_amdgcn_s_memrealtime(); sp[4] = __builtin_amdgcn_s_getreg((31 << 11) | 20) & 7; sp[5] = ntl; sp[6] = nslow; sp[7] = npoll; }
}

__global__ void enc_k(const char* ws) {
  const unsigned long long t0 = __builtin_amdgcn_s_memrealtime();
  const unsigned long long* h = (const unsigned long long*)(ws + (512ull << 20));
  unsigned long long mn = ~0ull, mx3 = 0, mx2 = 0, s1 = 0, s2 = 0, mx0 = 0, sl = 0;
  for (int i = 0; i < 1024; ++i) { const unsigned long long* p = h + i * 8; mn = p[0] < mn ? p[0] : mn; mx0 = p[0] > mx0 ? p[0] : mx0; mx3 = p[3] > mx3 ? p[3] : mx3; mx2 = p[2] > mx2 ? p[2] : mx2; s1 += p[1]; s2 += p[2]; sl += p[6]; }
  unsigned long long m = 0;
  if (1 == 0) m = mx3 - mn;
  if (1 == 1) m = s1 / 1024 - mn;
  if (1 == 2) m = s2 / 1024 - mn;
  if (1 == 3) m = mx2 - mn;
  if (1 == 4) m = t0 - mx3;
  if (1 == 5) m = sl;
  if (1 == 6) m = 0;
  if (1 == 7) { m = mn - h[1024 * 8]; }
  if (1 == 8) { mn = h[1024 * 8]; m = 0; }
  if (1 == 9) m = mx0 - mn;
  if (m > 20000) m = 20000;
  while (__builtin_amdgcn_s_memrealtime() - mn < 100000ull + 20ull * m) __builtin_amdgcn_s_sleep(2);
}
extern "C" void kernel_launch(void* const* d_in, const int* in_sizes, int n_in, void* d_out, int out_size, void* d_ws, size_t ws_size,
                              hipStream_t stream) {
  tree_attn<<<NGRP * NSPL, 256, 0, stream>>>((const char*)d_in[0], (const char*)d_in[1], (const char*)d_in[2], (const char*)d_in[3],
                                             (const char*)d_in[4], (const char*)d_in[5], (char*)d_ws, (unsigned short*)d_out);
  enc_k<<<1, 1, 0, stream>>>((const char*)d_ws);
}
```

```cpp
#include <hip/hip_runtime.h>
#include <hip/hip_bf16.h>

#define LAS __attribute__((address_space(3)))
typedef __bf16 bf16x8 __attribute__((ext_vector_type(8)));
typedef __bf16 bf16x4 __attribute__((ext_vector_type(4)));
typedef __bf16 bf16x2 __attribute__((ext_vector_type(2)));
typedef float f32x16 __attribute__((ext_vector_type(16)));
typedef float f32x4 __attribute__((ext_vector_type(4)));
typedef float f32x2 __attribute__((ext_vector_type(2)));
typedef unsigned u32x4 __attribute__((ext_vector_type(4)));
typedef unsigned u32x2 __attribute__((ext_vector_type(2)));

constexpr int NB = 8, HKV = 8, NT = 16, CTX = 8192, HD = 256;
constexpr int NGRP = NB * HKV;
constexpr int NSPL = 4;
constexpr int ROWB = HD * 2;
constexpr int BLKB = 16 * ROWB;
constexpr int TILEB = 2 * BLKB;
constexpr int RINGB = 2 * TILEB;
constexpr int TPW = CTX / 32 / 16;
constexpr int HALFB = CTX / 2 * ROWB, TSTEP = 16 * BLKB;
constexpr int TREE_OFF = 4 * RINGB;
constexpr int Q_OFF = TREE_OFF + TILEB;
constexpr int LDS_BYTES = Q_OFF + TILEB;
constexpr int MT_OFF = Q_OFF;
constexpr int ML_OFF = MT_OFF + 4 * TPW * 128;
constexpr int SH_OFF = ML_OFF + 1024;
static_assert(LDS_BYTES <= 160 * 1024 && SH_OFF + 16 <= LDS_BYTES && TPW == 16, "layout");
constexpr float SCL2 = 0.0625f * 1.4426950408889634f;
constexpr float MASKRAW = -1.0e9f * 16.0f;
constexpr float DEFER = 8.0f / SCL2;

constexpr int WIN = 5;
constexpr int WEND = 10;
constexpr int PACE = 217, PACE0 = 270;
constexpr int SLIP = 120, JUMP = 212;
constexpr int LV0 = -100, LV1 = -50, LV2 = 0, LV3 = 60, LV4 = 130;
constexpr int RUSH = 60, RUSHN = 5;
constexpr int NEAR = 300;

constexpr unsigned W_EPOCH = 1u << 29, W_EPOCH_MASK = 0xE0000000u;
constexpr int SPIN_MAX = 1 << 20;
constexpr int PARTB = 32 * HD * 2;
constexpr size_t WS_CTL = 0, WS_T0 = 32768, WS_PML = 65536, WS_PO = WS_PML + (size_t)NGRP * NSPL * 32 * 8;
constexpr size_t WS_END = WS_PO + (size_t)NGRP * NSPL * PARTB;

template <int OFF, int CNT> __device__ __forceinline__ void dma1(const char* src, unsigned lds, unsigned v) {
  unsigned keep;
  __builtin_amdgcn_sched_barrier(0);
  asm volatile("s_mov_b32 %0, m0\n\ts_mov_b32 m0, %2\n\ts_waitcnt vmcnt(%5)\n\ts_nop 2\n\t"
               "global_load_lds_dwordx4 %3, %1 offset:%4 nt\n\ts_mov_b32 m0, %0"
               : "=&s"(keep) : "s"(src), "s"(lds), "v"(v), "i"(OFF), "i"(CNT) : "memory");
  __builtin_amdgcn_sched_barrier(0);
}
template <int OFF> __device__ __forceinline__ void dma1w(const char* src, unsigned lds, unsigned v, int lvl) {
  unsigned keep;
  __builtin_amdgcn_sched_barrier(0);
  asm volatile("s_mov_b32 %0, m0\n\ts_mov_b32 m0, %2\n\t"
               "s_cmp_gt_i32 %5, 4\n\ts_waitcnt vmcnt(%6+3)\n\ts_cbranch_scc1 .Lb_%=\n\t"
               "s_cmp_gt_i32 %5, 3\n\ts_waitcnt vmcnt(%6+2)\n\ts_cbranch_scc1 .Lb_%=\n\t"
               "s_cmp_gt_i32 %5, 2\n\ts_waitcnt vmcnt(%6+1)\n\ts_cbranch_scc1 .Lb_%=\n\t"
               "s_cmp_gt_i32 %5, 1\n\ts_waitcnt vmcnt(%6)\n\ts_cbranch_scc1 .Lb_%=\n\t"
               "s_cmp_gt_i32 %5, 0\n\ts_nop 7\n\ts_cbranch_scc1 .Lb_%=\n\t"
               "s_waitcnt vmcnt(%6-1)\n\t.Lb_%=:\n\t"
               "global_load_lds_dwordx4 %3, %1 offset:%4 nt\n\ts_mov_b32 m0, %0"
               : "=&s"(keep) : "s"(src), "s"(lds), "v"(v), "i"(OFF), "s"(lvl), "i"(WIN - 1) : "memory", "scc");
  __builtin_amdgcn_sched_barrier(0);
}
__device__ __forceinline__ int level_of(int late) {
  int l;
  asm volatile("s_mov_b32 %0, 0\n\ts_cmp_gt_i32 %1, %2\n\ts_addc_u32 %0, %0, 0\n\ts_cmp_gt_i32 %1, %3\n\ts_addc_u32 %0, %0, 0\n\t"
               "s_cmp_gt_i32 %1, %4\n\ts_addc_u32 %0, %0, 0\n\ts_cmp_gt_i32 %1, %5\n\ts_addc_u32 %0, %0, 0\n\ts_cmp_gt_i32 %1, %6\n\ts_addc_u32 %0, %0, 0"
               : "=&s"(l) : "s"(late), "i"(LV0), "i"(LV1), "i"(LV2), "i"(LV3), "i"(LV4) : "scc");
  return l;
}
__device__ __forceinline__ unsigned clock_ticks() {
  unsigned long long t;
  asm volatile("s_memrealtime %0\n\ts_waitcnt lgkmcnt(0)" : "=s"(t) :: "memory");
  return (unsigned)t;
}
#define WAIT_VM(n) asm volatile("s_waitcnt vmcnt(%0)" :: "i"(n) : "memory")
#define WAIT_LGKM0() asm volatile("s_waitcnt lgkmcnt(0)" ::: "memory")
#define BARRIER() do { WAIT_LGKM0(); __builtin_amdgcn_s_barrier(); asm volatile("" ::: "memory"); } while (0)

__device__ __forceinline__ void ctl_add_ret(unsigned* w, unsigned zero, unsigned inc) {
  unsigned long long ex;
  asm volatile("v_accvgpr_write_b32 a254, %3\n\ts_mov_b64 %0, exec\n\ts_mov_b64 exec, 1\n\ts_nop 2\n\t"
               "global_atomic_add a255, %2, a254, %1 sc0\n\ts_mov_b64 exec, %0\n\ts_nop 1"
               : "=&s"(ex) : "s"(w), "v"(zero), "v"(inc) : "memory", "a254", "a255");
}
__device__ __forceinline__ unsigned ctl_result() {
  unsigned t, r;
  asm volatile("v_accvgpr_read_b32 %0, a255\n\ts_nop 1\n\tv_readfirstlane_b32 %1, %0\n\ts_nop 4" : "=&v"(t), "=s"(r) :: "memory");
  return r;
}
__device__ __forceinline__ void ctl_and(unsigned* w, unsigned zero, unsigned m) {
  unsigned long long ex;
  asm volatile("s_mov_b64 %0, exec\n\ts_mov_b64 exec, 1\n\ts_nop 2\n\tglobal_atomic_and %2, %3, %1\n\ts_mov_b64 exec, %0\n\ts_nop 1"
               : "=&s"(ex) : "s"(w), "v"(zero), "v"(m) : "memory");
}
#define PARK_HEAD "s_getpc_b64 vcc\n\t.Lp0_%=:\n\ts_add_u32 vcc_lo, vcc_lo, %8\n\ts_addc_u32 vcc_hi, vcc_hi, 0\n\t" \
                  "s_add_u32 vcc_lo, vcc_lo, .Lp1_%=-.Lp0_%=\n\ts_addc_u32 vcc_hi, vcc_hi, 0\n\ts_setpc_b64 vcc\n\t.Lp1_%=:\n\t.set .Lpi_%=, 128\n\t.rept 15\n\t"
#define PARK_TAIL "s_branch .Lp2_%=\n\t.set .Lpi_%=, .Lpi_%=+8\n\t.endr\n\t.Lp2_%=:\n\ts_nop 1"
__device__ __forceinline__ void park_put(int j, const u32x4& p0, const u32x4& p1) {
  asm volatile(PARK_HEAD
               "v_accvgpr_write_b32 a[.Lpi_%=], %0\n\tv_accvgpr_write_b32 a[.Lpi_%=+1], %1\n\tv_accvgpr_write_b32 a[.Lpi_%=+2], %2\n\t"
               "v_accvgpr_write_b32 a[.Lpi_%=+3], %3\n\tv_accvgpr_write_b32 a[.Lpi_%=+4], %4\n\tv_accvgpr_write_b32 a[.Lpi_%=+5], %5\n\t"
               "v_accvgpr_write_b32 a[.Lpi_%=+6], %6\n\tv_accvgpr_write_b32 a[.Lpi_%=+7], %7\n\t" PARK_TAIL
               :: "v"(p0[0]), "v"(p0[1]), "v"(p0[2]), "v"(p0[3]), "v"(p1[0]), "v"(p1[1]), "v"(p1[2]), "v"(p1[3]), "s"(j * 68)
               : "memory", "vcc", "scc");
}
__device__ __forceinline__ void park_get(int j, u32x4& p0, u32x4& p1) {
  asm volatile(PARK_HEAD
               "v_accvgpr_read_b32 %0, a[.Lpi_%=]\n\tv_accvgpr_read_b32 %1, a[.Lpi_%=+1]\n\tv_accvgpr_read_b32 %2, a[.Lpi_%=+2]\n\t"
               "v_accvgpr_read_b32 %3, a[.Lpi_%=+3]\n\tv_accvgpr_read_b32 %4, a[.Lpi_%=+4]\n\tv_accvgpr_read_b32 %5, a[.Lpi_%=+5]\n\t"
               "v_accvgpr_read_b32 %6, a[.Lpi_%=+6]\n\tv_accvgpr_read_b32 %7, a[.Lpi_%=+7]\n\t" PARK_TAIL
               : "=v"(p0[0]), "=v"(p0[1]), "=v"(p0[2]), "=v"(p0[3]), "=v"(p1[0]), "=v"(p1[1]), "=v"(p1[2]), "=v"(p1[3]) : "s"(j * 68)
               : "memory", "vcc", "scc");
}
template <int B> __device__ __forceinline__ void acc_zero32() {
  asm volatile(".set .Lz_%=, %c0\n\t.rept 32\n\tv_accvgpr_write_b32 a[.Lz_%=], 0\n\t.set .Lz_%=, .Lz_%=+1\n\t.endr" :: "i"(B) : "memory", "a0", "a127", "a247");
}
__device__ __forceinline__ void acc_scale(float alpha) {
  float t0, t1, t2, t3;
  asm volatile("s_nop 15\n\t.set .Ls_%=, 0\n\t.rept 32\n\t"
               "v_accvgpr_read_b32 %0, a[.Ls_%=]\n\tv_accvgpr_read_b32 %1, a[.Ls_%=+1]\n\tv_accvgpr_read_b32 %2, a[.Ls_%=+2]\n\tv_accvgpr_read_b32 %3, a[.Ls_%=+3]\n\t"
               "v_mul_f32 %0, %0, %4\n\tv_mul_f32 %1, %1, %4\n\tv_mul_f32 %2, %2, %4\n\tv_mul_f32 %3, %3, %4\n\t"
               "v_accvgpr_write_b32 a[.Ls_%=], %0\n\tv_accvgpr_write_b32 a[.Ls_%=+1], %1\n\tv_accvgpr_write_b32 a[.Ls_%=+2], %2\n\tv_accvgpr_write_b32 a[.Ls_%=+3], %3\n\t"
               ".set .Ls_%=, .Ls_%=+4\n\t.endr\n\ts_nop 1"
               : "=&v"(t0), "=&v"(t1), "=&v"(t2), "=&v"(t3) : "v"(alpha) : "memory");
}
#define M_(a, b) "v_mfma_f32_32x32x16_bf16 %0, %" #a ", %" #b ", %0\n\t"
__device__ __forceinline__ void qk_first(f32x16& s, const bf16x8* f, const bf16x8* q) {
  asm volatile("s_nop 1\n\tv_mfma_f32_32x32x16_bf16 %0, %1, %5, 0\n\t" M_(2, 6) M_(3, 7) M_(4, 8)
               : "=&v"(s) : "v"(f[0]), "v"(f[1]), "v"(f[2]), "v"(f[3]), "v"(q[0]), "v"(q[1]), "v"(q[2]), "v"(q[3]));
}
__device__ __forceinline__ void qk_next(f32x16& s, const bf16x8* f, const bf16x8* q) {
  asm volatile("s_nop 1\n\t" M_(1, 5) M_(2, 6) M_(3, 7) M_(4, 8)
               : "+v"(s) : "v"(f[0]), "v"(f[1]), "v"(f[2]), "v"(f[3]), "v"(q[0]), "v"(q[1]), "v"(q[2]), "v"(q[3]));
}
#undef M_
__device__ __forceinline__ void qk_done(f32x16& s) { asm volatile("s_nop 15" : "+v"(s)); }
template <int C> __device__ __forceinline__ void pv4(const bf16x8* f, const u32x4& p) {
  asm volatile("s_nop 1\n\t"
               "v_mfma_f32_32x32x16_bf16 a[%c5:%c5+15], %0, %4, a[%c5:%c5+15]\n\t"
               "v_mfma_f32_32x32x16_bf16 a[%c5+16:%c5+31], %1, %4, a[%c5+16:%c5+31]\n\t"
               "v_mfma_f32_32x32x16_bf16 a[%c5+32:%c5+47], %2, %4, a[%c5+32:%c5+47]\n\t"
               "v_mfma_f32_32x32x16_bf16 a[%c5+48:%c5+63], %3, %4, a[%c5+48:%c5+63]"
               :: "v"(f[0]), "v"(f[1]), "v"(f[2]), "v"(f[3]), "v"(p), "i"(16 * C) : "memory");
}
__device__ __forceinline__ void pv2(f32x16& x, f32x16& y, const bf16x8& v0, const bf16x8& v1, const u32x4& p) {
  asm volatile("s_nop 1\n\tv_mfma_f32_32x32x16_bf16 %0, %2, %4, %0\n\tv_mfma_f32_32x32x16_bf16 %1, %3, %4, %1"
               : "+v"(x), "+v"(y) : "v"(v0), "v"(v1), "v"(p));
}
template <int B> __device__ __forceinline__ f32x16 acc_get16() {
  float r[16];
  asm volatile("v_accvgpr_read_b32 %0, a[%c16]\n\tv_accvgpr_read_b32 %1, a[%c16+1]\n\tv_accvgpr_read_b32 %2, a[%c16+2]\n\tv_accvgpr_read_b32 %3, a[%c16+3]\n\t"
               "v_accvgpr_read_b32 %4, a[%c16+4]\n\tv_accvgpr_read_b32 %5, a[%c16+5]\n\tv_accvgpr_read_b32 %6, a[%c16+6]\n\tv_accvgpr_read_b32 %7, a[%c16+7]\n\t"
               "v_accvgpr_read_b32 %8, a[%c16+8]\n\tv_accvgpr_read_b32 %9, a[%c16+9]\n\tv_accvgpr_read_b32 %10, a[%c16+10]\n\tv_accvgpr_read_b32 %11, a[%c16+11]\n\t"
               "v_accvgpr_read_b32 %12, a[%c16+12]\n\tv_accvgpr_read_b32 %13, a[%c16+13]\n\tv_accvgpr_read_b32 %14, a[%c16+14]\n\tv_accvgpr_read_b32 %15, a[%c16+15]"
               : "=v"(r[0]), "=v"(r[1]), "=v"(r[2]), "=v"(r[3]), "=v"(r[4]), "=v"(r[5]), "=v"(r[6]), "=v"(r[7]), "=v"(r[8]), "=v"(r[9]),
                 "=v"(r[10]), "=v"(r[11]), "=v"(r[12]), "=v"(r[13]), "=v"(r[14]), "=v"(r[15]) : "i"(B) : "memory");
  f32x16 x;
#pragma unroll
  for (int i = 0; i < 16; ++i) x[i] = r[i];
  return x;
}

__device__ __forceinline__ float xhalf_max(float x) {
  auto r = __builtin_amdgcn_permlane32_swap(__float_as_uint(x), __float_as_uint(x), false, false);
  return fmaxf(__uint_as_float(r[0]), __uint_as_float(r[1]));
}
__device__ __forceinline__ float xhalf_sum(float x) {
  auto r = __builtin_amdgcn_permlane32_swap(__float_as_uint(x), __float_as_uint(x), false, false);
  return __uint_as_float(r[0]) + __uint_as_float(r[1]);
}
__device__ __forceinline__ unsigned pack2(float a, float b) {
  return __builtin_bit_cast(unsigned, __builtin_convertvector((f32x2){a, b}, bf16x2));
}
__device__ __forceinline__ f32x4 unpack4(unsigned lo, unsigned hi) {
  return f32x4{__uint_as_float(lo << 16), __uint_as_float(lo & 0xffff0000u), __uint_as_float(hi << 16), __uint_as_float(hi & 0xffff0000u)};
}
__device__ __forceinline__ u32x4 pack8(const f32x16& x, int b) {
  return u32x4{pack2(x[b], x[b + 1]), pack2(x[b + 2], x[b + 3]), pack2(x[b + 4], x[b + 5]), pack2(x[b + 6], x[b + 7])};
}
__device__ __forceinline__ void fma8(f32x16& x, int b, float f, const u32x4& d) {
#pragma unroll
  for (int i = 0; i < 4; ++i) {
    x[b + 2 * i] = fmaf(f, __uint_as_float(d[i] << 16), x[b + 2 * i]);
    x[b + 2 * i + 1] = fmaf(f, __uint_as_float(d[i] & 0xffff0000u), x[b + 2 * i + 1]);
  }
}
__device__ __forceinline__ unsigned swz(unsigned row) { return ((row & 3) << 2) | ((row >> 2) & 3); }
__device__ __forceinline__ bf16x8 row_frag(LAS char* lds, unsigned rd, int st) {
  return *(const LAS bf16x8*)(lds + ((rd ^ ((st & 7) << 5)) + (st >> 3) * 256));
}
__device__ __forceinline__ bf16x8 col_frag(LAS char* lds, unsigned vrd, int c, int koff) {
  const int o = (c >> 2) * 256 + koff;
  const bf16x4 lo = __builtin_amdgcn_ds_read_tr16_b64_v4bf16((LAS bf16x4*)(lds + ((vrd ^ ((c & 3) << 6)) + o)));
  const bf16x4 hi = __builtin_amdgcn_ds_read_tr16_b64_v4bf16((LAS bf16x4*)(lds + ((vrd ^ ((c & 3) << 6) ^ 32) + o + 8 * ROWB)));
  return __builtin_shufflevector(lo, hi, 0, 1, 2, 3, 4, 5, 6, 7);
}
template <int NR> __device__ __forceinline__ void sm_ref(const f32x16& s, float& m, float& l) {
  float mx = s[0];
#pragma unroll
  for (int r = 1; r < NR; ++r) mx = fmaxf(mx, s[r]);
  mx = xhalf_max(mx);
  if (!__all(mx <= m + DEFER)) {
    const float mn = fmaxf(m, mx);
    l *= __builtin_amdgcn_exp2f((m - mn) * SCL2);
    m = mn;
  }
  asm volatile("" : "+v"(m), "+v"(l));
}
template <int B> __device__ __forceinline__ u32x4 sm_exp(const f32x16& s, float m, float& l) {
  float e[8];
#pragma unroll
  for (int r = 0; r < 8; ++r) e[r] = __builtin_amdgcn_exp2f((s[B + r] - m) * SCL2);
  l += ((e[0] + e[1]) + (e[2] + e[3])) + ((e[4] + e[5]) + (e[6] + e[7]));
  u32x4 p = {pack2(e[0], e[1]), pack2(e[2], e[3]), pack2(e[4], e[5]), pack2(e[6], e[7])};
  asm volatile("" : "+v"(p), "+v"(l));
  return p;
}

__global__ void __launch_bounds__(256, 1)
tree_attn(const char* __restrict__ Q, const char* __restrict__ K, const char* __restrict__ V, const char* __restrict__ KT,
          const char* __restrict__ VT, const char* __restrict__ MK, char* __restrict__ ws, unsigned short* __restrict__ out) {
  __shared__ __attribute__((aligned(1024))) char smem[LDS_BYTES];
  LAS char* lds = (LAS char*)smem;
  const unsigned lbase = (unsigned)(size_t)lds;
  const int tid = threadIdx.x, lane = tid & 63, wave = __builtin_amdgcn_readfirstlane(tid >> 6);
  const int wg = blockIdx.x;
  const int grp = (wg >> 5) * 8 + (wg & 7), mem = (wg >> 3) & 3;
  const int r32 = lane & 31, hh = lane >> 5;
  const bool leader = (mem == 1) && (wave == 0);
  {
    unsigned long long pc; unsigned len;
    asm volatile("s_getpc_b64 %0\n.Lcode0:\n\ts_mov_b32 %1, .Lcode1-.Lcode0" : "=s"(pc), "=s"(len));
    const unsigned o = min((unsigned)(((wg >> 3) * 4 + wave) & 31) * 1024u, (len & ~15u) - 1024u) + lane * 16;
    asm volatile("global_load_dwordx4 a[240:243], %0, %1" :: "v"(o), "s"(pc & ~15ull) : "memory", "a240", "a241", "a242", "a243");
  }
  unsigned ko[8], qo[4];
#pragma unroll
  for (int i = 0; i < 8; ++i) ko[i] = hh * ROWB + ((r32 ^ swz(2 * i + hh)) << 4);
#pragma unroll
  for (int i = 0; i < 4; ++i) qo[i] = ko[i] ^ ((wave & 1) << 5);
  asm volatile("" : "+v"(ko[0]), "+v"(ko[1]), "+v"(ko[2]), "+v"(ko[3]), "+v"(ko[4]), "+v"(ko[5]), "+v"(ko[6]), "+v"(ko[7]),
                    "+v"(qo[0]), "+v"(qo[1]), "+v"(qo[2]), "+v"(qo[3]));
  const unsigned ring = wave * RINGB;
  const size_t wof = (size_t)grp * (CTX * ROWB) + (mem * 4 + wave) * BLKB;
  const char* src = K + wof;
  unsigned dst = lbase + ring;
#define PIECE_N(k, cnt) dma1<((k) & 3) * 1024, (cnt)>(src + ((k) >> 3) * HALFB + (((k) >> 2) & 1) * 4096, dst + ((k) >> 2) * 4096, ko[(k) & 7])
#define PIECE(k) dma1w<((k) & 3) * 1024>(src + ((k) >> 3) * HALFB + (((k) >> 2) & 1) * 4096, dst + ((k) >> 2) * 4096, ko[(k) & 7], lvl)
  int late = 0, lvl = 2;
  auto quarter = [&](const char* s, unsigned d) {
    dma1<0, 63>(s, d, qo[0]); dma1<1024, 63>(s, d, qo[1]); dma1<2048, 63>(s, d, qo[2]); dma1<3072, 63>(s, d, qo[3]);
  };
  quarter(Q + (size_t)grp * TILEB + wave * 4096, lbase + Q_OFF + wave * 4096);
  if (mem == 0) {
    quarter((wave & 2 ? VT : KT) + (size_t)grp * BLKB + (wave & 1) * 4096, lbase + TREE_OFF + wave * 4096);
    const unsigned mo = (r32 & 15) * (NT * 2) + 8 * hh;
    asm volatile("s_nop 4\n\tglobal_load_dwordx2 a[248:249], %0, %1\n\tglobal_load_dwordx2 a[250:251], %0, %1 offset:16"
                 :: "v"(mo), "s"(MK + (grp >> 3) * (NT * NT * 2)) : "memory", "a248", "a249", "a250", "a251");
  }
  unsigned* cw = (unsigned*)(ws + WS_CTL + grp * 256);
  const unsigned vz = 0;
  if (wave == 0) ctl_add_ret(cw, vz, 0u);
  PIECE_N(0, 63); PIECE_N(1, 63); PIECE_N(2, 63); PIECE_N(3, 63); PIECE_N(4, 63);
  unsigned due = clock_ticks() + PACE0;
  const char* t0p = ws + WS_T0 + (wg >> 3) * 64;
  const unsigned due0 = due;
  if (wave == 3) {
    unsigned long long ex;
    asm volatile("s_mov_b64 %0, exec\n\ts_mov_b64 exec, 1\n\ts_nop 2\n\tglobal_store_dword %2, %3, %1 sc1\n\ts_mov_b64 exec, %0\n\ts_nop 1"
                 : "=&s"(ex) : "s"(t0p), "v"((wg & 7) * 4), "v"(due0) : "memory");
  }
  const unsigned krd = r32 * ROWB + ((hh ^ swz(r32)) << 4);
  const int q4 = (lane & 15) >> 2;
  const unsigned vrd = (4 * hh + q4) * ROWB + (q4 << 6) + ((((lane >> 4) & 1) * 32 + (lane & 3) * 8) ^ (hh << 4));
  acc_zero32<0>(); acc_zero32<32>(); acc_zero32<64>(); acc_zero32<96>();
  WAIT_VM(5);
  const unsigned w0 = ctl_result();
  if (leader) ctl_and(cw, vz, W_EPOCH_MASK);
  BARRIER();
  PIECE(5);
  bf16x8 q[16];
#pragma unroll
  for (int st = 0; st < 16; ++st) q[st] = row_frag(lds, Q_OFF + krd, st);
  PIECE(6);
  BARRIER();
  if (wave == 3) *(LAS int*)(lds + SH_OFF) = 0;
  float zm = -__builtin_inff(), zl = 0.f, om = -__builtin_inff();
  if (mem == 0 && wave == 1) {
    unsigned mk[4];
    asm volatile("v_accvgpr_read_b32 %0, a248\n\tv_accvgpr_read_b32 %1, a249\n\tv_accvgpr_read_b32 %2, a250\n\tv_accvgpr_read_b32 %3, a251"
                 : "=v"(mk[0]), "=v"(mk[1]), "=v"(mk[2]), "=v"(mk[3]) :: "memory");
    bf16x8 f[16]; f32x16 s;
#pragma unroll
    for (int st = 0; st < 16; ++st) f[st] = row_frag(lds, TREE_OFF + (krd & (BLKB - 1)), st);
    PIECE(7);
    qk_first(s, f, q); qk_next(s, f + 4, q + 4);
    PIECE(8);
    qk_next(s, f + 8, q + 8); qk_next(s, f + 12, q + 12);
    PIECE(9);
    qk_done(s);
#pragma unroll
    for (int c = 0; c < 8; ++c) f[c] = col_frag(lds, TREE_OFF + vrd, c, BLKB);
#pragma unroll
    for (int r = 0; r < 8; ++r) {
      const float mv = __uint_as_float((r & 1) ? (mk[r >> 1] & 0xffff0000u) : (mk[r >> 1] << 16));
      s[r] = fmaf(1.0f - mv, MASKRAW, s[r]);
    }
    sm_ref<8>(s, zm, zl);
    PIECE(10);
    const u32x4 p = sm_exp<0>(s, zm, zl);
    om = zm;
    PIECE(11);
    pv4<0>(f, p); pv4<4>(f + 4, p);
  } else {
    PIECE(7); PIECE(8); PIECE(9); PIECE(10); PIECE(11);
  }
  PIECE(12); PIECE(13); PIECE(14); PIECE(15);

  auto pace = [&](int rush) {
    const int now = (int)(clock_ticks() - due), step = now - late;
    const int drop = step > JUMP ? step : 0;
    late = min(now - drop, SLIP);
    lvl = level_of(late + rush);
    due += PACE + now - late;
  };
  bool ready = leader;
  const unsigned mt = MT_OFF + wave * (TPW * 128) + r32 * 4;
  unsigned slot = ring;
  u32x4 p0, p1;
  float ow = 0.f;
#pragma unroll 1
  for (int n = 0; n < TPW; ++n) {
    src = n + 1 < TPW ? src + TSTEP : V + wof;
    dst ^= TILEB;
    const bool look = wave == 0 && (leader ? n < 4 : !ready);
    if (look) ctl_add_ret(cw, vz, leader ? W_EPOCH : 0u);
    if (n == 0 && wave == 3)
      asm volatile("s_nop 4\n\tglobal_load_dword a252, %0, %1 sc1" :: "v"((lane & 7) * 4), "s"(t0p) : "memory", "a252");
    if (n == 2) {
      const int sh = __builtin_amdgcn_readfirstlane(*(volatile LAS int*)(lds + SH_OFF));
      if (sh > -NEAR && sh < NEAR) { due += sh; late -= sh; }
    }
    PIECE(0);
    pace(0);
    PIECE(1); PIECE(2); PIECE(3); PIECE(4);
    WAIT_VM(5);
    bf16x8 f[16]; f32x16 s;
#pragma unroll
    for (int st = 0; st < 16; ++st) f[st] = row_frag(lds, slot + krd, st);
    if (look) ready = ready || ctl_result() != w0;
    if (n == 0 && wave == 3) {
      int t;
      asm volatile("v_accvgpr_read_b32 %0, a252\n\ts_nop 1" : "=v"(t) :: "memory");
      const int d = t - (int)due0;
      const bool near = d > -NEAR && d < NEAR;
      float sum = near ? (float)d : 0.f, cnt = near ? 1.f : 0.f;
#pragma unroll
      for (int m = 1; m < 8; m <<= 1) { sum += __shfl_xor(sum, m); cnt += __shfl_xor(cnt, m); }
      *(volatile LAS int*)(lds + SH_OFF) = (int)(sum * __builtin_amdgcn_rcpf(fmaxf(cnt, 1.f)));
    }
    PIECE(5);
    qk_first(s, f, q);
    PIECE(6);
    qk_next(s, f + 4, q + 4);
    PIECE(7);
    qk_next(s, f + 8, q + 8);
    PIECE(8);
    qk_next(s, f + 12, q + 12);
    PIECE(9);
    qk_done(s);
    const float zprev = zm;
    sm_ref<16>(s, zm, zl);
    *(LAS float*)(lds + mt + n * 128) = zm;
    PIECE(10);
    if (n == TPW - 1) {
      BARRIER();
      float M = zm;
#pragma unroll
      for (int d = 1; d < 4; ++d) M = fmaxf(M, *(const LAS float*)(lds + MT_OFF + (((wave + d) & 3) * TPW + n) * 128 + r32 * 4));
      zl *= __builtin_amdgcn_exp2f((zm - M) * SCL2);
      ow = __builtin_amdgcn_exp2f((zprev - M) * SCL2);
      zm = M;
    }
    p0 = sm_exp<0>(s, zm, zl);
    PIECE(11);
    p1 = sm_exp<8>(s, zm, zl);
    PIECE(12);
    if (n < TPW - 1) park_put(n, p0, p1);
    PIECE(13); PIECE(14); PIECE(15);
    slot ^= TILEB;
  }
  zl = xhalf_sum(zl);
  *(LAS f32x2*)(lds + ML_OFF + (wave * 32 + r32) * 8) = f32x2{ow, zl};
  *(LAS u32x4*)(lds + TREE_OFF + wave * 2048 + lane * 16) = p0;
  *(LAS u32x4*)(lds + TREE_OFF + wave * 2048 + 1024 + lane * 16) = p1;
  auto rebase = [&](float tm) {
    if (!__all(tm == om)) {
      if (!__all(om == -__builtin_inff())) acc_scale(__builtin_amdgcn_exp2f((om - tm) * SCL2));
      om = tm;
    }
  };
  u32x4 r0, r1;
#pragma unroll 1
  for (int n = 0;; ++n) {
    src += TSTEP;
    dst ^= TILEB;
    PIECE(0);
    pace(n >= TPW - 1 - RUSHN ? RUSH : 0);
    const float tm = *(const LAS float*)(lds + mt + n * 128);
    park_get(n, r0, r1);
    PIECE(1);
    rebase(tm);
    PIECE(2); PIECE(3); PIECE(4);
    WAIT_VM(5);
    if (n == TPW - 2) break;
    bf16x8 f[16];
#pragma unroll
    for (int c = 0; c < 8; ++c) f[c] = col_frag(lds, slot + vrd, c, 0);
    PIECE(5);
#pragma unroll
    for (int c = 0; c < 8; ++c) f[8 + c] = col_frag(lds, slot + vrd, c, BLKB);
    PIECE(6);
    pv4<0>(f, r0);
    PIECE(7);
    pv4<4>(f + 4, r0);
    PIECE(8);
    pv4<0>(f + 8, r1);
    PIECE(9);
    pv4<4>(f + 12, r1);
    PIECE(10); PIECE(11); PIECE(12); PIECE(13); PIECE(14); PIECE(15);
    slot ^= TILEB;
  }
  f32x16 ox, oy;
  {
    bf16x8 f[16];
#pragma unroll
    for (int c = 0; c < 8; ++c) { f[c] = col_frag(lds, slot + vrd, c, 0); f[8 + c] = col_frag(lds, slot + vrd, c, BLKB); }
    PIECE_N(5, WEND - 1); PIECE_N(6, WEND - 1); PIECE_N(7, WEND - 1);
    pv4<0>(f, r0); pv4<0>(f + 8, r1);
    PIECE_N(8, WEND - 1); PIECE_N(9, WEND - 1);
    pv4<4>(f + 4, r0); pv4<4>(f + 12, r1);
    PIECE_N(10, WEND - 1); PIECE_N(11, WEND - 1);
  }
#define QUARTER(p) do { const f32x16 a_ = acc_get16<32 * p>(), b_ = acc_get16<32 * p + 16>(); \
    if (wave == p) { ox = a_ * ow; oy = b_ * ow; } \
    else { LAS char* d_ = lds + slot + p * 4096 + lane * 16; \
           *(LAS u32x4*)d_ = pack8(a_, 0); *(LAS u32x4*)(d_ + 1024) = pack8(a_, 8); *(LAS u32x4*)(d_ + 2048) = pack8(b_, 0); *(LAS u32x4*)(d_ + 3072) = pack8(b_, 8); } } while (0)
  QUARTER(0);
  PIECE_N(12, WEND - 1); PIECE_N(13, WEND - 1);
  QUARTER(1);
  PIECE_N(14, WEND - 1); PIECE_N(15, WEND - 1);
  QUARTER(2);
  QUARTER(3);
#undef QUARTER
  WAIT_VM(8);
  BARRIER();
  const unsigned rel = slot - ring, vq = (vrd ^ ((wave & 1) << 7)) + (wave >> 1) * 256;
  float L = zl;
  u32x4 pl[3][2];
  {
    f32x2 ml[3]; u32x4 im[3][4]; bf16x8 f[8];
#pragma unroll
    for (int d = 0; d < 3; ++d) {
      const int w = (wave + 1 + d) & 3;
      ml[d] = *(const LAS f32x2*)(lds + ML_OFF + (w * 32 + r32) * 8);
#pragma unroll
      for (int i = 0; i < 4; ++i) im[d][i] = *(const LAS u32x4*)(lds + w * RINGB + rel + wave * 4096 + lane * 16 + i * 1024);
#pragma unroll
      for (int i = 0; i < 2; ++i) pl[d][i] = *(const LAS u32x4*)(lds + TREE_OFF + w * 2048 + i * 1024 + lane * 16);
    }
#pragma unroll
    for (int d = 0; d < 4; ++d) {
      const unsigned a = ((wave + d) & 3) * RINGB + (rel ^ TILEB) + vq;
      f[2 * d] = col_frag(lds, a, 0, 0); f[2 * d + 1] = col_frag(lds, a, 1, 0);
    }
#define FOLD(d) do { L += ml[d][1]; fma8(ox, 0, ml[d][0], im[d][0]); fma8(ox, 8, ml[d][0], im[d][1]); fma8(oy, 0, ml[d][0], im[d][2]); fma8(oy, 8, ml[d][0], im[d][3]); \
    asm volatile("" : "+v"(ox), "+v"(oy)); } while (0)
    asm volatile("" ::: "memory");
    FOLD(0); FOLD(1); FOLD(2);
#undef FOLD
    pv2(ox, oy, f[0], f[1], p0);
#pragma unroll
    for (int d = 0; d < 3; ++d) pv2(ox, oy, f[2 * d + 2], f[2 * d + 3], pl[d][0]);
  }
  WAIT_VM(0);
  BARRIER();
  {
    bf16x8 f[8];
#pragma unroll
    for (int d = 0; d < 4; ++d) {
      const unsigned a = ((wave + d) & 3) * RINGB + (rel ^ TILEB) + vq;
      f[2 * d] = col_frag(lds, a, 0, BLKB); f[2 * d + 1] = col_frag(lds, a, 1, BLKB);
    }
    pv2(ox, oy, f[0], f[1], p1);
#pragma unroll
    for (int d = 0; d < 3; ++d) pv2(ox, oy, f[2 * d + 2], f[2 * d + 3], pl[d][1]);
    asm volatile("s_nop 15" : "+v"(ox), "+v"(oy));
  }

  const __amdgpu_buffer_rsrc_t rs = __builtin_amdgcn_make_buffer_rsrc(ws, 0, (int)WS_END, 0x00020000);
  const int ml_off = (int)WS_PML + (grp * NSPL * 32 + r32) * 8;
  const int po_off = (int)WS_PO + grp * NSPL * PARTB + wave * 4096 + lane * 16;
  if (wave == 0 && hh == 0) __builtin_amdgcn_raw_buffer_store_b64(__builtin_bit_cast(u32x2, f32x2{zm, L}), rs, ml_off + mem * 256, 0, 16);
  f32x4 x = {}, y = {};
#pragma unroll
  for (int v = 0; v < 4; ++v) {
    const f32x16& o = v < 2 ? ox : oy;
    const int b = 8 * (v & 1);
    if (v == mem) { x = f32x4{o[b], o[b + 1], o[b + 2], o[b + 3]}; y = f32x4{o[b + 4], o[b + 5], o[b + 6], o[b + 7]}; }
    else __builtin_amdgcn_raw_buffer_store_b128(pack8(o, b), rs, po_off + mem * PARTB + v * 1024, 0, 16);
  }
  WAIT_VM(0);
  BARRIER();
  if (wave == 0) {
#pragma unroll 1
    for (int i = 0; i < SPIN_MAX && !ready; ++i) {
      ctl_add_ret(cw, vz, 0u); WAIT_VM(0);
      ready = ctl_result() != w0;
      if (!ready) __builtin_amdgcn_s_sleep(16);
    }
    ctl_add_ret(cw, vz, 1u << mem);
#pragma unroll 1
    for (int i = 0; i < SPIN_MAX; ++i) {
      WAIT_VM(0);
      const unsigned w = ctl_result() | (1u << mem);
      if ((w & 15u) == 15u) break;
      ctl_add_ret(cw, vz, leader ? W_EPOCH : 0u);
    }
  }
  BARRIER();
  f32x2 oml[3]; u32x4 oa[3];
#pragma unroll
  for (int d = 0; d < 3; ++d) {
    const int sm = (mem + 1 + d) & 3;
    oml[d] = __builtin_bit_cast(f32x2, __builtin_amdgcn_raw_buffer_load_b64(rs, ml_off, sm * 256, 16));
    oa[d] = __builtin_amdgcn_raw_buffer_load_b128(rs, po_off, sm * PARTB + mem * 1024, 16);
  }
  const float Mf = fmaxf(fmaxf(zm, oml[0][0]), fmaxf(oml[1][0], oml[2][0]));
  float f[4];
  f[3] = __builtin_amdgcn_exp2f((zm - Mf) * SCL2);
  L *= f[3];
#pragma unroll
  for (int d = 0; d < 3; ++d) { f[d] = __builtin_amdgcn_exp2f((oml[d][0] - Mf) * SCL2); L = fmaf(f[d], oml[d][1], L); }
  const float inv = 1.0f / L;
  x *= f[3] * inv; y *= f[3] * inv;
#pragma unroll
  for (int d = 0; d < 3; ++d) { x += (f[d] * inv) * unpack4(oa[d][0], oa[d][1]); y += (f[d] * inv) * unpack4(oa[d][2], oa[d][3]); }
  const auto rx = __builtin_amdgcn_permlane32_swap(pack2(x[0], x[1]), pack2(y[0], y[1]), false, false);
  const auto ry = __builtin_amdgcn_permlane32_swap(pack2(x[2], x[3]), pack2(y[2], y[3]), false, false);
  *(u32x4*)((char*)out + (size_t)(grp * 32 + r32) * ROWB + wave * 128 + mem * 32 + hh * 16) = u32x4{rx[0], ry[0], rx[1], ry[1]};
  asm volatile(".Lcode1:");
}

extern "C" void kernel_launch(void* const* d_in, const int* in_sizes, int n_in, void* d_out, int out_size, void* d_ws, size_t ws_size,
                              hipStream_t stream) {
  tree_attn<<<NGRP * NSPL, 256, 0, stream>>>((const char*)d_in[0], (const char*)d_in[1], (const char*)d_in[2], (const char*)d_in[3],
                                             (const char*)d_in[4], (const char*)d_in[5], (char*)d_ws, (unsigned short*)d_out);
}
```
